```python
import torch

def _softmax(x, axis=-1):
    return torch.softmax(x, dim=axis)
def _gelu(x):
    return torch.nn.functional.gelu(x, approximate="tanh")
def _silu(x):
    return torch.nn.functional.silu(x)
def _sigmoid(x):
    return torch.sigmoid(x)
def _layernorm(x, axis=-1, eps=1e-5):
    m = x.mean(dim=axis, keepdim=True)
    v = x.var(dim=axis, keepdim=True, unbiased=False)
    return (x - m) / torch.sqrt(v + eps)
def _scatter_add(tgt, idx, src, axis=0):
    return tgt.clone().index_add_(axis, idx, src)
def _segment_sum(src, seg, n):
    out = torch.zeros((n,) + src.shape[1:], dtype=src.dtype)
    return out.index_add_(0, seg, src)
def _pad(x, pw):
    flat = tuple(v for pair in reversed(pw) for v in pair)
    return torch.nn.functional.pad(x, flat)

kernel_name = 'standalone__deep_cross_fibinet__recsys'

def setup_inputs():

    _w = (lambda a: (lambda t: t.to(torch.bfloat16) if t.is_floating_point() else t)(torch.as_tensor(a)))
    B, F, D = 4096, 26, 64
    V = min(100000, 100000)
    Fr = max(F // 2, 1)
    H = min(F * D, 256)
    return {
        "idx": _w((lambda lo, hi, *s: torch.randint(lo, hi, s))(0, V, B, F).to(torch.int64)),
        "table": _w((lambda *s: torch.randn(*s, dtype=torch.bfloat16) * 1.6)(V, D)),
        "se_w1": _w((lambda *s: torch.randn(*s, dtype=torch.bfloat16) * 1.6)(F, Fr)),
        "se_w2": _w((lambda *s: torch.randn(*s, dtype=torch.bfloat16) * 1.6)(Fr, F)),
        "w_bi": _w((lambda *s: torch.randn(*s, dtype=torch.bfloat16) * 0.05)(D, D) * (D ** -0.5)),
        "w_cross": _w((lambda *s: torch.randn(*s, dtype=torch.bfloat16) * 0.05)(2 * F * F * D) * 0.01),
        "w_deep1": _w((lambda *s: torch.randn(*s, dtype=torch.bfloat16) * 0.8)(F * D, H) * ((F * D) ** -0.5)),
        "w_deep2": _w((lambda *s: torch.randn(*s, dtype=torch.bfloat16) * 0.05)(H) * 0.01),
    }

def reference(idx, table, se_w1, se_w2, w_bi, w_cross, w_deep1, w_deep2):
    emb = table[idx]
    B = emb.shape[0]
    z = emb.mean(axis=-1)
    a = _sigmoid(_gelu(z @ se_w1) @ se_w2)
    v = a[:, :, None] * emb
    eW = torch.einsum('bfd,de->bfe', emb, w_bi)
    vW = torch.einsum('bfd,de->bfe', v, w_bi)
    p = torch.einsum('bie,bje->bije', eW, emb).reshape(B, -1)
    q = torch.einsum('bie,bje->bije', vW, v).reshape(B, -1)
    cross_feat = torch.concatenate([p, q], axis=-1)
    logit_cross = cross_feat @ w_cross
    h = _gelu(emb.reshape(B, -1) @ w_deep1)
    logit_deep = h @ w_deep2
    return logit_cross + logit_deep
```

```cpp
#include <hip/hip_runtime.h>
#include <hip/hip_bf16.h>
#include <stdint.h>

#define NF 26
#define NP 13
#define PROD_CLASS(slot) ((((slot) & 3) == 1) && (((slot) >> 2) & 1) == 0)
#define STAMP(k)
#define TS(x)
#define TP(x)
#define TE(x) TS(x)
using bf16x8 = __attribute__((ext_vector_type(8))) short;
using f32x4 = __attribute__((ext_vector_type(4))) float;
using f32x2 = __attribute__((ext_vector_type(2))) float;
using s16x4 = __attribute__((ext_vector_type(4))) short;
using u32x4 = __attribute__((ext_vector_type(4))) unsigned;
using u32x8 = __attribute__((ext_vector_type(8))) unsigned;
using u32x2 = __attribute__((ext_vector_type(2))) unsigned;
using bf2 = __attribute__((ext_vector_type(2))) __bf16;
#define PIN(x) asm volatile("" : "+v"(x))
#define PINS(x) asm volatile("" : "+s"(x))
#define SB __builtin_amdgcn_sched_barrier(0);
#define CAT8(a, b) __builtin_shufflevector((a), (b), 0, 1, 2, 3, 4, 5, 6, 7)

#define SLOT 16384
#define OFF_B 65536
#define OFF_FL 131072
#define OFF_WBI (OFF_FL + 4096)
#define OFF_SW1 (OFF_WBI + 2048)
#define OFF_SW2 (OFF_SW1 + 1024)
#define OFF_WD2 (OFF_SW2 + 1024)
#define OFF_TAG (OFF_WD2 + 256)
#define OFF_NB (OFF_TAG + 768)
#define OFF_JNK (OFF_NB + 2048)
#define OFF_IDX (OFF_JNK + 2048)
#define OFF_RAW (OFF_IDX + 8192)
#define LDS_T (OFF_IDX + 16384)
constexpr int FOFF[4] = {OFF_B + SLOT, SLOT, OFF_B + 2 * SLOT, 2 * SLOT};
#define P_FLAG 3
#define P_POLL 6
#define P_CHK (P_POLL + 3)
#define P_STAT 9

#define WS_FLAG 1024
#define WS_SUM 4096
#define WS_SIDE 65536
#define WS_FRAG (1 << 20)

#define LDS(T, a) (*(__attribute__((address_space(3))) T*)(size_t)(a))
#define LDSV(a) (*(volatile const __attribute__((address_space(3))) unsigned*)(size_t)(a))
__device__ __forceinline__ unsigned pk_bf16(float lo, float hi) {
    const f32x2 v = {lo, hi};
    return __builtin_bit_cast(unsigned, __builtin_convertvector(v, bf2));
}
__device__ __forceinline__ unsigned cvt_o(float lo, float hi) { unsigned r; asm volatile("v_cvt_pk_bf16_f32 %0, %1, %2" : "=v"(r) : "v"(lo), "v"(hi)); return r; }
__device__ __forceinline__ float exp_o(float a) { float r; asm volatile("v_exp_f32 %0, %1" : "=v"(r) : "v"(a)); return r; }
template <typename A, typename B> __device__ __forceinline__ f32x4 mf0(const A& a, const B& b) { f32x4 d; asm volatile("v_mfma_f32_16x16x32_bf16 %0, %1, %2, 0" : "=v"(d) : "v"(a), "v"(b)); return d; }
template <typename A, typename B> __device__ __forceinline__ void mfa(f32x4& d, const A& a, const B& b) { asm volatile("v_mfma_f32_16x16x32_bf16 %0, %1, %2, %0" : "+v"(d) : "v"(a), "v"(b)); }
__device__ __forceinline__ float lo_f(unsigned x) { return __uint_as_float(x << 16); }
__device__ __forceinline__ float hi_f(unsigned x) { return __uint_as_float(x & 0xffff0000u); }
__device__ __forceinline__ s16x4 tr_read(unsigned a) {
    typedef short v4s __attribute__((ext_vector_type(4)));
    return __builtin_amdgcn_ds_read_tr16_b64_v4i16((__attribute__((address_space(3))) v4s*)(size_t)a);
}
#define DMA(name, ins) __device__ __forceinline__ void name(unsigned voff, const void* sbase, unsigned ldsaddr) { \
    asm volatile("s_mov_b32 m0, %2\n\ts_nop 0\n\t" ins :: "v"(voff), "s"(sbase), "s"(ldsaddr) : "memory"); }
DMA(dma16, "global_load_lds_dwordx4 %0, %1") DMA(dma4, "global_load_lds_dword %0, %1")
DMA(dma16sc, "global_load_lds_dwordx4 %0, %1 sc1") DMA(dma4sc, "global_load_lds_dword %0, %1 sc1")
__device__ __forceinline__ void st16sc(unsigned voff, void* sbase, u32x4 v) {
    asm volatile("global_store_dwordx4 %0, %1, %2 sc1\n\ts_nop 1" :: "v"(voff), "v"(v), "s"(sbase) : "memory");
}
template <int O> __device__ __forceinline__ void set_m0(unsigned base) { asm volatile("s_add_u32 m0, %0, %1" :: "s"(base), "n"(O) : "memory", "scc"); }
template <int O> __device__ __forceinline__ void bdma16o(unsigned voff, __amdgpu_buffer_rsrc_t rs, unsigned soff) {
    asm volatile("buffer_load_dwordx4 %0, %1, %2 offen offset:%3 lds" :: "v"(voff), "s"(rs), "s"(soff), "n"(O) : "memory");
}
template <int O> __device__ __forceinline__ void dma16o(unsigned voff, const void* sbase) {
    asm volatile("global_load_lds_dwordx4 %0, %1 offset:%2" :: "v"(voff), "s"(sbase), "n"(O) : "memory");
}
template <int O> __device__ __forceinline__ void dma16sco(unsigned voff, const void* sbase) {
    asm volatile("global_load_lds_dwordx4 %0, %1 offset:%2 sc1" :: "v"(voff), "s"(sbase), "n"(O) : "memory");
}
#define WAITCNT(vm, lgkm) __builtin_amdgcn_s_waitcnt(((vm) & 15) | (((vm) >> 4) << 14) | 0x70 | ((lgkm) << 8))
#define WAITX(vm, x, m) { WAITCNT((vm) + (x), 0); asm volatile("s_cmp_eq_u32 %0, 0\n\ts_cbranch_scc1 1f\n\ts_waitcnt vmcnt(%1)\n1:" :: "s"(m), "n"(vm) : "memory", "scc"); }
#define BARRIER asm volatile("s_barrier" ::: "memory")
__device__ __forceinline__ float sum32(float v) { const u32x2 p = __builtin_amdgcn_permlane32_swap(__float_as_uint(v), __float_as_uint(v), false, false); return __uint_as_float(p[0]) + __uint_as_float(p[1]); }
__device__ __forceinline__ float sum16(float v) { const u32x2 p = __builtin_amdgcn_permlane16_swap(__float_as_uint(v), __float_as_uint(v), false, false); return __uint_as_float(p[0]) + __uint_as_float(p[1]); }
__device__ __forceinline__ float sel(unsigned m, float a, float b) {
    return __uint_as_float(__builtin_amdgcn_bitop3_b32(m, __float_as_uint(a), __float_as_uint(b), 0xca));
}
__device__ __forceinline__ void wait_set(unsigned f, unsigned a) {
    if (__builtin_amdgcn_ballot_w64(f == 0u) != 0ull) {
#pragma unroll 1
        for (int it = 0; it < (1 << 20) && __builtin_amdgcn_ballot_w64(LDSV(a) == 0u) != 0ull; ++it) { }
    }
    asm volatile("" ::: "memory");
}

struct Com {
    char* ws;
    unsigned lane16, ldsb;
    int c, nq, rho, st;
};
typedef __attribute__((address_space(1))) char* gp;
__device__ __forceinline__ void deliver(const Com& c, float v, int part, unsigned s2, unsigned s8, double big, gp sums, gp outp) {
    const gp side = (gp)c.ws + (WS_SIDE + c.st * 2048 + c.c * 64);
    u32x2 mine = __builtin_bit_cast(u32x2, __builtin_fma((double)v, 0x1p42, big));
    mine[1] += 0x04004000u - 0x43380000u;
    const unsigned ab = __float_as_uint(v) << 1;
    if (__builtin_expect(ab - 0x61000000u >= 0x22000000u && ab != 0u, 0)) {
        mine = u32x2{0u, 0x04004000u + (0x40000u << part)};
        __hip_atomic_store((__attribute__((address_space(1))) float*)(side + part * 256 + s2 * 2), v, __ATOMIC_RELAXED, __HIP_MEMORY_SCOPE_AGENT);
        asm volatile("s_waitcnt vmcnt(0)" ::: "memory");
    }
    const unsigned long long mv = __builtin_bit_cast(unsigned long long, mine);
    const unsigned long long tot = mv + __hip_atomic_fetch_add((__attribute__((address_space(1))) unsigned long long*)(sums + s8), mv, __ATOMIC_RELAXED, __HIP_MEMORY_SCOPE_AGENT);
    const unsigned th = (unsigned)(tot >> 32);
    if (th >= 0x20000000u) {
        const u32x2 dv = {(unsigned)tot, (th & 0x3ffffu) | 0x43300000u};
        float r = (float)__builtin_fma(__builtin_bit_cast(double, dv), 0x1p-42, -0x1.2p10);
        if (__builtin_expect(th >= 0x20040000u, 0)) {
#pragma unroll 1
            for (int k = 0; k < 8; ++k)
                if ((th >> (18 + k)) & 1u) r += __hip_atomic_load((__attribute__((address_space(1))) float*)(side + k * 256 + s2 * 2), __ATOMIC_RELAXED, __HIP_MEMORY_SCOPE_AGENT);
        }
        *(__attribute__((address_space(1))) uint16_t*)(outp + s2) = (uint16_t)pk_bf16(r, r);
    }
}

struct Deep : Com {
    __amdgpu_buffer_rsrc_t rw1;
    const char* wsf; const char* wd2;
    unsigned sG[4], sB;
    unsigned m1, m3, m4;
    int unit;
    unsigned long long pmask;
    unsigned tad, tag, pl;
    unsigned boff[4], ra[4], rb[4];
    float w2[16];
    f32x4 ac[4][4];
    bf16x8 fa[2][4];
    s16x4 lo[2][4], hi[2][4];
};
template <int Q> __device__ __forceinline__ void prepB(Deep& c) {
    if constexpr (Q == 0) c.sB = 24 * 32768; else c.sB = c.sG[(Q - 1) & 3] + ((Q - 1) >> 2) * (8 * 32768);
    PINS(c.sB);
    set_m0<OFF_B + (Q & 3) * SLOT>(c.ldsb);
}
template <int I> __device__ __forceinline__ void issueB(Deep& c) { bdma16o<I * 1024>(c.boff[I], c.rw1, c.sB); }
__device__ __forceinline__ void mkboff(Deep& d, int lane, int cw, int nq) {
    const unsigned b0 = (unsigned)(((cw & 1) * 64 + (cw >> 1) * 32 + (lane >> 3)) * 512 + nq * 128 + ((lane & 7) ^ ((lane >> 3) & 2)) * 16);
    d.boff[0] = b0; d.boff[1] = (b0 ^ 64u) + 3072; d.boff[2] = b0 + 6144; d.boff[3] = (b0 ^ 64u) + 9216;
#pragma unroll
    for (int k = 0; k < 4; ++k) PIN(d.boff[k]);
}
template <int P, int N> __device__ __forceinline__ void rd1(Deep& c) {
    constexpr int tab[12] = {0, 4, 5, 6, 7, 1, 2, 3, 8, 9, 10, 11}, k = tab[N < 12 ? N : 0];
    if constexpr (N >= 12) { }
    else if constexpr (k < 4) c.fa[P & 1][k] = LDS(const bf16x8, c.ra[k] + (P & 3) * SLOT);
    else if constexpr (k & 1) c.hi[P & 1][(k - 4) >> 1] = tr_read(c.rb[(k - 4) >> 1] + (P & 3) * SLOT + 512);
    else c.lo[P & 1][(k - 4) >> 1] = tr_read(c.rb[(k - 4) >> 1] + (P & 3) * SLOT);
    SB
}
template <int P, int M, int K> __device__ __forceinline__ void mk(Deep& c) {
    if constexpr (P >= 0) mfa(c.ac[M][K], CAT8(c.lo[P & 1][K], c.hi[P & 1][K]), c.fa[P & 1][M]);
    SB
}
template <int P, int N> __device__ __forceinline__ void m1(Deep& c) { mk<P, (N >> 1) & 3, (N >> 3) * 2 + (N & 1)>(c); }
template <int P, int M> __device__ __forceinline__ void m4(Deep& c) { mk<P, M, 0>(c); mk<P, M, 1>(c); mk<P, M, 2>(c); mk<P, M, 3>(c); }
#define LSTEP 0xbbaa
constexpr int lstep(int k) { return (LSTEP >> (4 * k)) & 15; }
constexpr int lfirst(int S) { int k = 0; while (k < 4 && lstep(k) < S) ++k; return k; }
constexpr int dops(int S) { return S < 8 ? 4 : 4 * (lfirst(S + 1) - lfirst(S)); }
static_assert(lstep(2) > P_CHK && lstep(3) > 10, "fragments: after the check, chunk 9 stays during step 10");
constexpr int dyoung(int S, int U) { return 4 * ((S < 8 ? S + 4 : 11 + lfirst(S + 1)) - (U + 1)) + (S == 1 || S == 2); }
template <int S, int I> __device__ __forceinline__ void dissue1(Deep& c) {
    constexpr int U = S < 8 ? S + 3 : 11 + lfirst(S) + (I >> 2);
    if constexpr (U < NP) {
        if constexpr ((I & 3) == 0) { prepB<U>(c); asm volatile("s_nop 0"); }
        issueB<I & 3>(c);
    } else {
        if constexpr ((I & 3) == 0) { set_m0<FOFF[U - NP]>(c.ldsb); asm volatile("s_nop 0"); }
        dma16sco<(I & 3) * 1024>(c.lane16, c.wsf + (U - NP) * 16384);
    }
    SB
}
template <int S, int I0, int I1> __device__ __forceinline__ void dissue(Deep& c) {
    if constexpr (I0 < I1 && I0 < dops(S)) { dissue1<S, I0>(c); dissue<S, I0 + 1, I1>(c); }
}
__device__ __forceinline__ unsigned tag_addr(int lane, int unit, int nq, int st, int xcd) {
    const unsigned l4 = (unsigned)lane * 4u;
    const unsigned a0 = (unsigned)(WS_FLAG + unit * 4) + l4 * 32u;
    const unsigned a1 = (((unsigned)(nq * 256) + l4 * 64u) & 768u) + (unsigned)(st * 4);
    const unsigned a2 = (unsigned)(WS_FLAG + xcd * 128 - 64) + l4;
    const unsigned lo = l4 < 32u ? a0 : a1, hi = l4 < 168u ? a2 : (unsigned)(st * 4);
    return l4 < 64u ? lo : hi;
}
__device__ __forceinline__ void publish(Deep& c, unsigned sv) {
    asm volatile("s_cmp_eq_u64 %3, 0\n\ts_cbranch_scc1 1f\n\ts_mov_b64 exec, %3\n\tglobal_store_dword %0, %1, %2 sc1\n\ts_mov_b64 exec, -1\n1:"
                 :: "v"(c.tad), "v"(sv), "s"(c.ws), "s"(c.pmask) : "memory", "scc");
}
__device__ __forceinline__ void produce(Deep& c) {
    if (c.m1 == 0u) {
        const int unit = c.unit;
        const unsigned p8 = (c.lane16 << 4) & 0x300u, e2 = (c.lane16 >> 5) & 0x1eu;
        const unsigned rb = (unsigned)(OFF_RAW + c.c * 32) + e2;
        const unsigned dm = p8 == (c.lane16 & 0x300u) ? ~0u : 0u;
        const int m = unit < 24 ? 2 * (unit & 6) + (unit & 1) + ((unit >> 2) & 2) : 4 * (unit & 1) + 2;
        unsigned fo = (unsigned)(WS_FRAG + c.c * 65536 + (unit >> 4) * 1024 + m * 64) + e2 * 2048u + ((c.lane16 & 0x30u) ^ (unsigned)((m & 4) << 3));
        if (unit >= 24) fo += c.lane16 & 0x200u;
#pragma unroll
        for (int pq = 0; pq < 2; ++pq) {
            u32x4 fr;
#pragma unroll
            for (int d = 0; d < 4; ++d) {
                const unsigned rp = rb + (d < 3 ? p8 : 0u) + (unsigned)(pq * (NF * 128) + 1024 * d);
                fr[d] = LDS(const uint16_t, rp) | ((unsigned)LDS(const uint16_t, rp + 128) << 16);
            }
            fr[3] &= dm;
            st16sc(fo + pq * 2048u, c.ws, fr);
        }
        if (unit >= 24) { st16sc(fo ^ 512u, c.ws, u32x4{0u, 0u, 0u, 0u}); st16sc((fo ^ 512u) + 2048u, c.ws, u32x4{0u, 0u, 0u, 0u}); }
    }
    SB
}
__device__ __forceinline__ void check_flags(Deep& c) {
    if (c.m3 == 0u) {
        const unsigned l4 = c.lane16 >> 2;
        const unsigned long long open = __builtin_amdgcn_ballot_w64(c.tag == c.pl) & (c.nq ? 0x7ffffff0000ull : 0x3ffffff0000ull);
        if (__builtin_expect(open != 0ull, 0)) {
#pragma unroll 1
            for (int it = 0; it < (1 << 22); ++it) {
                dma4sc(c.tad, c.ws, OFF_TAG + 256);
                asm volatile("s_waitcnt vmcnt(0)" ::: "memory");
                if ((__builtin_amdgcn_ballot_w64(LDSV(OFF_TAG + 256 + l4) == c.tag) & open) == 0ull) break;
                __builtin_amdgcn_s_sleep(2);
            }
        }
    }
    SB
}
__device__ __forceinline__ void fetch_status(Deep& c) {
    if (c.m4 == 0u) {
        if (c.nq) dma16sc(c.lane16, c.ws, OFF_NB + 1024);
        else dma4sc(c.tad, c.ws, OFF_TAG + 512);
    }
    SB
}
constexpr int x3(int P) { return (P == P_FLAG || P == P_FLAG + 1) + (P == P_POLL || P == P_POLL + 1); }
#define DTOP 2
#define DTOPL 4
#define DTOPB 4
constexpr int dtop(int P) { return P < 8 ? ((P & 3) == 2 ? DTOPB : DTOP) : DTOPL; }
template <int P, int N> __device__ __forceinline__ void dbody(Deep& c) {
    constexpr int top = dtop(P);
    m1<P - 1, N>(c);
    if constexpr (P > 10) {
        if constexpr (N < 4) rd1<P, (N ? N + 4 : 0)>(c);
        else if constexpr (N < 7) dissue<P, top + N - 4, top + N - 3>(c);
        else if constexpr (N < 15) {
            if constexpr (N == 7) { WAITCNT(dyoung(P - 1, P) + (dops(P) < top + 3 ? dops(P) : top + 3), 15); SB }
            rd1<P, (N < 11 ? N - 6 : N - 3)>(c);
        } else dissue<P, top + 3, 16>(c);
    } else {
        rd1<P, N>(c);
        if constexpr (N >= 12) dissue<P, top + N - 12, (N == 15 ? 16 : top + N - 11)>(c);
    }
    if constexpr (N < 15) dbody<P, N + 1>(c);
}
template <int P> struct DStep {
    static __device__ __forceinline__ void run(Deep& c) {
        TS(c.ts[P])
        SB
        if constexpr (P == P_FLAG) publish(c, c.tag + 1u);
        if constexpr (P == 1) { dma4((unsigned)(c.nq * 128) + ((c.lane16 >> 2) & 124u), c.wd2, OFF_WD2); SB }
        if constexpr (P == P_CHK) check_flags(c);
        if constexpr (P == P_STAT) fetch_status(c);
        dissue<P, 0, dtop(P)>(c);
        dbody<P, 0>(c);
        if constexpr (P <= 10) {
            if constexpr (P == 4) {
#pragma unroll
                for (int k = 0; k < 4; ++k) {
                    const u32x2 v = LDS(const u32x2, (unsigned)(OFF_WD2 + k * 32) + ((c.lane16 >> 5) & 24u));
                    c.w2[4 * k] = lo_f(v[0]); c.w2[4 * k + 1] = hi_f(v[0]); c.w2[4 * k + 2] = lo_f(v[1]); c.w2[4 * k + 3] = hi_f(v[1]);
                }
#pragma unroll
                for (int k = 0; k < 16; ++k) PIN(c.w2[k]);
            }
            if constexpr (P == P_POLL) { if (c.m3 == 0u) dma4sc(c.tad, c.ws, OFF_TAG + 256); }
            SB
            if constexpr (P + 1 < P_FLAG && P < 2) WAITX(dyoung(P, P + 1), 2, c.m1)
            else if constexpr (P == P_STAT) WAITX(dyoung(P, P + 1), 1, c.m4)
            else if constexpr (x3(P) > 0) WAITX(dyoung(P, P + 1), x3(P), c.m3)
            else if constexpr (P == 10) WAITCNT(63, 0);
            else WAITCNT(dyoung(P, P + 1), 0);
            if constexpr (P == P_CHK - 1) { SB c.pl = LDSV(OFF_TAG + 256 + (c.lane16 >> 2)); SB }
            BARRIER;
        }
        if constexpr (P + 1 < NP) DStep<P + 1>::run(c);
    }
};

struct Cross : Com {
    const char* tb[4]; const char* wsf;
    unsigned ga[40];
    unsigned re[2], rx[2], rx0, six, wf[2];
    s16x4 wa[2];
    bf16x8 wb[2], wr[2];
    u32x4 fe[3][2][2];
    f32x4 ew[NF], zs[4];
    u32x4 X[NP], bp[4];
    float k0, k1, k2;
    unsigned am;
    u32x4 on[4], s1, zq;
};
template <int I> __device__ __forceinline__ void issueA(Cross& c, unsigned off) { dma16o<I * 1024>(off, c.tb[I]); }
template <int K, int S, int N> __device__ __forceinline__ void sw1(Cross& c) {
    constexpr int a = 1 + 4 * K + (S == 1 ? 2 : (S == 3 ? 1 : 0)), b = 1 + 4 * K + (S == 0 ? 1 : (S == 2 ? 2 : 3)), Q = N >> 2, R = N & 3;
    float x = c.ew[2 * a + Q][R], y = c.ew[2 * b + Q][R];
    if (S >= 2) asm volatile("v_permlane32_swap_b32 %0, %1" : "+v"(x), "+v"(y)); else asm volatile("v_permlane16_swap_b32 %0, %1" : "+v"(x), "+v"(y));
    c.ew[2 * a + Q][R] = x; c.ew[2 * b + Q][R] = y;
}
template <int K, int S, int N0 = 0, int N1 = 8> __device__ __forceinline__ void swn(Cross& c) { if constexpr (N0 < N1) { sw1<K, S, N0>(c); swn<K, S, N0 + 1, N1>(c); } }
constexpr int sw0(int P) { return P <= 3 ? 0 : P <= 5 ? 8 : P == 6 ? 20 : P == 7 ? 32 : P <= 9 ? 40 : P == 10 ? 52 : 64; }
template <int P, int N0, int N1> __device__ __forceinline__ void tsw(Cross& c) {
    if constexpr (N0 < N1 && sw0(P) + N0 < sw0(P + 1)) { sw1<((sw0(P) + N0) >> 5), ((sw0(P) + N0) >> 3) & 3, (sw0(P) + N0) & 7>(c); tsw<P, N0 + 1, N1>(c); }
}
constexpr int fbuf(int P) { return P == 12 ? 2 : P & 1; }
template <int P, int N> __device__ __forceinline__ void cmm(Cross& c) {
    if constexpr (P >= 0) {
        constexpr int B = fbuf(P);
        if constexpr (N < 2)
            asm volatile("v_smfmac_f32_16x16x64_bf16 %0, %1, %2, %3" : "+v"(c.zs[P ? (P - 1) >> 2 : 3])
                         : "v"(c.on[P ? (P - 1) & 3 : 0]), "v"(__builtin_shufflevector(c.fe[B][N][0], c.fe[B][N][1], 0, 1, 2, 3, 4, 5, 6, 7)), "v"(c.six));
        else if constexpr (N < 4) c.ew[2 * P + (N & 1)] = mf0(P ? c.wb[0] : c.wr[0], c.fe[B][0][N & 1]);
        else mfa(c.ew[2 * P + (N & 1)], P ? c.wb[1] : c.wr[1], c.fe[B][1][N & 1]);
    }
    SB
}
template <int P, int N> __device__ __forceinline__ void rdE(Cross& c) {
    c.fe[fbuf(P)][N >> 1][N & 1] = LDS(const u32x4, c.re[N >> 1] + (P & 3) * SLOT + (N & 1) * 2048);
    SB
}
template <int K> __device__ __forceinline__ void zpack(Cross& c) {
    float a = c.zs[K][0], b = c.zs[K][2];
    unsigned r;
    asm volatile("v_add_f32 %1, %1, %3\n\tv_add_f32 %2, %2, %4\n\tv_cvt_pk_bf16_f32 %0, %1, %2" : "=v"(r), "+v"(a), "+v"(b) : "v"(c.zs[K][1]), "v"(c.zs[K][3]));
    c.zq[K] = r;
    SB
}
template <int K> __device__ __forceinline__ void zclear(Cross& c) { c.zs[K] = f32x4{0.f, 0.f, 0.f, 0.f}; PIN(c.zs[K]); SB }
template <int P> __device__ __forceinline__ void rdX(Cross& c) {
    constexpr int k = 1 + ((P - 1) & ~3);
    if constexpr (P == 0) c.X[0] = LDS(const u32x4, c.rx0);
    else if constexpr ((P & 3) == 2)
        asm volatile("s_mov_b32 exec_hi, 0\n\tds_read_b128 %0, %4\n\tds_read_b128 %1, %4 offset:2048\n\tds_read_b128 %2, %5\n\tds_read_b128 %3, %5 offset:2048\n\ts_mov_b32 exec_hi, -1"
                     : "=&v"(c.X[k]), "=&v"(c.X[k + 1]), "=&v"(c.X[k + 2]), "=&v"(c.X[k + 3]) : "v"(c.rx[0]), "v"(c.rx[1]) : "memory");
    else if constexpr ((P & 3) == 0)
        asm volatile("s_mov_b32 exec_lo, 0\n\tds_read_b128 %0, %4\n\tds_read_b128 %1, %4 offset:2048\n\tds_read_b128 %2, %5\n\tds_read_b128 %3, %5 offset:2048\n\ts_mov_b32 exec_lo, -1"
                     : "+v"(c.X[k]), "+v"(c.X[k + 1]), "+v"(c.X[k + 2]), "+v"(c.X[k + 3]) : "v"(c.rx[0]), "v"(c.rx[1]) : "memory");
    SB
}
template <int P> struct CStep {
    static __device__ __forceinline__ void run(Cross& c) {
        TS(c.ts[P])
        SB
        cmm<P - 1, 2>(c); rdE<P, 0>(c);
        cmm<P - 1, 3>(c); rdE<P, 1>(c);
        cmm<P - 1, 4>(c); rdE<P, 2>(c);
        cmm<P - 1, 5>(c); rdE<P, 3>(c);
        cmm<P - 1, 0>(c);
        if constexpr (P + 3 < NP) set_m0<((P + 3) & 3) * SLOT>(c.ldsb);
        cmm<P - 1, 1>(c);
        rdX<P>(c);
        constexpr bool xw = P && !(P & 1);
#define GA(J) if constexpr (P + 3 < NP) issueA<J>(c, c.ga[4 * P + (J)]); SB
        if constexpr (P == 4 || P == 8) zclear<P / 4>(c);
        if constexpr (P == 8) {
#pragma unroll
            for (int r = 0; r < 4; ++r) { c.bp[r][3] = cvt_o(c.ew[0][r], c.ew[1][r]); c.bp[r][0] = cvt_o(c.ew[2][r], c.ew[3][r]); }
            c.k0 = -2.0f * 0.7978845608028654f * 1.4426950408889634f / 64.0f; c.k1 = c.k0 * (0.044715f / 4096.0f); c.k2 = -64.0f / 1.4426950408889634f;
            c.am = (c.wf[0] & 16u) ? ~0u : 0u;
            PIN(c.k0); PIN(c.k1); PIN(c.k2); PIN(c.am);
            SB
        }
        if constexpr (P == 2 || P == 6 || P == 10) zpack<(P == 2 ? 3 : (P - 6) / 4)>(c);
        if constexpr (xw) { tsw<P, 0, 4>(c); WAITCNT(63, 0); GA(0) tsw<P, 4, 6>(c); GA(1) tsw<P, 6, 8>(c); GA(2) tsw<P, 8, 10>(c); GA(3) tsw<P, 10, 12>(c); }
        else { GA(0) GA(1) tsw<P, 0, 4>(c); GA(2) tsw<P, 4, 8>(c); GA(3) tsw<P, 8, 12>(c); }
        if constexpr (P == 3) {
            const unsigned r6 = c.lane16 * 4u + (unsigned)c.rho * 256u;
            const unsigned am = (c.lane16 >> 8) + (unsigned)c.rho;
            c.wf[0] = (r6 & 0x3c0u) | (((am << 4) ^ ((r6 >> 3) & 0x20u)) & 0x30u) | SLOT;
            c.wf[1] = c.wf[0] + OFF_B;
            const unsigned wap = OFF_SW2 + (c.lane16 & 0x3c0u) + ((c.lane16 >> 1) & 0x18u);
            c.wa[0] = tr_read(wap); c.wa[1] = tr_read(wap + 32);
            const unsigned r2 = (c.lane16 >> 3) & 30u, s1 = OFF_SW1 + min(r2, 24u), sg = s1 + (am & 3u) * 52u;
#pragma unroll
            for (int k = 0; k < 4; ++k) {
                const unsigned a = (k < 3 ? sg : s1) + k * 208;
                const unsigned v = LDS(const uint16_t, a) | ((unsigned)LDS(const uint16_t, a + 26) << 16);
                c.s1[k] = r2 < 26u ? v : 0u;
            }
            PIN(c.s1);
            PIN(c.wf[0]); PIN(c.wf[1]); PIN(c.wa[0]); PIN(c.wa[1]);
        }
        SB
        WAITCNT(P < 10 ? 8 : 0, 0); BARRIER;
        if constexpr (P < 10) CStep<P + 1>::run(c);
    }
};

#define PFC 141
#define WPRE 6
__global__ __launch_bounds__(512) void k_main(const int* __restrict__ idx, const uint16_t* __restrict__ table,
        const uint16_t* __restrict__ w_bi, const uint16_t* __restrict__ w_deep1, const uint16_t* __restrict__ w_cross,
        unsigned* __restrict__ ws32, const uint16_t* __restrict__ se_w1, const uint16_t* __restrict__ se_w2,
        const uint16_t* __restrict__ w_deep2_, __hip_bfloat16* __restrict__ out_, unsigned long long* __restrict__ stamps) {
    SB
    const int tid = threadIdx.x, lane = tid & 63;
    const int w = __builtin_amdgcn_readfirstlane(tid >> 6);
    const int bid = blockIdx.x;
    const int xcd = bid & 7, slot = bid >> 3;
    const int st = xcd * 8 + (slot >> 2), nq = slot & 3;
    const int s0 = st * 64;
    const int rho = (st + nq) & 3, cw = w & 3;
    const int l15 = lane & 15, g = lane >> 4;
    LDS(u32x2, OFF_FL + tid * 8) = u32x2{0u, 0u};
    unsigned long long pc;
    {
        const unsigned ln = min((unsigned)(4 + w * 4 + (lane & 3) + ((lane >> 2) & 1) * (32 + nq * 32)), (unsigned)PFC) * 128u;
        asm volatile("s_getpc_b64 %0\n\ts_mov_b32 m0, %2\n\ts_mov_b64 exec, 0xff\n\tglobal_load_lds_dword %1, %0\n\ts_mov_b64 exec, -1"
                     : "=&s"(pc) : "v"(ln), "n"(OFF_JNK) : "memory");
    }
    SB
    float gc0 = -2.0f * 0.7978845608028654f * 1.4426950408889634f, gc1 = gc0 * 0.044715f;
#define FIN_ST(c)
#define FIN_SA(c)
#define FINISH(c) { const unsigned long long tn = __builtin_amdgcn_s_memrealtime(), te = tn + 4 * (zb - za) + 600 * (((unsigned long long)ka >> $) & 7); while (__builtin_amdgcn_s_memrealtime() < te) __builtin_amdgcn_s_sleep(2); } STAMP(5) FIN_ST(c) FIN_SA(c) __builtin_amdgcn_endpgm(); __builtin_unreachable();
    unsigned lane16 = (unsigned)lane * 16u;
    int deep = w >> 2;
    Deep d;
    d.ldsb = (unsigned)(cw * 4096);
    unsigned o2 = min(lane16, 624u), im0 = (unsigned)(OFF_IDX + cw * 2048), io = (unsigned)((s0 + 16 * cw) * (NF * 4));
    mkboff(d, lane, cw, nq);
#pragma unroll
    for (int k = 0; k < 4; ++k) { d.sG[k] = (unsigned)(((k + rho) & 3) * (2 * 32768)); PINS(d.sG[k]); }
    d.unit = xcd + 8 * ((st & 7) >> 1);
    d.m1 = (unsigned)((nq ^ 1) | (st & 1) | ((d.unit + 32 - NF) >> 5));
    PIN(lane16); PIN(o2); PINS(im0); PINS(io); PINS(deep); PINS(d.ldsb); PINS(d.m1);
    SB const unsigned long long za = __builtin_amdgcn_s_memrealtime(); SB
    asm volatile("s_mov_b32 m0, %0" :: "s"(im0) : "memory");
    SB
    asm volatile("" :: "s"(idx), "s"(table), "s"(se_w1), "s"(se_w2), "s"(w_bi), "s"(w_deep1), "s"(w_cross), "s"(ws32));
    asm volatile("s_cmp_lg_u32 %3, 0\n\ts_cbranch_scc1 1f\n\tglobal_load_lds_dwordx4 %0, %2\n\tglobal_load_lds_dwordx4 %1, %2 offset:1024\n1:"
                 :: "v"(lane16), "v"(o2), "s"((const char*)idx + io), "s"(deep) : "memory", "scc");
    SB
    typedef const __attribute__((address_space(4))) unsigned long long* kap;
    kap ka = (kap)__builtin_amdgcn_kernarg_segment_ptr();
    PINS(ka);
    d.wd2 = (const char*)ka[8];
    const gp out = (gp)ka[9];
    unsigned long long w1p;
    asm volatile("s_mov_b64 %0, %1" : "=s"(w1p) : "s"(w_deep1));
    d.rw1 = __builtin_amdgcn_make_buffer_rsrc((void*)w1p, (short)0, NF * 32768, 0x00020000);
    if (deep) {
        asm volatile("s_mov_b32 m0, %2\n\ts_nop 0\n\tbuffer_load_dword %0, %1, 0 offen lds" :: "v"(min(lane16, 80u) * 8u + (unsigned)((bid * 4 + cw) * 768)), "s"(d.rw1), "n"(OFF_JNK) : "memory");
    }
    prepB<0>(d);
    SB const unsigned long long zb = __builtin_amdgcn_s_memrealtime(); SB
    TS(tz1)
    BARRIER;
    TS(tz2)
    Cross x;
    x.c = cw; x.nq = nq; x.rho = rho; x.st = st; x.lane16 = lane16; x.ws = (char*)ws32; x.ldsb = d.ldsb;
    unsigned srcA, srcB, ib[4];
    u32x2 ia[NP], ic[NP];
    if (deep) {
        issueB<0>(d); issueB<1>(d); issueB<2>(d); issueB<3>(d);
        SB
        dma16((unsigned)((cw & 1) * 4096 + (lane >> 1) * 128 + nq * 32 + (lane & 1) * 16), w_bi, OFF_WBI + (cw & 1) * 1024);
        dma4((unsigned)min(cw * 64 + lane, 168) * 4u, se_w1, OFF_SW1 + cw * 256);
        const int k = (l15 & 1) | ((l15 >> 2) & 2), r = 4 * cw + g;
        dma4((unsigned)((r < 13 ? r : 12) * (NF * 2) + k * 16 + (k == 3 ? 0 : ((l15 + 2 * rho) & 6) * 2)), se_w2, OFF_SW2 + cw * 256);
        SB
        if (__builtin_expect(d.m1 == 0u, 1)) {
            const unsigned r0 = (unsigned)(16 * cw + (lane >> 3)), lo = (lane16 & 0x70u) + (unsigned)(d.unit * 128);
            dma16(min(r0, 2u * NF - 1) * (NF * 128) + lo, w_cross, OFF_RAW + cw * 2048);
            dma16(min(r0 + 8u, 2u * NF - 1) * (NF * 128) + lo, w_cross, OFF_RAW + cw * 2048 + 1024);
            dma4sc((lane16 >> 2) + (unsigned)(cw * 256), ws32, OFF_NB + cw * 256);
        }
        if (cw == 3) dma4sc(tag_addr(lane, d.unit, nq, st, xcd), ws32, OFF_TAG);
        if ((cw ^ 2) + nq == 0 && lane < 32) st16sc(lane16, (char*)ws32 + WS_SUM + st * 512, u32x4{0u, 0u, 0u, 0u});
        SB
        if (nq != 3) { prepB<1>(d); asm volatile("s_nop 0"); issueB<0>(d); issueB<1>(d); issueB<2>(d); issueB<3>(d); }
        SB
    } else {
        Cross& c = x;
        srcA = (unsigned)(((lane & 7) ^ g) * 16); srcB = srcA ^ 64u;
        unsigned ib0 = (unsigned)(OFF_IDX + cw * 2048 + (lane >> 3) * 104);
#pragma unroll
        for (int k = 0; k < 4; ++k) { ib[k] = ib0 + (unsigned)(((k + rho) & 3) * 8); PIN(ib[k]); c.tb[k] = (const char*)table - k * 1024; PINS(c.tb[k]); }
        PIN(srcA); PIN(srcB); PIN(ib0);
        set_m0<0>(c.ldsb);
        SB
        WAITCNT(0, 15);
        asm volatile("" ::: "memory");
        STAMP(1)
        TP(c.tq[0])
        ia[0] = LDS(const u32x2, ib0 + 96); ic[0] = LDS(const u32x2, ib0 + 96 + 832);
#pragma unroll
        for (int q = 1; q < 3; ++q) { ia[q] = LDS(const u32x2, ib[q - 1]); ic[q] = LDS(const u32x2, ib[q - 1] + 832); }
        SB
#define GATHER(q) issueA<0>(c, ia[q][0] * 128u + srcA); issueA<1>(c, ic[q][0] * 128u + srcB); issueA<2>(c, ia[q][1] * 128u + srcA); issueA<3>(c, ic[q][1] * 128u + srcB);
        GATHER(0)
        set_m0<SLOT>(c.ldsb);
        SB
        GATHER(1)
        set_m0<2 * SLOT>(c.ldsb);
        TP(c.tq[1])
    }
    TS(tz3)
    int crs = deep ^ 1;
    PINS(crs); PINS(deep);
    if (crs == 0) {
        Deep& c = d;
        int lane;
        asm volatile("v_mbcnt_lo_u32_b32 %0, -1, 0\n\tv_mbcnt_hi_u32_b32 %0, -1, %0" : "=v"(lane));
        const int l15 = lane & 15, g = lane >> 4, tid = w * 64 + lane;
        c.c = cw; c.nq = nq; c.rho = rho; c.st = st; c.lane16 = (unsigned)lane * 16u; c.ws = (char*)ws32;
        c.m3 = (unsigned)(cw ^ 3); c.m4 = c.m3 | min(c.m1, (unsigned)nq);
        c.tad = tag_addr(lane, c.unit, nq, st, xcd);
        PIN(c.tad);
        SB
        mkboff(d, lane, cw, nq);
        {
            const unsigned a0 = (unsigned)(cw * 4096 + (cw & 1) * 2048 + l15 * 128 + ((4 * (cw >> 1) + g) ^ (l15 >> 1)) * 16);
            const int q = l15 >> 2, p = l15 & 3;
            const unsigned b0 = (unsigned)(OFF_B + cw * 4096 + (8 * g + q) * 128 + ((p >> 1) ^ (q & 2) ^ ((g & 1) << 2)) * 16 + (p & 1) * 8);
#pragma unroll
            for (int k = 0; k < 4; ++k) { c.ra[k] = a0 ^ (unsigned)(k * 4096); c.rb[k] = b0 ^ (unsigned)(k * 32); PIN(c.ra[k]); PIN(c.rb[k]); }
#pragma unroll
            for (int k = 0; k < 16; ++k) { c.ac[k >> 2][k & 3] = f32x4{0.f, 0.f, 0.f, 0.f}; PIN(c.ac[k >> 2][k & 3]); }
        }
        unsigned exw[4], fl = (c.lane16 >> 2) + (unsigned)(OFF_FL + cw * 256), fr = c.lane16 + OFF_FL, s2 = c.lane16 >> 3, s8 = c.lane16 >> 1;
#pragma unroll
        for (int k = 0; k < 4; ++k) { exw[k] = c.lane16 + (unsigned)(OFF_B + (cw ^ k) * 4096); PIN(exw[k]); }
        double big = 0x1.8p52;
        float va[2] = {0.f, 0.f};
        const unsigned dl = (unsigned)(l15 - 4 * g), on = (dl & 1u) ? 0x3f800000u : 0x3f80u;
        u32x4 unit = {dl < 2u ? on : 0u, dl - 2u < 2u ? on : 0u, dl < 2u ? on : 0u, dl - 2u < 2u ? on : 0u};
        PIN(fl); PIN(fr); PIN(s2); PIN(s8); PIN(big); PIN(gc0); PIN(gc1); PIN(va[0]); PIN(va[1]); PIN(unit);
        c.wsf = c.ws + WS_FRAG + nq * 65536 + cw * 4096;
        c.pmask = cw != 3 ? 0ull : (c.m1 == 0u ? 0x1ffull : 0x100ull);
        PINS(c.pmask); PINS(c.m3); PINS(c.m4); PINS(c.wsf);
        if (nq == 3) { prepB<1>(c); asm volatile("s_nop 0"); issueB<0>(c); issueB<1>(c); issueB<2>(c); issueB<3>(c); }
        TP(c.tq[0])
        WAITCNT(4, 0); BARRIER;
        TP(c.tq[1])
        prepB<2>(c); asm volatile("s_nop 0"); issueB<0>(c); issueB<1>(c); issueB<2>(c); issueB<3>(c);
        TP(c.tq[2])
        produce(c);
        c.tag = LDSV(OFF_TAG + (c.lane16 >> 2));
        PIN(c.tag);
        TP(c.tq[3])
        BARRIER;
        TP(c.tq[4])
        gp sums = (gp)c.ws + (unsigned)(WS_SUM + (s0 + 16 * cw) * 8), outp = out + (unsigned)((s0 + 16 * cw) * 2);
        PINS(sums); PINS(outp);
        DStep<0>::run(c);
        TS(c.ts[13])
        u32x4 px[3][2];
#define EXC(e) { _Pragma("unroll") for (int k = 0; k < 8; ++k) px[(e) - 1][k >> 2][k & 3] = cvt_o(c.ac[e][k >> 1][2 * (k & 1)], c.ac[e][k >> 1][2 * (k & 1) + 1]); SB }
#define EXA(e, k) (((e) == 3 ? 3 * SLOT : ((e) - 1) * 2048) + (k) * 1024)
#define EXW(e) { LDS(u32x4, exw[0] + EXA(e, 0)) = px[(e) - 1][0]; LDS(u32x4, exw[0] + EXA(e, 1)) = px[(e) - 1][1]; SB }
#define EX2(e, k) { px[(e) - 1][(k) >> 1][2 * ((k) & 1)] = cvt_o(c.ac[e][k][0], c.ac[e][k][1]); px[(e) - 1][(k) >> 1][2 * ((k) & 1) + 1] = cvt_o(c.ac[e][k][2], c.ac[e][k][3]); SB }
        WAITCNT(0, 15); SB
        LDS(unsigned, fl + 2048) = fl; SB
        TE(c.tq[0])
        m4<12, 3>(c); m4<12, 2>(c);
        mk<12, 1, 0>(c); mk<12, 1, 1>(c); EX2(3, 0) mk<12, 1, 2>(c); EX2(3, 1) mk<12, 1, 3>(c); EX2(3, 2)
        mk<12, 0, 0>(c); EX2(3, 3) mk<12, 0, 1>(c); EX2(2, 0) mk<12, 0, 2>(c); EX2(2, 1) mk<12, 0, 3>(c); EX2(2, 2)
        EX2(2, 3) EXC(1)
        EXW(3) EXW(2) EXW(1)
        TE(c.tq[1])
        SB
        LDS(unsigned, fl + 1024) = fl;
        SB
        unsigned long long watch = 0ull;
        if (cw == 3) {
            watch = __builtin_amdgcn_ballot_w64(LDSV(OFF_TAG + 512 + (c.lane16 >> 2)) == c.tag) & (nq ? 0ull : 0xe00ull);
            if (c.m1 == 0u) {
                const u32x4 e4 = LDS(const u32x4, OFF_NB + c.lane16), l4 = LDS(const u32x4, OFF_NB + 1024 + c.lane16);
                watch |= __builtin_amdgcn_ballot_w64(e4[0] == l4[0] || e4[1] == l4[1] || e4[2] == l4[2] || e4[3] == l4[3]);
            }
            publish(c, c.tag + 2u);
        }
        TE(c.tq[2])
#pragma unroll 1
        for (int it = 0; it < (1 << 20) && __builtin_amdgcn_ballot_w64(LDSV(fr + 1024) == 0u) != 0ull; ++it) { }
        asm volatile("" ::: "memory");
        TE(c.tq[3])
        u32x2 pa[4], pb[4];
#pragma unroll
        for (int k = 0; k < 2; ++k) px[2][k] = LDS(const u32x4, exw[3] + EXA(3, k));
#pragma unroll
        for (int k = 0; k < 4; ++k) {
            pa[k] = *(volatile const __attribute__((address_space(3))) u32x2*)(size_t)(exw[1] + EXA(1, k >> 1) + (k & 1) * 8);
            pb[k] = *(volatile const __attribute__((address_space(3))) u32x2*)(size_t)(exw[2] + EXA(2, k >> 1) + (k & 1) * 8);
        }
        SB
#pragma unroll
        for (int k = 0; k < 4; ++k)
            asm volatile("v_mfma_f32_16x16x16_bf16 %0, %1, %2, %0" : "+v"(c.ac[0][k]) : "v"(u32x2{unit[0], unit[1]}), "v"(u32x2{px[2][k >> 1][2 * (k & 1)], px[2][k >> 1][2 * (k & 1) + 1]}));
        asm volatile("s_nop 2");
#pragma unroll
        for (int k = 0; k < 4; ++k) mfa(c.ac[0][k], unit, u32x4{pa[k][0], pa[k][1], pb[k][0], pb[k][1]});
        asm volatile("s_nop 4");
#pragma unroll
        for (int k = 0; k < 4; ++k) {
            float x[4] = {c.ac[0][k][0], c.ac[0][k][1], c.ac[0][k][2], c.ac[0][k][3]}, y[4];
            asm volatile("v_mul_f32 %4, %0, %0\n\tv_mul_f32 %5, %1, %1\n\tv_mul_f32 %6, %2, %2\n\tv_mul_f32 %7, %3, %3\n\t"
                         "v_fma_f32 %4, %4, %15, %14\n\tv_fma_f32 %5, %5, %15, %14\n\tv_fma_f32 %6, %6, %15, %14\n\tv_fma_f32 %7, %7, %15, %14\n\t"
                         "v_mul_f32 %4, %4, %0\n\tv_mul_f32 %5, %5, %1\n\tv_mul_f32 %6, %6, %2\n\tv_mul_f32 %7, %7, %3\n\t"
                         "v_exp_f32 %4, %4\n\tv_exp_f32 %5, %5\n\tv_exp_f32 %6, %6\n\tv_exp_f32 %7, %7\n\t"
                         "v_mul_f32 %0, %0, %10\n\tv_mul_f32 %1, %1, %11\n\tv_mul_f32 %2, %2, %12\n\tv_mul_f32 %3, %3, %13\n\t"
                         "v_add_f32 %4, 1.0, %4\n\tv_add_f32 %5, 1.0, %5\n\tv_add_f32 %6, 1.0, %6\n\tv_add_f32 %7, 1.0, %7\n\t"
                         "v_rcp_f32 %4, %4\n\tv_rcp_f32 %5, %5\n\tv_rcp_f32 %6, %6\n\tv_rcp_f32 %7, %7\n\t"
                         "v_fmac_f32 %8, %0, %4\n\tv_fmac_f32 %9, %1, %5\n\tv_fmac_f32 %8, %2, %6\n\tv_fmac_f32 %9, %3, %7"
                         : "+v"(x[0]), "+v"(x[1]), "+v"(x[2]), "+v"(x[3]), "=&v"(y[0]), "=&v"(y[1]), "=&v"(y[2]), "=&v"(y[3]), "+v"(va[0]), "+v"(va[1])
                         : "v"(c.w2[4 * k]), "v"(c.w2[4 * k + 1]), "v"(c.w2[4 * k + 2]), "v"(c.w2[4 * k + 3]), "v"(gc0), "v"(gc1));
        }
        TE(c.tq[4])
        const float v = sum16(sum32(va[0] + va[1]));
        TS(c.ts[14])
        if (__builtin_amdgcn_inverse_ballot_w64(0xffffull)) deliver(c, v, 2 * nq, s2, s8, big, sums, outp);
        if (__builtin_expect(watch != 0ull, 0)) {
            const u32x4 e4 = LDS(const u32x4, OFF_NB + c.lane16), l4 = LDS(const u32x4, OFF_NB + 1024 + c.lane16);
            bool opn[4];
#pragma unroll
            for (int k = 0; k < 4; ++k) opn[k] = c.m1 == 0u && e4[k] == l4[k];
            bool own = nq == 0 && lane >= 9 && lane < 12 && LDSV(OFF_TAG + 512 + lane * 4) == c.tag;
            unsigned f = c.tag + 3u;
#pragma unroll 1
            for (int it = 0; it < (1 << 22) && __builtin_amdgcn_ballot_w64(opn[0] || opn[1] || opn[2] || opn[3] || own) != 0ull; ++it) {
                bool chg = false;
#pragma unroll
                for (int k = 0; k < 4; ++k)
                    if (opn[k] && __hip_atomic_load(ws32 + lane * 4 + k, __ATOMIC_RELAXED, __HIP_MEMORY_SCOPE_AGENT) != e4[k]) { opn[k] = false; chg = true; }
                if (own && __hip_atomic_load(ws32 + (c.tad >> 2), __ATOMIC_RELAXED, __HIP_MEMORY_SCOPE_AGENT) != c.tag) { own = false; chg = true; }
                if (__builtin_amdgcn_ballot_w64(chg) != 0ull) { publish(c, f); ++f; }
                else __builtin_amdgcn_s_sleep(2);
            }
        }
        FINISH(c)
    } else {
        Cross& c = x;
#pragma unroll
        for (int q = 3; q < NP; ++q) { ia[q] = LDS(const u32x2, ib[(q - 1) & 3] + ((q - 1) >> 2) * 32); ic[q] = LDS(const u32x2, ib[(q - 1) & 3] + ((q - 1) >> 2) * 32 + 832); }
#pragma unroll
        for (int q = 3; q < NP; ++q) {
            c.ga[4 * q - 12] = ia[q][0] * 128u + srcA; c.ga[4 * q - 11] = ic[q][0] * 128u + srcB;
            c.ga[4 * q - 10] = ia[q][1] * 128u + srcA; c.ga[4 * q - 9] = ic[q][1] * 128u + srcB;
        }
#pragma unroll
        for (int k = 0; k < 40; ++k) PIN(c.ga[k]);
        {
            const int G = (g + rho) & 3;
            const unsigned ea = (unsigned)(cw * 4096 + l15 * 128);
            c.re[0] = ea + (unsigned)((g ^ (l15 >> 1)) * 16); c.re[1] = c.re[0] ^ 64u;
            c.rx[0] = ea + (unsigned)(((g + 1) & 3) * SLOT + ((2 * nq) ^ (l15 >> 1)) * 16); c.rx[1] = c.rx[0] ^ 16u;
            c.rx0 = ea + (unsigned)((G & 1) * 2048 + ((2 * nq + (G >> 1)) ^ (l15 >> 1)) * 16);
            c.six = (l15 & 1) ? 0xeeeeu : 0x4444u;
#pragma unroll
            for (int k = 0; k < 4; ++k) { const unsigned o = (l15 >> 1) == 2 * k + (g >> 1) ? 0x3f803f80u : 0u; c.on[k] = u32x4{o, o, o, o}; PIN(c.on[k]); }
            PIN(c.re[0]); PIN(c.re[1]); PIN(c.rx[0]); PIN(c.rx[1]); PIN(c.rx0); PIN(c.six);
            zclear<3>(c); zclear<0>(c);
        }
        SB
        BARRIER;
        GATHER(2)
        TP(c.tq[2])
        {
            const int q = (lane >> 2) & 3, p = lane & 3;
            const unsigned a0 = (unsigned)(OFF_WBI + (8 * g + q) * 32 + p * 8), a1 = (unsigned)(OFF_WBI + (8 * g + q) * 32 + ((p + rho) & 3) * 8);
#pragma unroll
            for (int k = 0; k < 2; ++k) {
                c.wb[k] = CAT8(tr_read(a0 + k * 1024), tr_read(a0 + k * 1024 + 128));
                c.wr[k] = CAT8(tr_read(a1 + k * 1024), tr_read(a1 + k * 1024 + 128));
            }
#pragma unroll
            for (int k = 0; k < 2; ++k) { PIN(c.wb[k]); PIN(c.wr[k]); }
        }
        gp sums = (gp)c.ws + (unsigned)(WS_SUM + (s0 + 16 * cw) * 8), outp = out + (unsigned)((s0 + 16 * cw) * 2);
        PINS(sums); PINS(outp);
        c.wsf = c.ws + WS_FRAG + 32768 + nq * 65536 + cw * 4096; PINS(c.wsf);
        TP(c.tq[3])
        WAITCNT(8, 0); BARRIER;
        TP(c.tq[4])
        __builtin_amdgcn_s_setprio(1);
        CStep<0>::run(c);
        TS(c.ts[11])
        u32x4 bq[4];
        u32x4 (&bp)[4] = c.bp;
        f32x2 T[6][8];
#define SPLIT(s0, s1, i0, i1) { _Pragma("unroll") for (int s = s0; s < s1; ++s) _Pragma("unroll") for (int i_ = i0; i_ < i1; ++i_) { \
            const f32x4 x = c.ew[2 * (1 + 4 * (s >> 1) + (i_ >> 1)) + (s & 1)]; T[s][i_] = f32x2{x[2 * (i_ & 1)], x[2 * (i_ & 1) + 1]}; } }
#define CB(B, i, h, k) { B[(2 * (i) + (h)) & 3][k] = cvt_o(T[2 * (k)][i][h], T[2 * (k) + 1][i][h]); }
#define SC(i, k) { asm volatile("v_pk_mul_f32 %0, %1, %0 op_sel_hi:[0,1]" : "+v"(T[2 * (k)][i]) : "v"(ao[k])); asm volatile("v_pk_mul_f32 %0, %1, %0 op_sel:[1,0]" : "+v"(T[2 * (k) + 1][i]) : "v"(ao[k])); }
        SPLIT(0, 4, 0, 8)
        SB
#define FPOS 2
#define FI(n) if ((n) >= FPOS && (n) < FPOS + 4) { if (!(((n) - FPOS) & 1)) { set_m0<FOFF[2 + ((((n) - FPOS) >> 1) & 1)]>(c.ldsb); asm volatile("s_nop 0"); } \
            dma16sco<(((n) - FPOS) & 1) * 2048>(c.lane16, c.wsf + (((n) - FPOS) >> 1) * 16384); dma16sco<(((n) - FPOS) & 1) * 2048 + 1024>(c.lane16, c.wsf + (((n) - FPOS) >> 1) * 16384); SB }
        cmm<10, 2>(c); rdE<11, 0>(c);
        cmm<10, 3>(c); rdE<11, 1>(c);
        cmm<10, 4>(c); rdE<11, 2>(c);
        cmm<10, 5>(c); rdE<11, 3>(c);
        cmm<10, 0>(c); rdE<12, 0>(c); rdE<12, 1>(c);
        cmm<10, 1>(c); rdE<12, 2>(c); rdE<12, 3>(c);
        FI(0)
        CB(bp, 0, 0, 1) CB(bp, 0, 1, 1) CB(bp, 1, 0, 1) CB(bp, 1, 1, 1)
        cmm<11, 0>(c); sw1<2, 0, 0>(c);
        cmm<11, 1>(c); sw1<2, 0, 1>(c);
        cmm<12, 0>(c); sw1<2, 0, 2>(c);
        cmm<12, 1>(c); sw1<2, 0, 3>(c);
        STAMP(2)
        TS(c.ts[12])
        cmm<11, 2>(c); sw1<2, 0, 4>(c);
        cmm<11, 3>(c); sw1<2, 0, 5>(c);
        cmm<11, 4>(c); sw1<2, 0, 6>(c);
        cmm<11, 5>(c); zpack<2>(c);
        cmm<12, 2>(c); sw1<2, 0, 7>(c);
        const f32x4 hp = mf0(c.s1, c.zq);
        cmm<12, 3>(c); cmm<12, 4>(c); cmm<12, 5>(c);
        rdX<12>(c);
        FI(1)
        float hq[4];
        asm volatile("v_mul_f32 %0, %4, %4\n\tv_mul_f32 %1, %5, %5\n\tv_mul_f32 %2, %6, %6\n\tv_mul_f32 %3, %7, %7\n\t"
                     "v_fma_f32 %0, %0, %9, %8\n\tv_fma_f32 %1, %1, %9, %8\n\tv_fma_f32 %2, %2, %9, %8\n\tv_fma_f32 %3, %3, %9, %8\n\t"
                     "v_mul_f32 %0, %0, %4\n\tv_mul_f32 %1, %1, %5\n\tv_mul_f32 %2, %2, %6\n\tv_mul_f32 %3, %3, %7\n\t"
                     "v_exp_f32 %0, %0\n\tv_exp_f32 %1, %1\n\tv_exp_f32 %2, %2\n\tv_exp_f32 %3, %3\n\t"
                     "v_fma_f32 %0, %0, %10, %10\n\tv_fma_f32 %1, %1, %10, %10\n\tv_fma_f32 %2, %2, %10, %10\n\tv_fma_f32 %3, %3, %10, %10\n\t"
                     "v_rcp_f32 %0, %0\n\tv_rcp_f32 %1, %1\n\tv_rcp_f32 %2, %2\n\tv_rcp_f32 %3, %3\n\t"
                     "v_mul_f32 %0, %0, %4\n\tv_mul_f32 %1, %1, %5\n\tv_mul_f32 %2, %2, %6\n\tv_mul_f32 %3, %3, %7\n\t"
                     "v_cvt_pk_bf16_f32 %0, %0, %1\n\tv_cvt_pk_bf16_f32 %1, %2, %3"
                     : "=&v"(hq[0]), "=&v"(hq[1]), "=&v"(hq[2]), "=&v"(hq[3]) : "v"(hp[0]), "v"(hp[1]), "v"(hp[2]), "v"(hp[3]), "v"(c.k0), "v"(c.k1), "v"(c.k2));
        FI(2)
        const f32x2 hb = {hq[0], hq[1]};
        swn<2, 1, 0, 2>(c);
        f32x4 av[2];
        asm volatile("v_mfma_f32_16x16x16_bf16 %0, %1, %2, 0" : "=&v"(av[0]) : "v"(c.wa[0]), "v"(hb));
        asm volatile("v_mfma_f32_16x16x16_bf16 %0, %1, %2, 0" : "=&v"(av[1]) : "v"(c.wa[1]), "v"(hb));
        FI(3)
        swn<2, 1, 2, 8>(c);
        swn<2, 2, 0, 2>(c);
        FI(4)
        TS(c.ts[13])
        SB
        float as[8];
#pragma unroll
        for (int r = 0; r < 8; ++r) as[r] = exp_o(av[r >> 2][r & 3]);
        asm volatile("v_add_f32 %0, 1.0, %0\n\tv_add_f32 %1, 1.0, %1\n\tv_add_f32 %2, 1.0, %2\n\tv_add_f32 %3, 1.0, %3\n\tv_add_f32 %4, 1.0, %4\n\tv_add_f32 %5, 1.0, %5\n\tv_add_f32 %6, 1.0, %6\n\tv_add_f32 %7, 1.0, %7\n\t"
                     "v_rcp_f32 %0, %0\n\tv_rcp_f32 %1, %1\n\tv_rcp_f32 %2, %2\n\tv_rcp_f32 %3, %3\n\tv_rcp_f32 %4, %4\n\tv_rcp_f32 %5, %5\n\tv_rcp_f32 %6, %6\n\tv_rcp_f32 %7, %7"
                     : "+v"(as[0]), "+v"(as[1]), "+v"(as[2]), "+v"(as[3]), "+v"(as[4]), "+v"(as[5]), "+v"(as[6]), "+v"(as[7]));
        FI(5)
        unsigned flg = LDSV(c.wf[1] + (OFF_FL + 2048 - OFF_B - SLOT));
        SB
        swn<2, 2, 2, 8>(c);
        FI(6)
        SPLIT(4, 6, 0, 2) SPLIT(4, 6, 4, 6)
        TE(c.tq[0])
        SB
        WAITCNT(63, 0);
        wait_set(flg, c.wf[1] + (OFF_FL + 2048 - OFF_B - SLOT));
        SB
#define WSEQ(n) LDS(const u32x4, c.wf[(((n) >> 4) & 1) ^ 1] + ((n) >> 5) * SLOT + ((n) & 15) * 1024)
        u32x4 wq[64];
#pragma unroll
        for (int r = 0; r < WPRE; ++r) wq[r] = WSEQ(r);
        SB
        CB(bp, 0, 0, 2) CB(bp, 0, 1, 2)
        f32x2 ao[4] = {{as[0], as[1]}, {as[2], as[3]}, {as[4], as[5]}, {as[6], as[7]}};
        {
            f32x2 m[4] = {{c.ew[0][0], c.ew[0][1]}, {c.ew[1][0], c.ew[1][1]}, {c.ew[0][2], c.ew[0][3]}, {c.ew[1][2], c.ew[1][3]}};
#pragma unroll
            for (int r = 0; r < 4; r += 2) {
                asm volatile("v_pk_mul_f32 %0, %1, %0 op_sel_hi:[0,1]" : "+v"(m[r]) : "v"(ao[3]));
                asm volatile("v_pk_mul_f32 %0, %1, %0 op_sel:[1,0]" : "+v"(m[r + 1]) : "v"(ao[3]));
            }
            SC(0, 0) SC(0, 1) SC(0, 2)
#pragma unroll
            for (int r = 0; r < 4; ++r) bq[r][3] = cvt_o(m[r & 2][r & 1], m[(r & 2) + 1][r & 1]);
        }
        ao[3][0] = sel(c.am, as[7], as[6]);
        PIN(ao[3]);
        SB
        TE(c.tq[1])
        f32x4 dg;
        f32x4 u[2][4];
        f32x2 sv[2][4];
        u32x4 pk[7];
#define FM(h, tl) { _Pragma("unroll") for (int q = 0; q < 2; ++q) asm volatile("v_pk_fma_f32 %0, %1, %2, %3" : "=v"(sv[h][2 * (tl) + q]) : "v"(f32x2{u[h][2 + (tl)][2 * q], u[h][2 + (tl)][2 * q + 1]}), "v"(ao[2 * (tl) + q]), "v"(f32x2{u[h][tl][2 * q], u[h][tl][2 * q + 1]})); }
#define PK(i, s) { pk[s][(i) & 3] = cvt_o(sv[0][(s) >> 1][(s) & 1], sv[1][(s) >> 1][(s) & 1]); }
#define DOT(eh, s) { const u32x4 x_ = (s) < 6 ? c.X[1 + 4 * ((s) >> 1) + ((s) & 1) + 2 * (eh)] : c.X[0]; if ((eh) || (s)) mfa(dg, pk[s], x_); else dg = mf0(pk[s], x_); }
#pragma unroll
        for (int i = 0; i < 8; ++i) {
            if (i == 3) { SB wait_set(flg, c.wf[1] + (OFF_FL - OFF_B - SLOT)); SB }
            if (i == 1) { SB WAITCNT(0, 15); LDS(unsigned, (c.lane16 >> 2) + (unsigned)(OFF_FL + cw * 256)) = c.wf[0]; SB }
            if (i == 2) { TE(c.tq[2]) }
            if (i == 4) { TE(c.tq[3]) }
            if (i == 1) { SPLIT(4, 6, 2, 4) SPLIT(4, 6, 6, 8) }
#pragma unroll
            for (int j = 0; j < 8; ++j) {
                const int n = 8 * i + j, h = j >> 2;
                const bool nx = i < 7, pv = i >= 1;
                SB
                if (!(j & 1)) { if (n + WPRE <= 64) WAITCNT(63, WPRE - 2); SB }
                u[h][j & 3] = mf0(wq[n], (j & 2) ? bq[(2 * i + h) & 3] : bp[(2 * i + h) & 3]);
                SB
                if (n == 17) flg = LDSV(c.wf[1] + (OFF_FL - OFF_B - SLOT));
                if (n + WPRE < 64) wq[n + WPRE] = WSEQ(n + WPRE);
                SB
                if (i == 0) { if (j == 2) swn<2, 3, 0, 1>(c); if (j == 3) swn<2, 3, 1, 2>(c); if (j == 4) swn<2, 3, 2, 4>(c); if (j == 5) swn<2, 3, 4, 5>(c); if (j == 6) swn<2, 3, 5, 7>(c); if (j == 7) swn<2, 3, 7, 8>(c); }
                if (j == 0) { CB(bq, i, 0, 0) if (i == 5) DOT(0, 4) CB(bq, i, 0, 1) }
                if (j == 1) { CB(bq, i, 0, 2) if (i == 5) DOT(0, 5) CB(bq, i, 1, 0) CB(bq, i, 1, 1) }
                if (j == 2) { CB(bq, i, 1, 2) if (i == 5) DOT(0, 6) if (pv) PK(i - 1, 0) }
                if (j == 3) { if (pv) { PK(i - 1, 1) if (i == 4) DOT(0, 0) PK(i - 1, 2) } }
                if (j == 4) { if (pv) { PK(i - 1, 3) if (i == 4) DOT(0, 1) PK(i - 1, 4) } }
                if (j == 5) { if (pv) { PK(i - 1, 5) if (i == 4) DOT(0, 2) PK(i - 1, 6) } }
                if (j == 6) { if (nx && i) { CB(bp, i + 1, 0, 0) if (i == 4) DOT(0, 3) CB(bp, i + 1, 1, 0) } }
                if (j == 7) {
                    if (nx && i) { CB(bp, i + 1, 0, 1) CB(bp, i + 1, 1, 1) }
                    if (nx) { CB(bp, i + 1, 0, 2) CB(bp, i + 1, 1, 2) }
                    FM(0, 0) FM(0, 1)
                    if (nx) { SC(i + 1, 0) SC(i + 1, 1) SC(i + 1, 2) } else asm volatile("s_nop 3");
                    FM(1, 0) FM(1, 1)
                }
            }
        }
        PK(7, 0) PK(7, 1) PK(7, 2) DOT(1, 0) PK(7, 3) PK(7, 4) DOT(1, 1) PK(7, 5) PK(7, 6) DOT(1, 2)
        unsigned s2;
        asm volatile("v_mbcnt_lo_u32_b32 %0, -1, 0\n\tv_mbcnt_hi_u32_b32 %0, -1, %0" : "=v"(s2));
        DOT(1, 3)
        asm volatile("v_lshlrev_b32 %0, 1, %0\n\tv_and_b32 %0, 30, %0" : "+v"(s2));
        DOT(1, 4)
        unsigned s8 = s2 * 4u;
        double big = 0x1.8p52;
        PIN(s8); PIN(big);
        SB
        DOT(1, 5) DOT(1, 6)
        float v, v2;
        asm volatile("s_mov_b64 vcc, %6\n\ts_nop 6\n\tv_cndmask_b32 %0, %2, %3, vcc\n\tv_cndmask_b32 %1, %4, %5, vcc\n\tv_cndmask_b32 %0, %0, %1, %7"
                     : "=&v"(v), "=&v"(v2) : "v"(dg[0]), "v"(dg[1]), "v"(dg[2]), "v"(dg[3]), "s"(0xaaaaaaaaaaaaaaaaull), "s"(0xccccccccccccccccull) : "vcc");
        TE(c.tq[4])
        STAMP(4)
        TS(c.ts[14])
        if (__builtin_amdgcn_inverse_ballot_w64(0xf0000f0000f0000full)) deliver(c, v, 2 * nq + 1, s2, s8, big, sums, outp);
        const int ln = __builtin_amdgcn_mbcnt_hi(~0u, __builtin_amdgcn_mbcnt_lo(~0u, 0u));
        const int lane = ln, tid = w * 64 + ln;
        (void)lane; (void)tid;
        FINISH(c)
    }
}

extern "C" void kernel_launch(void* const* d_in, const int* in_sizes, int n_in,
                              void* d_out, int out_size, void* d_ws, size_t ws_size,
                              hipStream_t stream) {
    const int* idx = (const int*)d_in[0];
    const uint16_t* table = (const uint16_t*)d_in[1];
    const uint16_t* se_w1 = (const uint16_t*)d_in[2];
    const uint16_t* se_w2 = (const uint16_t*)d_in[3];
    const uint16_t* w_bi = (const uint16_t*)d_in[4];
    const uint16_t* w_cross = (const uint16_t*)d_in[5];
    const uint16_t* w_deep1 = (const uint16_t*)d_in[6];
    const uint16_t* w_deep2 = (const uint16_t*)d_in[7];
    (void)hipFuncSetAttribute((const void*)k_main, hipFuncAttributeMaxDynamicSharedMemorySize, LDS_T);
    k_main<<<256, 512, LDS_T, stream>>>(idx, table, w_bi, w_deep1, w_cross, (unsigned*)d_ws, se_w1, se_w2, w_deep2, (__hip_bfloat16*)d_out, (unsigned long long*)((char*)d_ws + (128ull << 20)));
}
```

```cpp
#include <hip/hip_runtime.h>
#include <hip/hip_bf16.h>
#include <stdint.h>

#define EXM 1
#define PU 0
#define WSX 0
#define NF 26
#define TRC 1.0028f
#define NP 13
#define PROD_CLASS(slot) ((((slot) & 3) == 1) && (((slot) >> 2) & 1) == 0)
#define STAMP(k)
#define TS(x)
#define TSP(P)
#define TX(k)
#define TP(x)
#define TEM 0x3ff
#define TE(x)
#define TI(n)
#define TF(k)
#define TH(k)
#define TG(x) TE(x)
using bf16x8 = __attribute__((ext_vector_type(8))) short;
using f32x4 = __attribute__((ext_vector_type(4))) float;
using f32x8 = __attribute__((ext_vector_type(8))) float;
using f32x2 = __attribute__((ext_vector_type(2))) float;
using s16x4 = __attribute__((ext_vector_type(4))) short;
using u32x4 = __attribute__((ext_vector_type(4))) unsigned;
using u32x8 = __attribute__((ext_vector_type(8))) unsigned;
using u32x2 = __attribute__((ext_vector_type(2))) unsigned;
using bf2 = __attribute__((ext_vector_type(2))) __bf16;
#define PIN(x) asm volatile("" : "+v"(x))
#define PINS(x) asm volatile("" : "+s"(x))
#define SB __builtin_amdgcn_sched_barrier(0);
#define CAT8(a, b) __builtin_shufflevector((a), (b), 0, 1, 2, 3, 4, 5, 6, 7)

#define SLOT 16384
#define OFF_B 65536
#define OFF_FL 131072
#define OFF_WBI (OFF_FL + 4096)
#define OFF_SW1 (OFF_WBI + 2048)
#define OFF_SW2 (OFF_SW1 + 1024)
#define OFF_WD2 (OFF_SW2 + 1024)
#define OFF_TAG (OFF_WD2 + 256)
#define OFF_NB (OFF_TAG + 768)
#define OFF_JNK (OFF_NB + 2048)
#define OFF_IDX (OFF_JNK + 2048)
#define OFF_RAW (OFF_IDX + 8192)
#define LDS_T (OFF_IDX + 16384)
constexpr int FOFF[4] = {SLOT, OFF_B + SLOT, 2 * SLOT, OFF_B + 2 * SLOT};
#define P_FLAG 3
#define P_POLL 6
#define P_CHK (P_POLL + 3)
#define P_STAT 9

#define WS_FLAG 1024
#define WS_SUM 4096
#define WS_SIDE 65536
#define WS_FRAG (1 << 20)

#define LDS(T, a) (*(__attribute__((address_space(3))) T*)(size_t)(a))
#define LDSV(a) (*(volatile const __attribute__((address_space(3))) unsigned*)(size_t)(a))
__device__ __forceinline__ unsigned pk_bf16(float lo, float hi) {
    const f32x2 v = {lo, hi};
    return __builtin_bit_cast(unsigned, __builtin_convertvector(v, bf2));
}
__device__ __forceinline__ unsigned cvt_o(float lo, float hi) { unsigned r; asm volatile("v_cvt_pk_bf16_f32 %0, %1, %2" : "=v"(r) : "v"(lo), "v"(hi)); return r; }
__device__ __forceinline__ float exp_o(float a) { float r; asm volatile("v_exp_f32 %0, %1" : "=v"(r) : "v"(a)); return r; }
template <typename A, typename B> __device__ __forceinline__ f32x4 mf0(const A& a, const B& b) { f32x4 d; asm volatile("v_mfma_f32_16x16x32_bf16 %0, %1, %2, 0" : "=v"(d) : "v"(a), "v"(b)); return d; }
template <typename A, typename B> __device__ __forceinline__ void mfa(f32x4& d, const A& a, const B& b) { asm volatile("v_mfma_f32_16x16x32_bf16 %0, %1, %2, %0" : "+v"(d) : "v"(a), "v"(b)); }
__device__ __forceinline__ float lo_f(unsigned x) { return __uint_as_float(x << 16); }
__device__ __forceinline__ float hi_f(unsigned x) { return __uint_as_float(x & 0xffff0000u); }
__device__ __forceinline__ s16x4 tr_read(unsigned a) {
    typedef short v4s __attribute__((ext_vector_type(4)));
    return __builtin_amdgcn_ds_read_tr16_b64_v4i16((__attribute__((address_space(3))) v4s*)(size_t)a);
}
#define DMA(name, ins) __device__ __forceinline__ void name(unsigned voff, const void* sbase, unsigned ldsaddr) { \
    asm volatile("s_mov_b32 m0, %2\n\ts_nop 0\n\t" ins :: "v"(voff), "s"(sbase), "s"(ldsaddr) : "memory"); }
DMA(dma16, "global_load_lds_dwordx4 %0, %1") DMA(dma4, "global_load_lds_dword %0, %1")
DMA(dma16sc, "global_load_lds_dwordx4 %0, %1 sc1") DMA(dma4sc, "global_load_lds_dword %0, %1 sc1")
__device__ __forceinline__ void st16sc(unsigned voff, void* sbase, u32x4 v) {
    asm volatile("global_store_dwordx4 %0, %1, %2 sc1\n\ts_nop 1" :: "v"(voff), "v"(v), "s"(sbase) : "memory");
}
template <int O> __device__ __forceinline__ void set_m0(unsigned base) { asm volatile("s_add_u32 m0, %0, %1" :: "s"(base), "n"(O) : "memory", "scc"); }
template <int O> __device__ __forceinline__ void bdma16o(unsigned voff, __amdgpu_buffer_rsrc_t rs, unsigned soff) {
    asm volatile("buffer_load_dwordx4 %0, %1, %2 offen offset:%3 lds" :: "v"(voff), "s"(rs), "s"(soff), "n"(O) : "memory");
}
template <int O> __device__ __forceinline__ void dma16o(unsigned voff, const void* sbase) {
    asm volatile("global_load_lds_dwordx4 %0, %1 offset:%2" :: "v"(voff), "s"(sbase), "n"(O) : "memory");
}
template <int O> __device__ __forceinline__ void dma16sco(unsigned voff, const void* sbase) {
    asm volatile("global_load_lds_dwordx4 %0, %1 offset:%2 sc1" :: "v"(voff), "s"(sbase), "n"(O) : "memory");
}
#define WAITCNT(vm, lgkm) __builtin_amdgcn_s_waitcnt(((vm) & 15) | (((vm) >> 4) << 14) | 0x70 | ((lgkm) << 8))
#define WAITX(vm, x, m) { WAITCNT((vm) + (x), 0); asm volatile("s_cmp_eq_u32 %0, 0\n\ts_cbranch_scc1 1f\n\ts_waitcnt vmcnt(%1)\n1:" :: "s"(m), "n"(vm) : "memory", "scc"); }
#define BARRIER asm volatile("s_barrier" ::: "memory")
__device__ __forceinline__ float sum32(float v) { const u32x2 p = __builtin_amdgcn_permlane32_swap(__float_as_uint(v), __float_as_uint(v), false, false); return __uint_as_float(p[0]) + __uint_as_float(p[1]); }
__device__ __forceinline__ float sum16(float v) { const u32x2 p = __builtin_amdgcn_permlane16_swap(__float_as_uint(v), __float_as_uint(v), false, false); return __uint_as_float(p[0]) + __uint_as_float(p[1]); }
__device__ __forceinline__ float sel(unsigned m, float a, float b) {
    return __uint_as_float(__builtin_amdgcn_bitop3_b32(m, __float_as_uint(a), __float_as_uint(b), 0xca));
}
__device__ __forceinline__ void wait_set(unsigned f, unsigned a) {
    if (__builtin_expect(__builtin_amdgcn_ballot_w64(f == 0u) != 0ull, WSX)) {
#pragma unroll 1
        for (int it = 0; it < (1 << 20) && __builtin_amdgcn_ballot_w64(LDSV(a) == 0u) != 0ull; ++it) { }
    }
    asm volatile("" ::: "memory");
}

struct Com {
    char* ws;
    unsigned lane16, ldsb;
    int c, nq, rho, st;
};
typedef __attribute__((address_space(1))) char* gp;
__device__ __forceinline__ void deliver(const Com& c, float v, int part, unsigned s2, unsigned s8, double big, gp sums, gp outp) {
    const gp side = (gp)c.ws + (WS_SIDE + c.st * 2048 + c.c * 64);
    u32x2 mine = __builtin_bit_cast(u32x2, __builtin_fma((double)v, 0x1p42, big));
    mine[1] += 0x04004000u - 0x43380000u;
    const unsigned ab = __float_as_uint(v) << 1;
    if (__builtin_expect(ab - 0x61000000u >= 0x22000000u && ab != 0u, 0)) {
        mine = u32x2{0u, 0x04004000u + (0x40000u << part)};
        __hip_atomic_store((__attribute__((address_space(1))) float*)(side + part * 256 + s2 * 2), v, __ATOMIC_RELAXED, __HIP_MEMORY_SCOPE_AGENT);
        asm volatile("s_waitcnt vmcnt(0)" ::: "memory");
    }
    const unsigned long long mv = __builtin_bit_cast(unsigned long long, mine);
    const unsigned long long tot = mv + __hip_atomic_fetch_add((__attribute__((address_space(1))) unsigned long long*)(sums + s8), mv, __ATOMIC_RELAXED, __HIP_MEMORY_SCOPE_AGENT);
    const unsigned th = (unsigned)(tot >> 32);
    if (th >= 0x20000000u) {
        const u32x2 dv = {(unsigned)tot, (th & 0x3ffffu) | 0x43300000u};
        float r = (float)__builtin_fma(__builtin_bit_cast(double, dv), 0x1p-42, -0x1.2p10);
        if (__builtin_expect(th >= 0x20040000u, 0)) {
#pragma unroll 1
            for (int k = 0; k < 8; ++k)
                if ((th >> (18 + k)) & 1u) r += __hip_atomic_load((__attribute__((address_space(1))) float*)(side + k * 256 + s2 * 2), __ATOMIC_RELAXED, __HIP_MEMORY_SCOPE_AGENT);
        }
        *(__attribute__((address_space(1))) uint16_t*)(outp + s2) = (uint16_t)pk_bf16(r, r);
    }
}

struct Deep : Com {
    __amdgpu_buffer_rsrc_t rw1;
    const char* wsf; const char* wd2; const char* wsh;
    unsigned ldsh;
    unsigned sG[4], sB;
    unsigned m1, m3, m4;
    int unit;
    unsigned long long pmask;
    unsigned tad, tag, pl;
    unsigned boff[4], ra[4], rb[4];
    u32x4 w2[2];
    f32x4 ac[4][4];
    bf16x8 fa[2][4];
    s16x4 lo[2][4], hi[2][4];
};
template <int Q> __device__ __forceinline__ void prepB(Deep& c) {
    if constexpr (Q == 0) c.sB = 24 * 32768; else c.sB = c.sG[(Q - 1) & 3] + ((Q - 1) >> 2) * (8 * 32768);
    PINS(c.sB);
    set_m0<OFF_B + (Q & 3) * SLOT>(c.ldsb);
}
template <int I> __device__ __forceinline__ void issueB(Deep& c) { bdma16o<I * 1024>(c.boff[I], c.rw1, c.sB); }
__device__ __forceinline__ void mkboff(Deep& d, int lane, int cw, int nq) {
    const unsigned b0 = (unsigned)(((cw & 1) * 64 + (cw >> 1) * 32 + (lane >> 3)) * 512 + nq * 128 + ((lane & 7) ^ ((lane >> 3) & 2)) * 16);
    d.boff[0] = b0; d.boff[1] = (b0 ^ 64u) + 3072; d.boff[2] = b0 + 6144; d.boff[3] = (b0 ^ 64u) + 9216;
#pragma unroll
    for (int k = 0; k < 4; ++k) PIN(d.boff[k]);
}
template <int P, int N> __device__ __forceinline__ void rd1(Deep& c) {
    constexpr int tab[12] = {0, 4, 5, 6, 7, 1, 2, 3, 8, 9, 10, 11}, k = tab[N < 12 ? N : 0];
    if constexpr (N >= 12) { }
    else if constexpr (k < 4) c.fa[P & 1][k] = LDS(const bf16x8, c.ra[k] + (P & 3) * SLOT);
    else if constexpr (k & 1) c.hi[P & 1][(k - 4) >> 1] = tr_read(c.rb[(k - 4) >> 1] + (P & 3) * SLOT + 512);
    else c.lo[P & 1][(k - 4) >> 1] = tr_read(c.rb[(k - 4) >> 1] + (P & 3) * SLOT);
    SB
}
template <int P, int M, int K> __device__ __forceinline__ void mk(Deep& c) {
    if constexpr (P >= 0) mfa(c.ac[M][K], CAT8(c.lo[P & 1][K], c.hi[P & 1][K]), c.fa[P & 1][M]);
    SB
}
#define DEF11 0
template <int P, int N> __device__ __forceinline__ void m1(Deep& c) { if constexpr (!(DEF11 && P == 11 && ((N >> 1) & 3) == 0)) mk<P, (N >> 1) & 3, (N >> 3) * 2 + (N & 1)>(c); }
template <int P, int M> __device__ __forceinline__ void m4(Deep& c) { mk<P, M, 0>(c); mk<P, M, 1>(c); mk<P, M, 2>(c); mk<P, M, 3>(c); }
#define E9 2
#define T9 0
#define TOP10 4
constexpr int dyoung(int S) { return S < 7 ? 8 + (S == 1 || S == 2) : S < 9 ? 4 : E9; }
template <int U0, int I> __device__ __forceinline__ void dq(Deep& c) {
    constexpr int U = U0 + (I >> 2);
    if constexpr (U < NP) {
        if constexpr ((I & 3) == 0) { prepB<U>(c); asm volatile("s_nop 0"); }
        issueB<I & 3>(c);
    } else if constexpr (U == 15) {
        if constexpr ((I & 1) == 0) { set_m0<FOFF[2] + (I & 2) * 4096>(c.ldsh); asm volatile("s_nop 0"); }
        dma16sco<(I & 1) * 1024>(c.lane16, c.wsh + (I & 2) * 4096);
    } else {
        if constexpr ((I & 3) == 0) { set_m0<FOFF[U - NP]>(c.ldsb); asm volatile("s_nop 0"); }
        dma16sco<(I & 3) * 1024>(c.lane16, c.wsf + (U - NP) * 16384);
    }
    SB
}
template <int S, int I0, int I1> __device__ __forceinline__ void dissue(Deep& c) {
    if constexpr (I0 < I1 && I0 < 4 && S < 7) { dq<S + 3, I0>(c); dissue<S, I0 + 1, I1>(c); }
}
constexpr int cum(unsigned long long t, int n) { int k = 0; for (int i = 0; i < n; ++i) k += (t >> (4 * i)) & 15; return k; }
template <int U0, int I0, int I1> __device__ __forceinline__ void dqs(Deep& c) { if constexpr (I0 < I1) { dq<U0, I0>(c); dqs<U0, I0 + 1, I1>(c); } }
template <int P, int N0, int N1> __device__ __forceinline__ void rdn(Deep& c) { if constexpr (N0 < N1) { rd1<P, N0>(c); rdn<P, N0 + 1, N1>(c); } }
__device__ __forceinline__ unsigned tag_addr(int lane, int unit, int nq, int st, int xcd) {
    const unsigned l4 = (unsigned)lane * 4u;
    const unsigned a0 = (unsigned)(WS_FLAG + unit * 4) + l4 * 32u;
    const unsigned a1 = (((unsigned)(nq * 256) + l4 * 64u) & 768u) + (unsigned)(st * 4);
    const unsigned a2 = (unsigned)(WS_FLAG + xcd * 128 - 64) + l4;
    const unsigned lo = l4 < 32u ? a0 : a1, hi = l4 < 168u ? a2 : (unsigned)(st * 4);
    return l4 < 64u ? lo : hi;
}
__device__ __forceinline__ void publish(Deep& c, unsigned sv) {
    asm volatile("s_cmp_eq_u64 %3, 0\n\ts_cbranch_scc1 1f\n\ts_mov_b64 exec, %3\n\tglobal_store_dword %0, %1, %2 sc1\n\ts_mov_b64 exec, -1\n1:"
                 :: "v"(c.tad), "v"(sv), "s"(c.ws), "s"(c.pmask) : "memory", "scc");
}
__device__ __forceinline__ void produce(Deep& c) {
    if (c.m1 == 0u) {
        const int unit = c.unit;
        const unsigned p8 = (c.lane16 << 4) & 0x300u, e2 = (c.lane16 >> 5) & 0x1eu;
        const unsigned rb = (unsigned)(OFF_RAW + c.c * 32) + e2;
        const unsigned dm = p8 == (c.lane16 & 0x300u) ? ~0u : 0u;
        const int m = unit < 24 ? 2 * (unit & 6) + (unit & 1) + ((unit >> 2) & 2) : 4 * (unit & 1) + 2;
        unsigned fo = (unsigned)(WS_FRAG + c.c * 65536 + (unit >> 4) * 1024 + m * 64) + (((e2 & 12u) << 1) | ((e2 >> 2) & 4u) | (e2 & 2u)) * 2048u + ((c.lane16 & 0x30u) ^ (unsigned)((m & 4) << 3));
        if (unit >= 24) fo += (c.lane16 << 1) & 0x200u;
#pragma unroll
        for (int pq = 0; pq < 2; ++pq) {
            u32x4 fr;
#pragma unroll
            for (int d = 0; d < 4; ++d) {
                const unsigned rp = rb + (d < 3 ? p8 : 0u) + (unsigned)(pq * (NF * 128) + 1024 * d);
                fr[d] = LDS(const uint16_t, rp) | ((unsigned)LDS(const uint16_t, rp + 128) << 16);
            }
            fr[3] &= dm;
            st16sc(fo + pq * 2048u, c.ws, fr);
        }
        if (unit >= 24) { st16sc(fo ^ 512u, c.ws, u32x4{0u, 0u, 0u, 0u}); st16sc((fo ^ 512u) + 2048u, c.ws, u32x4{0u, 0u, 0u, 0u}); }
    }
    SB
}
__device__ __forceinline__ void check_flags(Deep& c) {
    if (c.m3 == 0u) {
        const unsigned l4 = c.lane16 >> 2;
        const unsigned long long open = __builtin_amdgcn_ballot_w64(c.tag == c.pl) & (c.nq ? 0x7ffffff0000ull : 0x3ffffff0000ull);
        if (__builtin_expect(open != 0ull, 0)) {
#pragma unroll 1
            for (int it = 0; it < (1 << 22); ++it) {
                dma4sc(c.tad, c.ws, OFF_TAG + 256);
                asm volatile("s_waitcnt vmcnt(0)" ::: "memory");
                if ((__builtin_amdgcn_ballot_w64(LDSV(OFF_TAG + 256 + l4) == c.tag) & open) == 0ull) break;
                __builtin_amdgcn_s_sleep(2);
            }
        }
    }
    SB
}
__device__ __forceinline__ void fetch_status(Deep& c) {
    if (c.m4 == 0u) {
        if (c.nq) dma16sc(c.lane16, c.ws, OFF_NB + 1024);
        else dma4sc(c.tad, c.ws, OFF_TAG + 512);
    }
    SB
}
constexpr int x3(int P) { return (P == P_FLAG || P == P_FLAG + 1) + (P == P_POLL || P == P_POLL + 1); }
#define DTOP 2
#define DTOPB 4
#define A8 12
#define A9 12
#define A10 16
#define T8 0x842100
#define T10 0xaa
constexpr int adv(int P) { return P == 8 ? A8 : P == 9 ? A9 : P == 10 ? A10 : 0; }
constexpr int bits(unsigned t, int n) { return __builtin_popcount(n < 32 ? t & ((1u << n) - 1u) : t); }
template <int P, int N> __device__ __forceinline__ void drun(Deep& c) {
    constexpr int old = 16 - adv(P - 1), tot = old + adv(P), U0 = P == 8 ? 10 : 11, E = P == 8 ? 4 : P == 10 ? 8 : E9, I0 = P == 10 ? TOP10 : 0, per = old >= 12 ? 1 : old >= 6 ? 2 : old >= 4 ? 3 : 12;
    constexpr unsigned T = P == 8 ? T8 : P == 10 ? T10 : T9;
    if constexpr (P == 10 && N == 0) { if constexpr (E9 & 3) { prepB<11>(c); asm volatile("s_nop 0"); } dqs<U0, E9, TOP10>(c); }
    dqs<U0, I0 + bits(T, N), I0 + bits(T, N + 1)>(c);
    if constexpr (N == 0 && per == 12) rdn<P, 0, 12>(c);
    if constexpr (N < old) m1<P - 1, adv(P - 1) + N>(c); else m1<P, N - old>(c);
    if constexpr (per < 12) rdn<P, N * per, (N * per + per < 12 ? N * per + per : 12)>(c);
    if constexpr (N == 5) { TX(0) }
    if constexpr (N == 11) { TX(1) }
    if constexpr (N == 17) { TX(2) }
    if constexpr (N + 1 < tot) drun<P, N + 1>(c); else { dqs<U0, I0 + bits(T, tot), E>(c); TX(3) }
}
template <int P, int N> __device__ __forceinline__ void mrest(Deep& c) { if constexpr (N < 16) { m1<P, N>(c); mrest<P, N + 1>(c); } }
#define T11 0x1249
#define NE 6
#define TOP11 1
#define E11 0
#define R12 6
constexpr int dtop(int P) { return (P & 3) == 2 ? DTOPB : DTOP; }
template <int P, int N> __device__ __forceinline__ void dbody(Deep& c) {
    constexpr int top = dtop(P);
    m1<P - 1, N>(c);
    rd1<P, N>(c);
    if constexpr (N >= 12) dissue<P, top + N - 12, (N == 15 ? 16 : top + N - 11)>(c);
    if constexpr (N < 15) dbody<P, N + 1>(c);
}
template <int N> __device__ __forceinline__ void dlast(Deep& c) {
    dqs<14, TOP11 + bits(T11, N), TOP11 + bits(T11, N + 1)>(c);
    m1<11, N>(c);
    if constexpr (N == R12) { WAITCNT(TOP11 + bits(T11, N + 1), 15); SB }
    if constexpr (N >= R12 && N < R12 + 8) rd1<12, (N - R12 < 4 ? N - R12 + 1 : N - R12 + 4)>(c);
    if constexpr (N < 15) dlast<N + 1>(c); else dqs<14, TOP11 + bits(T11, 16), NE>(c);
}
template <int P> struct DStep {
    static __device__ __forceinline__ void run(Deep& c) {
        TSP(P)
        SB
        if constexpr (P == P_FLAG) publish(c, c.tag + 1u);
        if constexpr (P == 1) { dma4((unsigned)(c.nq * 128) + ((c.lane16 >> 2) & 124u), c.wd2, OFF_WD2); SB }
        if constexpr (P == P_CHK) check_flags(c);
        if constexpr (P == P_STAT) fetch_status(c);
        if constexpr (P < 8) {
            dissue<P, 0, dtop(P)>(c);
            dbody<P, 0>(c);
        } else if constexpr (P <= 10) {
            drun<P, 0>(c);
            if constexpr (P == 10 && E11) { WAITCNT(4, 15); SB rdn<11, 1, 5>(c); rdn<11, 8, 12>(c); }
        } else {
            if constexpr (PU == 11) __builtin_amdgcn_s_setprio(2);
            rd1<11, 0>(c); rdn<11, 5, 8>(c);
            dqs<14, 0, TOP11>(c);
            if constexpr (!E11) { WAITCNT(4 + TOP11, 15); SB rdn<11, 1, 5>(c); rdn<11, 8, 12>(c); }
            mrest<10, A10>(c);
            rd1<12, 0>(c); rdn<12, 5, 8>(c);
            TS(c.ts[12])
            if constexpr (PU == 12) __builtin_amdgcn_s_setprio(2);
            dlast<0>(c);
        }
        if constexpr (P <= 10) {
            if constexpr (P == 4) {
                c.w2[0] = LDS(const u32x4, c.w2[0][0]); c.w2[1] = LDS(const u32x4, c.w2[1][0]);
            }
            if constexpr (P == P_POLL) { if (c.m3 == 0u) dma4sc(c.tad, c.ws, OFF_TAG + 256); }
            SB
            if constexpr (P + 1 < P_FLAG && P < 2) WAITX(dyoung(P), 2, c.m1)
            else if constexpr (P == P_STAT) WAITX(dyoung(P), 1, c.m4)
            else if constexpr (x3(P) > 0) WAITX(dyoung(P), x3(P), c.m3)
            else if constexpr (P == 10) WAITCNT(63, 0);
            else WAITCNT(dyoung(P), 0);
            TX(4)
            if constexpr (P == P_CHK - 1) { SB c.pl = LDSV(OFF_TAG + 256 + (c.lane16 >> 2)); SB }
            BARRIER;
            DStep<P + 1>::run(c);
        }
    }
};

struct Cross : Com {
    const char* tb[4]; const char* wsf;
    unsigned ga[40];
    unsigned re[2], rx[2], rx0[2], six, wf[2], pl, ph, fl;
    unsigned long long xm[2];
    f32x4 dg;
    s16x4 wa[2];
    bf16x8 wb[2], wr[2];
    u32x4 fe[2][2][2];
    f32x4 ew[NF], zs[4];
    u32x4 X[NP], Y[12], bp[4];
    float k0, k1, k2;
    unsigned am;
    u32x4 on[4], s1, zq;
};
template <int I> __device__ __forceinline__ void issueA(Cross& c, unsigned off) { dma16o<I * 1024>(off, c.tb[I]); }
template <int K, int S, int N> __device__ __forceinline__ void sw1(Cross& c) {
    constexpr int a = 1 + 4 * K + (S == 1 ? 2 : (S == 3 ? 1 : 0)), b = 1 + 4 * K + (S == 0 ? 1 : (S == 2 ? 2 : 3)), Q = N >> 2, R = N & 3;
    float x = c.ew[2 * a + Q][R], y = c.ew[2 * b + Q][R];
    if (S >= 2) asm volatile("v_permlane32_swap_b32 %0, %1" : "+v"(x), "+v"(y)); else asm volatile("v_permlane16_swap_b32 %0, %1" : "+v"(x), "+v"(y));
    c.ew[2 * a + Q][R] = x; c.ew[2 * b + Q][R] = y;
}
template <int K, int S, int N0 = 0, int N1 = 8> __device__ __forceinline__ void swn(Cross& c) { if constexpr (N0 < N1) { sw1<K, S, N0>(c); swn<K, S, N0 + 1, N1>(c); } }
#define S8 24
#define S9 24
template <int N0, int N1> __device__ __forceinline__ void swl(Cross& c) { if constexpr (N0 < N1 && N0 < 32) { sw1<2, (N0 >> 3), (N0 & 7)>(c); swl<N0 + 1, N1>(c); } }
constexpr int sw0(int P) { return P <= 3 ? 0 : P <= 5 ? 8 : P == 6 ? 20 : P == 7 ? 32 : P == 8 ? 40 : P == 9 ? 40 + S8 : P == 10 ? 40 + S9 : 64; }
template <int P, int N0, int N1> __device__ __forceinline__ void tsw(Cross& c) {
    if constexpr (N0 < N1 && sw0(P) + N0 < sw0(P + 1)) { sw1<((sw0(P) + N0) >> 5), ((sw0(P) + N0) >> 3) & 3, (sw0(P) + N0) & 7>(c); tsw<P, N0 + 1, N1>(c); }
}
constexpr int fbuf(int P) { return P & 1; }
template <int P, int N> __device__ __forceinline__ void cmm(Cross& c) {
    if constexpr (P >= 0) {
        constexpr int B = fbuf(P);
        if constexpr (N < 2)
            asm volatile("v_smfmac_f32_16x16x64_bf16 %0, %1, %2, %3" : "+v"(c.zs[P ? (P - 1) >> 2 : 3])
                         : "v"(c.on[P ? (P - 1) & 3 : 0]), "v"(__builtin_shufflevector(c.fe[B][N][0], c.fe[B][N][1], 0, 1, 2, 3, 4, 5, 6, 7)), "v"(c.six));
        else if constexpr (N < 4) c.ew[2 * P + (N & 1)] = mf0(P ? c.wb[0] : c.wr[0], c.fe[B][0][N & 1]);
        else mfa(c.ew[2 * P + (N & 1)], P ? c.wb[1] : c.wr[1], c.fe[B][1][N & 1]);
    }
    SB
}
template <int P, int N> __device__ __forceinline__ void rdE(Cross& c) {
    c.fe[fbuf(P)][N >> 1][N & 1] = LDS(const u32x4, c.re[N >> 1] + (P & 3) * SLOT + (N & 1) * 2048);
    SB
}
template <int K> __device__ __forceinline__ void zpack(Cross& c) {
    float a = c.zs[K][0], b = c.zs[K][2];
    unsigned r;
    asm volatile("v_add_f32 %1, %1, %3\n\tv_add_f32 %2, %2, %4\n\tv_cvt_pk_bf16_f32 %0, %1, %2" : "=v"(r), "+v"(a), "+v"(b) : "v"(c.zs[K][1]), "v"(c.zs[K][3]));
    c.zq[K] = r;
    SB
}
template <int K> __device__ __forceinline__ void zclear(Cross& c) { c.zs[K] = f32x4{0.f, 0.f, 0.f, 0.f}; PIN(c.zs[K]); SB }
#define NJP 3
template <int P> __device__ __forceinline__ void rdX(Cross& c) {
    constexpr int k = 1 + ((P - 1) & ~3), a = ((P - 1) & 3) * 32, b = (P & 3) * 32;
    constexpr bool jp = (P - 1) / 4 < NJP;
#define RDX "s_mov_b64 exec, %6\n\tds_read2st64_b64 %0, %4 offset0:%7 offset1:%8\n\tds_read2st64_b64 %1, %4 offset0:%9 offset1:%10\n\t" \
            "ds_read2st64_b64 %2, %5 offset0:%7 offset1:%8\n\tds_read2st64_b64 %3, %5 offset0:%9 offset1:%10\n\ts_mov_b64 exec, -1"
#define RDY "s_mov_b64 exec, %5\n\tds_read_b128 %0, %4 offset:%6\n\tds_read_b128 %1, %4 offset:%7\n\tds_read_b128 %2, %4 offset:%8\n\tds_read_b128 %3, %4 offset:%9\n\ts_mov_b64 exec, -1"
    if constexpr (P == 0) { const u32x2 lo = LDS(const u32x2, c.rx0[0]), hi = LDS(const u32x2, c.rx0[1]); c.X[0] = u32x4{lo[0], lo[1], hi[0], hi[1]}; }
    else if constexpr ((P & 3) == 2 && jp)
        asm volatile(RDY : "=&v"(c.X[k]), "=&v"(c.X[k + 1]), "=&v"(c.X[k + 2]), "=&v"(c.X[k + 3]) : "v"(c.rx[0]), "s"(c.xm[0]), "n"(a * 512), "n"(a * 512 + 2048), "n"(b * 512), "n"(b * 512 + 2048) : "memory");
    else if constexpr ((P & 3) == 0 && jp)
        asm volatile(RDY : "+v"(c.X[k]), "+v"(c.X[k + 1]), "+v"(c.X[k + 2]), "+v"(c.X[k + 3]) : "v"(c.rx[0]), "s"(c.xm[1]), "n"(a * 512), "n"(a * 512 + 2048), "n"(b * 512), "n"(b * 512 + 2048) : "memory");
    else if constexpr ((P & 3) == 2)
        asm volatile(RDX : "=&v"(c.X[k]), "=&v"(c.X[k + 1]), "=&v"(c.X[k + 2]), "=&v"(c.X[k + 3]) : "v"(c.rx[0]), "v"(c.rx[1]), "s"(c.xm[0]), "n"(a), "n"(b), "n"(a + 4), "n"(b + 4) : "memory");
    else if constexpr ((P & 3) == 0)
        asm volatile(RDX : "+v"(c.X[k]), "+v"(c.X[k + 1]), "+v"(c.X[k + 2]), "+v"(c.X[k + 3]) : "v"(c.rx[0]), "v"(c.rx[1]), "s"(c.xm[1]), "n"(a), "n"(b), "n"(a + 4), "n"(b + 4) : "memory");
    SB
}
template <int K, int N0, int N1> __device__ __forceinline__ void xpair(Cross& c) {
    if constexpr (N0 < N1 && K < NJP) {
        constexpr int m = N0 >> 2, d = N0 & 3, s = 1 + 4 * K + (d & 2), e = (m & 2) + (d & 1);
        unsigned r;
        asm volatile("v_perm_b32 %0, %1, %2, %3" : "=v"(r) : "v"(c.X[s + 1][e]), "v"(c.X[s][e]), "s"((m & 1) ? c.ph : c.pl));
        c.Y[4 * K + m][d] = r;
        xpair<K, N0 + 1, N1>(c);
    }
}
#define C11 1
#define G8 0x5421
#define G8P(n) { if constexpr ((G8 & 15) == (n)) GQ(10, 0) if constexpr (((G8 >> 4) & 15) == (n)) GQ(10, 1) if constexpr (((G8 >> 8) & 15) == (n)) GQ(10, 2) if constexpr (((G8 >> 12) & 15) == (n)) GQ(10, 3) }
#define FPOS 0x01504241
#define FFL 4
#define BP 1
#define FI1(k, n) if (((FPOS >> (8 * (k))) & 255) == (n)) { if (!(k)) { set_m0<FOFF[0]>(c.ldsb); asm volatile("s_nop 0"); } dma16sco<(k) * 1024>(c.lane16, c.wsf); SB }
#define FI(n) FI1(0, n) FI1(1, n) FI1(2, n) FI1(3, n) if ((n) == BP) { BARRIER; SB } if ((n) == FFL) { WAITCNT(0, 15); SB LDS(unsigned, c.fl) = c.fl; SB }
#define GQ(Q, J) { if constexpr ((J) == 0) { set_m0<((Q) & 3) * SLOT>(c.ldsb); asm volatile("s_nop 0"); } issueA<J>(c, c.ga[4 * (Q) - 12 + (J)]); SB }
template <int P> struct CStep {
    static __device__ __forceinline__ void run(Cross& c) {
        TSP(P)
        if constexpr (P == 7) { STAMP(6) }
        SB
        if constexpr (P <= 8) {
            cmm<P - 1, 2>(c); rdE<P, 0>(c);
            cmm<P - 1, 3>(c); rdE<P, 1>(c);
            cmm<P - 1, 4>(c); rdE<P, 2>(c);
            cmm<P - 1, 5>(c); rdE<P, 3>(c);
            cmm<P - 1, 0>(c);
            if constexpr (P < 7) set_m0<((P + 3) & 3) * SLOT>(c.ldsb);
            cmm<P - 1, 1>(c);
            rdX<P>(c);
        }
        constexpr bool xw = P && !(P & 1);
#define GA(J) if constexpr (P < 7) issueA<J>(c, c.ga[4 * P + (J)]); SB
        if constexpr (P == 4 || P == 7) zclear<(P + 1) / 4>(c);
        if constexpr (P == 7) {
#pragma unroll
            for (int r = 0; r < 4; ++r) { c.bp[r][3] = cvt_o(c.ew[0][r], c.ew[1][r]); c.bp[r][0] = cvt_o(c.ew[2][r], c.ew[3][r]); }
            c.k0 = -2.0f * 0.7978845608028654f * 1.4426950408889634f / 64.0f; c.k1 = c.k0 * (0.044715f / 4096.0f); c.k2 = -64.0f / 1.4426950408889634f;
            c.am = (c.wf[0] & 16u) ? ~0u : 0u;
            c.fl = (c.lane16 >> 2) + (unsigned)(OFF_FL + 2048 + c.c * 256);
            PIN(c.k0); PIN(c.k1); PIN(c.k2); PIN(c.am); PIN(c.fl);
            SB
        }
        if constexpr (P == 2 || P == 6) zpack<(P == 2 ? 3 : 0)>(c);
        if constexpr (P == 8) {
            TX(0) G8P(0)
            cmm<8, 2>(c); cmm<8, 3>(c); cmm<8, 4>(c); cmm<8, 5>(c); cmm<8, 0>(c); cmm<8, 1>(c);
            G8P(1) TX(1)
            WAITCNT(63, 0); SB
            xpair<1, 0, 8>(c); SB G8P(2) xpair<1, 8, 16>(c); SB G8P(3)
            zpack<1>(c); TX(2)
            tsw<8, 0, S8 / 2>(c); SB G8P(4) tsw<8, S8 / 2, S8>(c); SB G8P(5)
        } else if constexpr (P == 9) {
            GQ(11, 0) rdE<9, 0>(c); rdE<9, 1>(c); rdE<9, 2>(c); rdE<9, 3>(c); tsw<9, 0, 2>(c); SB GQ(11, 1) tsw<9, 2, 6>(c); SB GQ(11, 2) TX(0)
            cmm<9, 2>(c); cmm<9, 3>(c); cmm<9, 4>(c); GQ(11, 3) cmm<9, 5>(c); cmm<9, 0>(c); cmm<9, 1>(c); TX(1)
            GQ(12, 0) tsw<9, 6, 10>(c); SB GQ(12, 1) tsw<9, 10, 14>(c); SB GQ(12, 2) TX(2) tsw<9, 14, 24>(c); SB GQ(12, 3)
        } else if constexpr (P == 10) {
            rdE<10, 0>(c); rdE<10, 1>(c); rdE<10, 2>(c); rdE<10, 3>(c);
            tsw<10, 0, 24>(c); SB
#pragma unroll
            for (int r = 0; r < 4; ++r) c.bp[r][1] = cvt_o(c.ew[10][r], c.ew[11][r]);
            SB
            cmm<10, 2>(c); cmm<10, 3>(c); cmm<10, 4>(c); cmm<10, 5>(c); rdX<10>(c); cmm<10, 0>(c); cmm<10, 1>(c); TX(0)
            WAITCNT(4, 15); asm volatile("" ::: "memory"); SB TX(1)
            rdE<11, 0>(c); rdE<11, 1>(c); rdE<11, 2>(c); rdE<11, 3>(c);
            swn<2, 0, 0, 8>(c); SB
            WAITCNT(63, 0); SB TX(2)
            if constexpr (C11) { cmm<11, 0>(c); cmm<11, 1>(c); cmm<11, 2>(c); cmm<11, 3>(c); cmm<11, 4>(c); cmm<11, 5>(c); }
        }
        else if constexpr (xw) { tsw<P, 0, 4>(c); WAITCNT(63, 0); GA(0) tsw<P, 4, 6>(c); GA(1) tsw<P, 6, 8>(c); GA(2) tsw<P, 8, 10>(c); GA(3) tsw<P, 10, 12>(c); }
        else { GA(0) GA(1) tsw<P, 0, 4>(c); GA(2) tsw<P, 4, 8>(c); GA(3) tsw<P, 8, 12>(c); }
        if constexpr (P == 4) { xpair<0, 0, 16>(c); SB }
        if constexpr (P == 3) {
            const unsigned r6 = c.lane16 * 4u + (unsigned)c.rho * 256u;
            const unsigned am = (c.lane16 >> 8) + (unsigned)c.rho;
            c.wf[0] = (r6 & 0x3c0u) | (((am << 4) ^ ((r6 >> 3) & 0x20u)) & 0x30u) | SLOT;
            c.wf[1] = c.wf[0] + OFF_B;
            const unsigned wap = OFF_SW2 + (c.lane16 & 0x3c0u) + ((c.lane16 >> 1) & 0x18u);
            c.wa[0] = tr_read(wap); c.wa[1] = tr_read(wap + 32);
            const unsigned r2 = (c.lane16 >> 3) & 30u, s1 = OFF_SW1 + min(r2, 24u), sg = s1 + (am & 3u) * 52u;
#pragma unroll
            for (int k = 0; k < 4; ++k) {
                const unsigned a = (k < 3 ? sg : s1) + k * 208;
                const unsigned v = LDS(const uint16_t, a) | ((unsigned)LDS(const uint16_t, a + 26) << 16);
                c.s1[k] = r2 < 26u ? v : 0u;
            }
            PIN(c.s1);
            PIN(c.wf[0]); PIN(c.wf[1]); PIN(c.wa[0]); PIN(c.wa[1]);
        }
        SB
        TX(3)
        WAITCNT(P == 7 || P == 8 ? 4 : P < 10 ? 8 : 0, 0);
        TX(4)
        if constexpr (P == 10) { asm volatile("" ::: "memory"); SB rdE<12, 0>(c); rdE<12, 1>(c); rdE<12, 2>(c); rdE<12, 3>(c); FI(0x41) }
        else { BARRIER; CStep<P + 1>::run(c); }
    }
};

#define PFC 145
#define WPRE 5
__global__ __launch_bounds__(512) void k_main(const int* __restrict__ idx, const uint16_t* __restrict__ table,
        const uint16_t* __restrict__ w_bi, const uint16_t* __restrict__ w_deep1, const uint16_t* __restrict__ w_cross,
        unsigned* __restrict__ ws32, const uint16_t* __restrict__ se_w1, const uint16_t* __restrict__ se_w2,
        const uint16_t* __restrict__ w_deep2_, __hip_bfloat16* __restrict__ out_, unsigned long long* __restrict__ stamps) {
    SB
    const int tid = threadIdx.x, lane = tid & 63;
    const int w = __builtin_amdgcn_readfirstlane(tid >> 6);
    const int bid = blockIdx.x;
    const int xcd = bid & 7, slot = bid >> 3;
    const int st = xcd * 8 + (slot >> 2), nq = slot & 3;
    const int s0 = st * 64;
    const int rho = (st + nq) & 3, cw = w & 3;
    const int l15 = lane & 15, g = lane >> 4;
    LDS(u32x2, OFF_FL + tid * 8) = u32x2{0u, 0u};
    unsigned long long pc;
    {
        const unsigned ln = min((unsigned)(4 + w * 4 + (lane & 3) + ((lane >> 2) & 1) * (32 + nq * 32)), (unsigned)PFC) * 128u;
        asm volatile("s_getpc_b64 %0\n\ts_mov_b32 m0, %2\n\ts_mov_b64 exec, 0xff\n\tglobal_load_lds_dword %1, %0\n\ts_mov_b64 exec, -1"
                     : "=&s"(pc) : "v"(ln), "n"(OFF_JNK) : "memory");
    }
    SB
    float gc0 = -2.0f * 0.7978845608028654f * 1.4426950408889634f, gc1 = gc0 * 0.044715f;
#define FIN_ST(c)
#define FIN_SA(c)
#define FINISH(c) STAMP(5) FIN_ST(c) FIN_SA(c) __builtin_amdgcn_endpgm(); __builtin_unreachable();
    unsigned lane16 = (unsigned)lane * 16u;
    int deep = w >> 2;
    Deep d;
    d.ldsb = (unsigned)(cw * 4096);
    unsigned o2 = min(lane16, 624u), im0 = (unsigned)(OFF_IDX + cw * 2048), io = (unsigned)((s0 + 16 * cw) * (NF * 4));
    mkboff(d, lane, cw, nq);
#pragma unroll
    for (int k = 0; k < 4; ++k) { d.sG[k] = (unsigned)(((k + rho) & 3) * (2 * 32768)); PINS(d.sG[k]); }
    d.unit = xcd + 8 * ((st & 7) >> 1);
    d.m1 = (unsigned)((nq ^ 1) | (st & 1) | ((d.unit + 32 - NF) >> 5));
    PIN(lane16); PIN(o2); PINS(im0); PINS(io); PINS(deep); PINS(d.ldsb); PINS(d.m1);
    asm volatile("s_mov_b32 m0, %0" :: "s"(im0) : "memory");
    SB
    asm volatile("" :: "s"(idx), "s"(table), "s"(se_w1), "s"(se_w2), "s"(w_bi), "s"(w_deep1), "s"(w_cross), "s"(ws32));
    asm volatile("s_cmp_lg_u32 %3, 0\n\ts_cbranch_scc1 1f\n\tglobal_load_lds_dwordx4 %0, %2\n\tglobal_load_lds_dwordx4 %1, %2 offset:1024\n1:"
                 :: "v"(lane16), "v"(o2), "s"((const char*)idx + io), "s"(deep) : "memory", "scc");
    SB
    typedef const __attribute__((address_space(4))) unsigned long long* kap;
    kap ka = (kap)__builtin_amdgcn_kernarg_segment_ptr();
    PINS(ka);
    d.wd2 = (const char*)ka[8];
    const gp out = (gp)ka[9];
    unsigned long long w1p;
    asm volatile("s_mov_b64 %0, %1" : "=s"(w1p) : "s"(w_deep1));
    d.rw1 = __builtin_amdgcn_make_buffer_rsrc((void*)w1p, (short)0, NF * 32768, 0x00020000);
    if (deep) {
        asm volatile("s_mov_b32 m0, %2\n\ts_nop 0\n\tbuffer_load_dword %0, %1, 0 offen lds" :: "v"(min(lane16, 80u) * 8u + (unsigned)((bid * 4 + cw) * 768)), "s"(d.rw1), "n"(OFF_JNK) : "memory");
    }
    prepB<0>(d);
    BARRIER;
    Cross x;
    x.c = cw; x.nq = nq; x.rho = rho; x.st = st; x.lane16 = lane16; x.ws = (char*)ws32; x.ldsb = d.ldsb;
    unsigned srcA, srcB, ib[4];
    u32x2 ia[NP], ic[NP];
    if (deep) {
        issueB<0>(d); issueB<1>(d); issueB<2>(d); issueB<3>(d);
        SB
        dma16((unsigned)((cw & 1) * 4096 + (lane >> 1) * 128 + nq * 32 + (lane & 1) * 16), w_bi, OFF_WBI + (cw & 1) * 1024);
        dma4((unsigned)min(cw * 64 + lane, 168) * 4u, se_w1, OFF_SW1 + cw * 256);
        const int k = (l15 & 1) | ((l15 >> 2) & 2), r = 4 * cw + g;
        dma4((unsigned)((r < 13 ? r : 12) * (NF * 2) + k * 16 + (k == 3 ? 0 : ((l15 + 2 * rho) & 6) * 2)), se_w2, OFF_SW2 + cw * 256);
        SB
        if (__builtin_expect(d.m1 == 0u, 1)) {
            const unsigned r0 = (unsigned)(16 * cw + (lane >> 3)), lo = (lane16 & 0x70u) + (unsigned)(d.unit * 128);
            dma16(min(r0, 2u * NF - 1) * (NF * 128) + lo, w_cross, OFF_RAW + cw * 2048);
            dma16(min(r0 + 8u, 2u * NF - 1) * (NF * 128) + lo, w_cross, OFF_RAW + cw * 2048 + 1024);
            dma4sc((lane16 >> 2) + (unsigned)(cw * 256), ws32, OFF_NB + cw * 256);
        }
        if (cw == 3) dma4sc(tag_addr(lane, d.unit, nq, st, xcd), ws32, OFF_TAG);
        if ((cw ^ 2) + nq == 0 && lane < 32) st16sc(lane16, (char*)ws32 + WS_SUM + st * 512, u32x4{0u, 0u, 0u, 0u});
        SB
        if (nq != 3) { prepB<1>(d); asm volatile("s_nop 0"); issueB<0>(d); issueB<1>(d); issueB<2>(d); issueB<3>(d); }
        SB
    } else {
        Cross& c = x;
        srcA = (unsigned)(((lane & 7) ^ g) * 16); srcB = srcA ^ 64u;
        unsigned ib0 = (unsigned)(OFF_IDX + cw * 2048 + (lane >> 3) * 104);
#pragma unroll
        for (int k = 0; k < 4; ++k) { ib[k] = ib0 + (unsigned)(((k + rho) & 3) * 8); PIN(ib[k]); c.tb[k] = (const char*)table - k * 1024; PINS(c.tb[k]); }
        PIN(srcA); PIN(srcB); PIN(ib0);
        set_m0<0>(c.ldsb);
        SB
        WAITCNT(0, 15);
        asm volatile("" ::: "memory");
        STAMP(1)
        TP(c.tq[0])
        ia[0] = LDS(const u32x2, ib0 + 96); ic[0] = LDS(const u32x2, ib0 + 96 + 832);
#pragma unroll
        for (int q = 1; q < 3; ++q) { ia[q] = LDS(const u32x2, ib[q - 1]); ic[q] = LDS(const u32x2, ib[q - 1] + 832); }
        SB
#define GATHER(q) issueA<0>(c, ia[q][0] * 128u + srcA); issueA<1>(c, ic[q][0] * 128u + srcB); issueA<2>(c, ia[q][1] * 128u + srcA); issueA<3>(c, ic[q][1] * 128u + srcB);
        GATHER(0)
        set_m0<SLOT>(c.ldsb);
        SB
        GATHER(1)
        set_m0<2 * SLOT>(c.ldsb);
        TP(c.tq[1])
    }
    int crs = deep ^ 1;
    PINS(crs); PINS(deep);
    if (crs == 0) {
        Deep& c = d;
        int lane;
        asm volatile("v_mbcnt_lo_u32_b32 %0, -1, 0\n\tv_mbcnt_hi_u32_b32 %0, -1, %0" : "=v"(lane));
        const int l15 = lane & 15, g = lane >> 4, tid = w * 64 + lane;
        c.c = cw; c.nq = nq; c.rho = rho; c.st = st; c.lane16 = (unsigned)lane * 16u; c.ws = (char*)ws32;
        c.m3 = (unsigned)(cw ^ 3); c.m4 = c.m3 | min(c.m1, (unsigned)nq);
        c.tad = tag_addr(lane, c.unit, nq, st, xcd);
        PIN(c.tad);
        SB
        mkboff(d, lane, cw, nq);
        {
            const unsigned a0 = (unsigned)(cw * 4096 + (cw & 1) * 2048 + l15 * 128 + ((4 * (cw >> 1) + g) ^ (l15 >> 1)) * 16);
            const int q = l15 >> 2, p = l15 & 3;
            const unsigned b0 = (unsigned)(OFF_B + cw * 4096 + (8 * g + q) * 128 + ((p >> 1) ^ (q & 2) ^ ((g & 1) << 2)) * 16 + (p & 1) * 8);
#pragma unroll
            for (int k = 0; k < 4; ++k) { c.ra[k] = a0 ^ (unsigned)(k * 4096); c.rb[k] = b0 ^ (unsigned)(k * 32); PIN(c.ra[k]); PIN(c.rb[k]); }
#pragma unroll
            for (int k = 0; k < 16; ++k) { c.ac[k >> 2][k & 3] = f32x4{0.f, 0.f, 0.f, 0.f}; PIN(c.ac[k >> 2][k & 3]); }
        }
        unsigned exw[4], fl = (c.lane16 >> 2) + (unsigned)(OFF_FL + cw * 256), fr = c.lane16 + OFF_FL, s2 = c.lane16 >> 3, s8 = c.lane16 >> 1;
#pragma unroll
        for (int k = 1; k < 4; ++k) { exw[k] = (c.lane16 >> 2) + (unsigned)(OFF_B + (cw ^ k) * 4096); PIN(exw[k]); }
        double big = 0x1.8p52;
        f32x4 va = {0.f, 0.f, 0.f, 0.f};
        float gc2 = 1.0f / TRC;
        c.w2[0][0] = OFF_WD2 + g * 16; c.w2[1][0] = OFF_WD2 + 64 + g * 16; PIN(c.w2[0]); PIN(c.w2[1]);
        const unsigned dl = (unsigned)((l15 >> 2) ^ (2 * (g & 1))), on = (l15 & 1) ? 0x3f800000u : 0x3f80u;
        u32x4 unit = {dl == 0u ? on : 0u, dl == 0u ? on : 0u, dl == 1u ? on : 0u, dl == 1u ? on : 0u};
        unsigned uix = (l15 & 2) ? 0xddddeeeeu : 0xdddd4444u;
        u32x2 pr[4][4];
#pragma unroll
        for (int k = 0; k < 4; ++k) { pr[0][k] = u32x2{0u, 0u}; PIN(pr[0][k]); }
        PIN(uix);
        PIN(fl); PIN(fr); PIN(s2); PIN(s8); PIN(big); PIN(gc0); PIN(gc1); PIN(gc2); PIN(va); PIN(unit);
        c.wsf = c.ws + WS_FRAG + nq * 65536 + cw * 4096;
        c.wsh = c.ws + WS_FRAG + 32768 + nq * 65536 + cw * 2048; c.ldsh = (unsigned)(cw * 2048);
        PINS(c.wsh); PINS(c.ldsh);
        c.pmask = cw != 3 ? 0ull : (c.m1 == 0u ? 0x1ffull : 0x100ull);
        unsigned long long wmask = (cw ^ 3) + nq ? 0ull : 0xe00ull;
        unsigned m5 = c.m3 | c.m1;
        PINS(wmask); PINS(m5);
        PINS(c.pmask); PINS(c.m3); PINS(c.m4); PINS(c.wsf);
        if (nq == 3) { prepB<1>(c); asm volatile("s_nop 0"); issueB<0>(c); issueB<1>(c); issueB<2>(c); issueB<3>(c); }
        TP(c.tq[0])
        WAITCNT(4, 0); BARRIER;
        TP(c.tq[1])
        prepB<2>(c); asm volatile("s_nop 0"); issueB<0>(c); issueB<1>(c); issueB<2>(c); issueB<3>(c);
        TP(c.tq[2])
        produce(c);
        c.tag = LDSV(OFF_TAG + (c.lane16 >> 2));
        unsigned tag2 = c.tag + 2u;
        PIN(c.tag); PIN(tag2);
        TP(c.tq[3])
        BARRIER;
        TP(c.tq[4])
        gp sums = (gp)c.ws + (unsigned)(WS_SUM + (s0 + 16 * cw) * 8), outp = out + (unsigned)((s0 + 16 * cw) * 2);
        PINS(sums); PINS(outp);
        DStep<0>::run(c);
        TS(c.ts[13])
        u32x2 px[4][4];
#define EX2(e, k) { px[e][k][0] = cvt_o(c.ac[e][k][0], c.ac[e][k][1]); px[e][k][1] = cvt_o(c.ac[e][k][2], c.ac[e][k][3]); SB }
#define EXO(e, k) (((e) - 1) * 1024 + ((k) & 1) * 512)
#define WT1(e, k, d) { asm volatile("ds_write_addtid_b32 %0 offset:%1" :: "v"(px[e][k][d]), "n"(((k) < 2 ? 62464 : 32768) + EXO(e, k) + (d) * 256) : "memory"); SB }
#define WT(e, k) { asm volatile("ds_write_addtid_b32 %0 offset:%2\n\tds_write_addtid_b32 %1 offset:%3" :: "v"(px[e][k][0]), "v"(px[e][k][1]), \
            "n"(((k) < 2 ? 62464 : 32768) + EXO(e, k)), "n"(((k) < 2 ? 62464 : 32768) + EXO(e, k) + 256) : "memory"); SB }
#define W6(k) WT(3, k) WT(2, k) WT(1, k)
#define RD(h) { if (!(h)) f = LDSV(fr + 1024); asm volatile("" ::: "memory"); SB \
            _Pragma("unroll") for (int k = 2 * (h); k < 2 * (h) + 2; ++k) _Pragma("unroll") for (int e = 3; e > 0; --e) _Pragma("unroll") for (int d = 0; d < 2; ++d) \
                pr[e][k][d] = LDS(const unsigned, exw[e] + ((h) ? 0 : 3 * SLOT) + EXO(e, k) + d * 256); SB }
#define CK(h) { if (__builtin_expect(__builtin_amdgcn_ballot_w64(f == 0u) != 0ull, 0)) { \
            _Pragma("unroll 1") for (int it = 0; it < (1 << 20); ++it) { RD(0) RD(1) if (__builtin_amdgcn_ballot_w64(f == 0u) == 0ull) break; } } SB }
#define AD(k) asm volatile("v_smfmac_f32_16x16x64_bf16 %0, %1, %2, %3" : "+v"(c.ac[0][k]) : "v"(unit), \
            "v"(u32x8{pr[0][k][0], pr[0][k][1], pr[1][k][0], pr[1][k][1], pr[2][k][0], pr[2][k][1], pr[3][k][0], pr[3][k][1]}), "v"(uix));
#define G4(m, k, ins) asm volatile(ins : m(y[4 * k]), m(y[4 * k + 1]), m(y[4 * k + 2]), m(y[4 * k + 3]) \
            : "v"(c.ac[0][k][0]), "v"(c.ac[0][k][1]), "v"(c.ac[0][k][2]), "v"(c.ac[0][k][3]), "v"(gc0), "v"(gc1), "v"(gc2));
#define GA_ "v_mul_f32 %0, %4, %4\n\tv_mul_f32 %1, %5, %5\n\tv_mul_f32 %2, %6, %6\n\tv_mul_f32 %3, %7, %7\n\t" \
            "v_fma_f32 %0, %0, %9, %8\n\tv_fma_f32 %1, %1, %9, %8\n\tv_fma_f32 %2, %2, %9, %8\n\tv_fma_f32 %3, %3, %9, %8\n\t"
#define GM_ "v_mul_f32 %0, %0, %4\n\tv_mul_f32 %1, %1, %5\n\tv_mul_f32 %2, %2, %6\n\tv_mul_f32 %3, %3, %7\n\t"
#define GB_ GM_ "v_exp_f32 %0, %0\n\tv_exp_f32 %1, %1\n\tv_exp_f32 %2, %2\n\tv_exp_f32 %3, %3\n\t"
#define GC_ "v_fma_f32 %0, %0, %10, %10\n\tv_fma_f32 %1, %1, %10, %10\n\tv_fma_f32 %2, %2, %10, %10\n\tv_fma_f32 %3, %3, %10, %10\n\t" \
            "v_rcp_f32 %0, %0\n\tv_rcp_f32 %1, %1\n\tv_rcp_f32 %2, %2\n\tv_rcp_f32 %3, %3\n\t" GM_
#define GELU(k) { G4("=&v", k, GA_ GB_ GC_) }
#define GEL1(k, I0) { G4("=&v", k, GA_) I0 G4("+v", k, GB_ GC_) }
#define GEL2(k, I1, I2) { G4("=&v", k, GA_ GB_) I1 G4("+v", k, GC_) }
#define WDOT(q) asm volatile("v_smfmac_f32_16x16x64_bf16 %0, %1, %2, %3 abid:1" : "+v"(va) : "v"(c.w2[q]), \
            "v"(f32x8{y[8 * q], y[8 * q + 1], y[8 * q + 2], y[8 * q + 3], y[8 * q + 4], y[8 * q + 5], y[8 * q + 6], y[8 * q + 7]}), "v"(uix));
#define PROTO { watch = __builtin_amdgcn_ballot_w64(tw == c.tag) & wmask; \
            if (__builtin_expect(m5 == 0u, 0)) { \
                const u32x4 e4 = LDS(const u32x4, fr + (OFF_NB - OFF_FL)), l4 = LDS(const u32x4, fr + (OFF_NB + 1024 - OFF_FL)); \
                watch |= __builtin_amdgcn_ballot_w64(e4[0] == l4[0]) | __builtin_amdgcn_ballot_w64(e4[1] == l4[1]) | __builtin_amdgcn_ballot_w64(e4[2] == l4[2]) | __builtin_amdgcn_ballot_w64(e4[3] == l4[3]); \
            } \
            PINS(watch); } SB
#define FLA { LDS(unsigned, fl + 1024) = fl; SB }
#define FLB { LDS(unsigned, fl + 3072) = fl; SB }
        unsigned f;
        float y[16];
        unsigned long long watch = 0ull;
#define FW 4
#define FLP 1
#define WSP 1
#define PD 3
#define PR(n) { if ((n) == PU) __builtin_amdgcn_s_setprio(2); if ((n) == PD || (n) == PD2) __builtin_amdgcn_s_setprio(0); if ((n) == PU2) __builtin_amdgcn_s_setprio(2); SB }
#define PU2 -1
#define PD2 -1
#define FH(n) if ((n) == FW) { WAITCNT(0, 15); SB set_m0<OFF_B + 3 * SLOT - 62464>(c.ldsb); LDS(unsigned, fl + 3072) = fl; SB }
#define FD(k) { set_m0<FOFF[2 + (k)]>(c.ldsb); asm volatile("s_nop 0"); dma16sco<0>(c.lane16, c.wsf + 32768 + (k) * 16384); dma16sco<1024>(c.lane16, c.wsf + 32768 + (k) * 16384); \
            dma16sco<2048>(c.lane16, c.wsf + 32768 + (k) * 16384); dma16sco<3072>(c.lane16, c.wsf + 32768 + (k) * 16384); SB }
#define FS(n) if ((n) == FLP) { WAITCNT(0, 15); SB LDS(unsigned, fl) = fl; SB publish(c, tag2); SB TE(c.tq[3]) }
#define PP 4
#define PQ(n) if ((n) == PP) { PROTO }
        PR(0) FH(0)
        TE(c.tq[0])
        mk<12, 3, 0>(c); mk<12, 2, 0>(c); mk<12, 1, 0>(c); FH(1) mk<12, 3, 1>(c); mk<12, 2, 1>(c); mk<12, 1, 1>(c); FH(2)
        mk<12, 3, 2>(c); mk<12, 2, 2>(c); mk<12, 1, 2>(c); FH(3) EX2(3, 0) mk<12, 3, 3>(c); EX2(2, 0) mk<12, 2, 3>(c); EX2(1, 0) mk<12, 1, 3>(c); FH(4) PR(1)
#define OWN 1
#define OW(k) if (OWN) mk<12, 0, k>(c);
        WT(3, 0) EX2(3, 1) OW(0) WT(2, 0) EX2(2, 1) WT(1, 0) EX2(1, 1) OW(1) WT(3, 1) EX2(3, 2) WT(2, 1) EX2(2, 2) OW(2) WT(1, 1) set_m0<OFF_B - 32768>(c.ldsb); EX2(1, 2) WT(3, 2) EX2(3, 3) OW(3) WT(2, 2) EX2(2, 3) WT(1, 2) EX2(1, 3)
        W6(3) FLA
        TE(c.tq[1])
        PR(2) dqs<14, NE, 12>(c); PR(3)
        const unsigned tw = LDSV(fl + (OFF_TAG + 512 - OFF_FL - 768));
        SB
        if (DEF11) m4<11, 0>(c);
        RD(0) RD(1)
        if (!OWN) m4<12, 0>(c);
        PQ(2)
        PR(4) CK(0)
        TE(c.tq[2])
        AD(0) AD(1) AD(2) AD(3) asm volatile("s_nop 4");
        PR(5) PQ(4) FS(0)
        if (FLP == 1) GEL2(0, FS(1), ) else GELU(0)
        FS(2) PR(6) PQ(5)
        if (FLP == 3) GEL2(1, FS(3) PR(7), ) else GELU(1)
        FS(4) PR(8)
#define WDP 1
        if (WDP == 0) GEL1(2, WDOT(0)) else GELU(2)
        GELU(3)
        if (WDP == 1) WDOT(0)
        asm volatile("s_nop 0");
        WDOT(1)
        asm volatile("s_nop 4" : "+v"(va));
        TE(c.tq[4])
        const float v = va[0];
        TS(c.ts[14])
        if (__builtin_amdgcn_inverse_ballot_w64(0xffffull)) deliver(c, v, 2 * nq, s2, s8, big, sums, outp);
        if (__builtin_expect(watch != 0ull, 0)) {
            const u32x4 e4 = LDS(const u32x4, OFF_NB + c.lane16), l4 = LDS(const u32x4, OFF_NB + 1024 + c.lane16);
            bool opn[4];
#pragma unroll
            for (int k = 0; k < 4; ++k) opn[k] = c.m1 == 0u && e4[k] == l4[k];
            bool own = nq == 0 && lane >= 9 && lane < 12 && LDSV(OFF_TAG + 512 + lane * 4) == c.tag;
            unsigned f = c.tag + 3u;
#pragma unroll 1
            for (int it = 0; it < (1 << 22) && __builtin_amdgcn_ballot_w64(opn[0] || opn[1] || opn[2] || opn[3] || own) != 0ull; ++it) {
                bool chg = false;
#pragma unroll
                for (int k = 0; k < 4; ++k)
                    if (opn[k] && __hip_atomic_load(ws32 + lane * 4 + k, __ATOMIC_RELAXED, __HIP_MEMORY_SCOPE_AGENT) != e4[k]) { opn[k] = false; chg = true; }
                if (own && __hip_atomic_load(ws32 + (c.tad >> 2), __ATOMIC_RELAXED, __HIP_MEMORY_SCOPE_AGENT) != c.tag) { own = false; chg = true; }
                if (__builtin_amdgcn_ballot_w64(chg) != 0ull) { publish(c, f); ++f; }
                else __builtin_amdgcn_s_sleep(2);
            }
        }
        FINISH(c)
    } else {
        Cross& c = x;
#pragma unroll
        for (int q = 3; q < NP; ++q) { ia[q] = LDS(const u32x2, ib[(q - 1) & 3] + ((q - 1) >> 2) * 32); ic[q] = LDS(const u32x2, ib[(q - 1) & 3] + ((q - 1) >> 2) * 32 + 832); }
#pragma unroll
        for (int q = 3; q < NP; ++q) {
            c.ga[4 * q - 12] = ia[q][0] * 128u + srcA; c.ga[4 * q - 11] = ic[q][0] * 128u + srcB;
            c.ga[4 * q - 10] = ia[q][1] * 128u + srcA; c.ga[4 * q - 9] = ic[q][1] * 128u + srcB;
        }
#pragma unroll
        for (int k = 0; k < 40; ++k) PIN(c.ga[k]);
        {
            const int G = 2 * g + rho;
            const unsigned ea = (unsigned)(cw * 4096 + l15 * 128);
            c.re[0] = ea + (unsigned)((g ^ (l15 >> 1)) * 16); c.re[1] = c.re[0] ^ 64u;
            c.rx[0] = ea + (unsigned)(((2 * nq + (g >> 1)) ^ (l15 >> 1)) * 16); c.rx[1] = c.rx[0] + 8u;
            c.rx0[0] = c.rx[0] + (unsigned)((rho & 1) * 2048 + (G & 2) * 4); c.rx0[1] = c.rx[0] + (unsigned)((~rho & 1) * 2048 + ((G + 1) & 2) * 4);
            c.six = (l15 & 1) ? 0xddddeeeeu : 0xdddd4444u;
            c.pl = 0x05040100u; c.ph = 0x07060302u; c.xm[0] = 0x0000ffff0000ffffull; c.xm[1] = 0xffff0000ffff0000ull;
            PINS(c.pl); PINS(c.ph); PINS(c.xm[0]); PINS(c.xm[1]);
#pragma unroll
            for (int k = 0; k < 4; ++k) { const unsigned o = (l15 >> 1) == 2 * k + (g >> 1) ? 0x3f803f80u : 0u; c.on[k] = u32x4{o, o, o, o}; PIN(c.on[k]); }
            PIN(c.re[0]); PIN(c.re[1]); PIN(c.rx[0]); PIN(c.rx[1]); PIN(c.rx0[0]); PIN(c.rx0[1]); PIN(c.six);
            zclear<3>(c); zclear<0>(c);
        }
        SB
        BARRIER;
        GATHER(2)
        TP(c.tq[2])
        {
            const int q = (lane >> 2) & 3, p = lane & 3;
            const unsigned a0 = (unsigned)(OFF_WBI + (8 * g + q) * 32 + p * 8), a1 = (unsigned)(OFF_WBI + (8 * g + q) * 32 + ((p + rho) & 3) * 8);
#pragma unroll
            for (int k = 0; k < 2; ++k) {
                c.wb[k] = CAT8(tr_read(a0 + k * 1024), tr_read(a0 + k * 1024 + 128));
                c.wr[k] = CAT8(tr_read(a1 + k * 1024), tr_read(a1 + k * 1024 + 128));
            }
#pragma unroll
            for (int k = 0; k < 2; ++k) { PIN(c.wb[k]); PIN(c.wr[k]); }
        }
        gp sums = (gp)c.ws + (unsigned)(WS_SUM + (s0 + 16 * cw) * 8), outp = out + (unsigned)((s0 + 16 * cw) * 2);
        PINS(sums); PINS(outp);
        c.wsf = c.ws + WS_FRAG + nq * 65536 + cw * 4096; PINS(c.wsf);
        TP(c.tq[3])
        WAITCNT(8, 0); BARRIER;
        TP(c.tq[4])
        __builtin_amdgcn_s_setprio(1);
        CStep<0>::run(c);
        TS(c.ts[11])
        u32x4 bq[4];
        u32x4 (&bp)[4] = c.bp;
        f32x2 T[6][8];
#define SPLIT(s0, s1, i0, i1) { _Pragma("unroll") for (int s = s0; s < s1; ++s) _Pragma("unroll") for (int i_ = i0; i_ < i1; ++i_) { \
            const f32x4 x = c.ew[2 * (1 + 4 * (s >> 1) + (i_ >> 1)) + (s & 1)]; T[s][i_] = f32x2{x[2 * (i_ & 1)], x[2 * (i_ & 1) + 1]}; } }
#define CB(B, i, h, k) { B[(2 * (i) + (h)) & 3][k] = cvt_o(T[2 * (k)][i][h], T[2 * (k) + 1][i][h]); }
#define SC(i, k) { asm volatile("v_pk_mul_f32 %0, %1, %0 op_sel_hi:[0,1]" : "+v"(T[2 * (k)][i]) : "v"(ao[k])); asm volatile("v_pk_mul_f32 %0, %1, %0 op_sel:[1,0]" : "+v"(T[2 * (k) + 1][i]) : "v"(ao[k])); }
        SPLIT(0, 4, 0, 8)
        SB
#define FLZ 8
#define SW3 6
#define SWA 4
#define FL1 3072
#define FLR 25
#define FLW 27
        STAMP(2)
        FI(0)
        if (!C11) { cmm<11, 0>(c); cmm<11, 1>(c); cmm<11, 2>(c); cmm<11, 3>(c); cmm<11, 4>(c); cmm<11, 5>(c); }
        cmm<12, 0>(c); cmm<12, 1>(c); cmm<12, 2>(c); cmm<12, 3>(c);
        rdX<12>(c);
        FI(0x42)
        zpack<2>(c);
        cmm<12, 4>(c); cmm<12, 5>(c);
        const f32x4 hp = mf0(c.s1, c.zq);
        TS(c.ts[12])
        FI(0x50) TH(0)
        asm volatile("s_nop 4");
        swl<8, 8 + SWA>(c);
        FI(1) TH(1)
        float hq[4];
        asm volatile("v_mul_f32 %0, %4, %4\n\tv_mul_f32 %1, %5, %5\n\tv_mul_f32 %2, %6, %6\n\tv_mul_f32 %3, %7, %7\n\t"
                     "v_fma_f32 %0, %0, %9, %8\n\tv_fma_f32 %1, %1, %9, %8\n\tv_fma_f32 %2, %2, %9, %8\n\tv_fma_f32 %3, %3, %9, %8\n\t"
                     "v_mul_f32 %0, %0, %4\n\tv_mul_f32 %1, %1, %5\n\tv_mul_f32 %2, %2, %6\n\tv_mul_f32 %3, %3, %7\n\t"
                     "v_exp_f32 %0, %0\n\tv_exp_f32 %1, %1\n\tv_exp_f32 %2, %2\n\tv_exp_f32 %3, %3\n\t"
                     "v_fma_f32 %0, %0, %10, %10\n\tv_fma_f32 %1, %1, %10, %10\n\tv_fma_f32 %2, %2, %10, %10\n\tv_fma_f32 %3, %3, %10, %10\n\t"
                     "v_rcp_f32 %0, %0\n\tv_rcp_f32 %1, %1\n\tv_rcp_f32 %2, %2\n\tv_rcp_f32 %3, %3\n\t"
                     "v_mul_f32 %0, %0, %4\n\tv_mul_f32 %1, %1, %5\n\tv_mul_f32 %2, %2, %6\n\tv_mul_f32 %3, %3, %7\n\t"
                     "v_cvt_pk_bf16_f32 %0, %0, %1\n\tv_cvt_pk_bf16_f32 %1, %2, %3"
                     : "=&v"(hq[0]), "=&v"(hq[1]), "=&v"(hq[2]), "=&v"(hq[3]) : "v"(hp[0]), "v"(hp[1]), "v"(hp[2]), "v"(hp[3]), "v"(c.k0), "v"(c.k1), "v"(c.k2));
        FI(2) TH(2)
        const f32x2 hb = {hq[0], hq[1]};
        swl<8 + SWA, 10 + SWA>(c);
        f32x4 av[2];
        asm volatile("v_mfma_f32_16x16x16_bf16 %0, %1, %2, 0" : "=&v"(av[0]) : "v"(c.wa[0]), "v"(hb));
        asm volatile("v_mfma_f32_16x16x16_bf16 %0, %1, %2, 0" : "=&v"(av[1]) : "v"(c.wa[1]), "v"(hb));
        FI(3)
        swl<10 + SWA, 18 + SWA>(c);
        TH(3) FI(4)
        TS(c.ts[13])
        SB
        float as[8];
#pragma unroll
        for (int r = 0; r < 8; ++r) as[r] = exp_o(av[r >> 2][r & 3]);
        asm volatile("v_add_f32 %0, 1.0, %0\n\tv_add_f32 %1, 1.0, %1\n\tv_add_f32 %2, 1.0, %2\n\tv_add_f32 %3, 1.0, %3\n\tv_add_f32 %4, 1.0, %4\n\tv_add_f32 %5, 1.0, %5\n\tv_add_f32 %6, 1.0, %6\n\tv_add_f32 %7, 1.0, %7\n\t"
                     "v_rcp_f32 %0, %0\n\tv_rcp_f32 %1, %1\n\tv_rcp_f32 %2, %2\n\tv_rcp_f32 %3, %3\n\tv_rcp_f32 %4, %4\n\tv_rcp_f32 %5, %5\n\tv_rcp_f32 %6, %6\n\tv_rcp_f32 %7, %7"
                     : "+v"(as[0]), "+v"(as[1]), "+v"(as[2]), "+v"(as[3]), "+v"(as[4]), "+v"(as[5]), "+v"(as[6]), "+v"(as[7]));
        FI(5) TH(4)
        unsigned flg = LDSV(c.wf[1] + (OFF_FL + 2048 - OFF_B - SLOT));
        SB
        swl<18 + SWA, 18 + SWA + SW3>(c);
        FI(6)
        TG(c.tq[0]) TF(0)
        SB
        WAITCNT(63, 0);
        wait_set(flg, c.wf[1] + (OFF_FL + 2048 - OFF_B - SLOT));
        SB TF(1)
#define WSEQ(n) LDS(const u32x4, c.wf[((n) >> 4) & 1] + ((n) >> 5) * SLOT + (((n) & 15) ^ ((~(n) >> 2) & 1)) * 1024)
        u32x4 wq[64];
#pragma unroll
        for (int r = 0; r < WPRE; ++r) wq[r] = WSEQ(r);
        SB TF(2)
        swl<18 + SWA + SW3, 32>(c);
        SPLIT(4, 6, 0, 8)
        CB(bp, 0, 0, 2) CB(bp, 0, 1, 2)
        f32x2 ao[4] = {{as[0], as[1]}, {as[2], as[3]}, {as[4], as[5]}, {as[6], as[7]}};
        {
            f32x2 m[4] = {{c.ew[0][0], c.ew[0][1]}, {c.ew[1][0], c.ew[1][1]}, {c.ew[0][2], c.ew[0][3]}, {c.ew[1][2], c.ew[1][3]}};
#pragma unroll
            for (int r = 0; r < 4; r += 2) {
                asm volatile("v_pk_mul_f32 %0, %1, %0 op_sel_hi:[0,1]" : "+v"(m[r]) : "v"(ao[3]));
                asm volatile("v_pk_mul_f32 %0, %1, %0 op_sel:[1,0]" : "+v"(m[r + 1]) : "v"(ao[3]));
            }
            SC(0, 0) SC(0, 1) SC(0, 2) TF(3)
#pragma unroll
            for (int r = 0; r < 4; ++r) bq[r][3] = cvt_o(m[r & 2][r & 1], m[(r & 2) + 1][r & 1]);
        }
        ao[3][0] = sel(c.am, as[7], as[6]);
        PIN(ao[3]);
        SB
        FI(8)
        TG(c.tq[1]) TF(4)
        f32x4 u[2][4];
        float S1[7][16];
#define SQ(n) (((n) >> 1) + 4 * ((n) & 1))
#define FM(n, h, k) if ((k) < NJP) { asm volatile("v_pk_fma_f32 %0, %1, %2, %3" : "+v"(T[2 * (k) + (h)][SQ(n)]) : "v"(f32x2{u[h][2 + ((k) >> 1)][2 * ((k) & 1)], u[h][2 + ((k) >> 1)][2 * ((k) & 1) + 1]}), "v"(ao[k]), "v"(f32x2{u[h][(k) >> 1][2 * ((k) & 1)], u[h][(k) >> 1][2 * ((k) & 1) + 1]})); }
#define F1(n, h, s) if ((s) >= 2 * NJP) { float r_; \
            if ((s) < 6) { r_ = T[(s) % 6][SQ(n)][h]; asm volatile("v_fma_f32 %0, %1, %2, %3" : "+v"(r_) : "v"(u[h][2 + ((s) >> 2)][(s) & 3]), "v"(ao[(s) >> 1][(s) & 1]), "v"(u[h][(s) >> 2][(s) & 3])); } \
            else { asm volatile("v_fma_f32 %0, %1, %2, %3" : "=v"(r_) : "v"(u[h][3][2]), "v"(ao[3][0]), "v"(u[h][1][2]), "v"(u[h][3][3]), "v"(u[h][1][3])); } S1[s][2 * SQ(n) + (h)] = r_; }
#define TT8(s, q) f32x8{T[s][2 * (q)][0], T[s][2 * (q)][1], T[s][2 * (q) + 1][0], T[s][2 * (q) + 1][1], T[s][2 * (q) + 4][0], T[s][2 * (q) + 4][1], T[s][2 * (q) + 5][0], T[s][2 * (q) + 5][1]}
#define SS8(s, q) f32x8{S1[s][4 * (q)], S1[s][4 * (q) + 1], S1[s][4 * (q) + 2], S1[s][4 * (q) + 3], S1[s][4 * (q) + 8], S1[s][4 * (q) + 9], S1[s][4 * (q) + 10], S1[s][4 * (q) + 11]}
#define DOT1(a_, b_) asm volatile("v_smfmac_f32_16x16x64_bf16 %0, %1, %2, %3 abid:1" : "+v"(c.dg) : "v"(a_), "v"(b_), "v"(c.six));
#define DOT(q, s) { if ((s) == 6) { DOT1(c.X[0], (SS8(6, q))) } else if ((s) < 2 * NJP) { DOT1(c.Y[(4 * ((s) >> 1) + ((s) & 1) + 2 * (q)) % 12], (TT8((s) % 6, q))) } \
            else { DOT1(c.X[1 + 4 * (((s) % 6) >> 1) + ((s) & 1) + 2 * (q)], (SS8(s, q))) } }
#define DPOS 0x0600000504070301ull
#define DPOS2 0x20
#define DP(n, j) if (n >= 4 && n < 6) { constexpr int s_ = (((DPOS | DPOS2) >> (4 * ((8 * n + j) & 15))) & 15); if (s_) DOT(0, (s_ + 6) % 7) }
#define XQ(n, j) (((n) & 3) * 4 + ((j) > 5 ? 4 : (j) > 3 ? 2 : (j) - 2))
#define PJ(n, j) { constexpr int i = SQ(n), i1 = SQ((n + 1) & 7), np = (n + 7) & 7, z = 8 * n + j, h = j >> 2; \
                constexpr bool nx = n < 7, pv = n >= 1, al = nx && n != 1; \
                SB \
                if (z == FLW + 4 * NE - 16) { wait_set(flg, c.wf[1] + (OFF_FL - OFF_B - SLOT)); SB } \
                if (z == FLW - 16) { wait_set(flg, c.wf[1] + (OFF_FL + FL1 - OFF_B - SLOT)); SB } \
                if (!(j & 1)) { if (z + WPRE <= 64) WAITCNT(63, WPRE - 2); SB } \
                if (j & 2) u[h][(j & 3) ^ h ^ 1] = mf0(wq[z], bq[(2 * i + h) & 3]); else u[h][(j & 3) ^ h ^ 1] = mf0(wq[z], bp[(2 * i + h) & 3]); \
                SB \
                if (z == FLR + 4 * NE - 16) flg = LDSV(c.wf[1] + (OFF_FL - OFF_B - SLOT)); \
                if (z == FLR - 16) flg = LDSV(c.wf[1] + (OFF_FL + FL1 - OFF_B - SLOT)); \
                if (z + WPRE < 64) wq[(z + WPRE) & 63] = WSEQ(z + WPRE); \
                SB \
                if (j == 0) { CB(bq, i, 0, 0) DP(n, 0) CB(bq, i, 0, 1) } \
                if (j == 1) { CB(bq, i, 0, 2) DP(n, 1) CB(bq, i, 1, 0) CB(bq, i, 1, 1) } \
                if (n >= 4 && (j == 2 || j == 3 || j == 5)) xpair<2, XQ(n, j), XQ(n, j + 1)>(c); \
                if (j == 2) { if (pv) F1(np, 1, 4) CB(bq, i, 1, 2) DP(n, 2) if (pv) { F1(np, 1, 0) F1(np, 1, 1) F1(np, 1, 2) F1(np, 1, 3) } } \
                if (j == 3) { if (pv) F1(np, 1, 5) DP(n, 3) if (pv) F1(np, 1, 6) } \
                if (j == 4) { if (al) CB(bp, i1, 0, 0) DP(n, 4) if (al) CB(bp, i1, 0, 1) } \
                if (j == 5) { F1(n, 0, 4) DP(n, 5) F1(n, 0, 5) F1(n, 0, 6) } \
                if (j == 6) { if (nx) CB(bp, i1, 0, 2) DP(n, 6) if (al) CB(bp, i1, 1, 0) F1(n, 0, 0) F1(n, 0, 1) } \
                if (j == 7) { \
                    F1(n, 0, 2) F1(n, 0, 3) \
                    if (al) CB(bp, i1, 1, 1) \
                    if (nx) CB(bp, i1, 1, 2) \
                    FM(n, 0, 0) FM(n, 0, 1) FM(n, 0, 2) \
                    if (nx) { SC(i1, 0) SC(i1, 1) SC(i1, 2) } else asm volatile("s_nop 3"); \
                    FM(n, 1, 0) FM(n, 1, 1) FM(n, 1, 2) \
                    if (n == 3) { f32x2 a_, b_; asm volatile("v_mov_b64 %0, 0\n\tv_mov_b64 %1, 0" : "=v"(a_), "=v"(b_)); c.dg = f32x4{a_[0], a_[1], b_[0], b_[1]}; } \
                } }
        static_assert(FLW + WPRE <= 32 && FLZ <= FLR && FLR < FLW, "");
#define PN(n) TI(n) PJ(n, 0) PJ(n, 1) PJ(n, 2) PJ(n, 3) PJ(n, 4) PJ(n, 5) PJ(n, 6) PJ(n, 7)
        PN(0)
        PN(1)
        TG(c.tq[2])
        PN(2)
        PN(3)
        TG(c.tq[3])
        PN(4) PN(5) PN(6) PN(7) TI(8)
        unsigned s2;
        asm volatile("v_mbcnt_lo_u32_b32 %0, -1, 0\n\tv_mbcnt_hi_u32_b32 %0, -1, %0" : "=v"(s2));
        F1(7, 1, 0) F1(7, 1, 1) F1(7, 1, 2) F1(7, 1, 3)
        DOT(1, 0) F1(7, 1, 4) F1(7, 1, 5) DOT(1, 2) F1(7, 1, 6) DOT(1, 1)
        asm volatile("v_lshlrev_b32 %0, 1, %0\n\tv_and_b32 %0, 30, %0" : "+v"(s2));
        DOT(1, 3) DOT(1, 4)
        unsigned s8 = s2 * 4u;
        double big = 0x1.8p52;
        PIN(s8); PIN(big);
        SB
        DOT(1, 5) DOT(1, 6)
        const f32x4 dg = c.dg;
        float v, v2;
        asm volatile("s_mov_b64 vcc, %6\n\ts_nop 6\n\tv_cndmask_b32 %0, %2, %3, vcc\n\tv_cndmask_b32 %1, %4, %5, vcc\n\tv_cndmask_b32 %0, %0, %1, %7"
                     : "=&v"(v), "=&v"(v2) : "v"(dg[0]), "v"(dg[1]), "v"(dg[2]), "v"(dg[3]), "s"(0xaaaaaaaaaaaaaaaaull), "s"(0xccccccccccccccccull) : "vcc");
        TG(c.tq[4])
        STAMP(4)
        TS(c.ts[14])
        if (__builtin_amdgcn_inverse_ballot_w64(0xf0000f0000f0000full)) deliver(c, v, 2 * nq + 1, s2, s8, big, sums, outp);
        const int ln = __builtin_amdgcn_mbcnt_hi(~0u, __builtin_amdgcn_mbcnt_lo(~0u, 0u));
        const int lane = ln, tid = w * 64 + ln;
        (void)lane; (void)tid;
        FINISH(c)
    }
}

extern "C" void kernel_launch(void* const* d_in, const int* in_sizes, int n_in,
                              void* d_out, int out_size, void* d_ws, size_t ws_size,
                              hipStream_t stream) {
    const int* idx = (const int*)d_in[0];
    const uint16_t* table = (const uint16_t*)d_in[1];
    const uint16_t* se_w1 = (const uint16_t*)d_in[2];
    const uint16_t* se_w2 = (const uint16_t*)d_in[3];
    const uint16_t* w_bi = (const uint16_t*)d_in[4];
    const uint16_t* w_cross = (const uint16_t*)d_in[5];
    const uint16_t* w_deep1 = (const uint16_t*)d_in[6];
    const uint16_t* w_deep2 = (const uint16_t*)d_in[7];
    (void)hipFuncSetAttribute((const void*)k_main, hipFuncAttributeMaxDynamicSharedMemorySize, LDS_T);
    k_main<<<256, 512, LDS_T, stream>>>(idx, table, w_bi, w_deep1, w_cross, (unsigned*)d_ws, se_w1, se_w2, w_deep2, (__hip_bfloat16*)d_out, (unsigned long long*)((char*)d_ws + (128ull << 20)));
}
```

```cpp
#include <hip/hip_runtime.h>
#include <hip/hip_bf16.h>
#include <stdint.h>

#define EXM 1
#define PU 0
#define WSX 0
#define TB 0x2344
#define TBP 4
#define SMP 0
#define B1P 0
#define L1M 8
#define TGE 0
#define PT 7
#define NF 26
#define TRC 1.0028f
#define NP 13
#define PROD_CLASS(slot) ((((slot) & 3) == 1) && (((slot) >> 2) & 1) == 0)
#define STAMP(k)
#define TS(x)
#define TSP(P)
#define TX(k)
#define TP(x)
#define TEM 0x3ff
#define TE(x)
#define TI(n)
#define TF(k)
#define TH(k)
#define TG(x) TE(x)
using bf16x8 = __attribute__((ext_vector_type(8))) short;
using f32x4 = __attribute__((ext_vector_type(4))) float;
using f32x8 = __attribute__((ext_vector_type(8))) float;
using f32x2 = __attribute__((ext_vector_type(2))) float;
using s16x4 = __attribute__((ext_vector_type(4))) short;
using u32x4 = __attribute__((ext_vector_type(4))) unsigned;
using u32x8 = __attribute__((ext_vector_type(8))) unsigned;
using u32x2 = __attribute__((ext_vector_type(2))) unsigned;
using bf2 = __attribute__((ext_vector_type(2))) __bf16;
#define ST "\n\tSTUB"
#define PIN(x) asm volatile("" : "+v"(x))
#define PINS(x) asm volatile("" : "+s"(x))
#define SB __builtin_amdgcn_sched_barrier(0);
#define CAT8(a, b) __builtin_shufflevector((a), (b), 0, 1, 2, 3, 4, 5, 6, 7)

#define SLOT 16384
#define OFF_B 65536
#define OFF_FL 131072
#define OFF_WBI (OFF_FL + 4096)
#define OFF_SW1 (OFF_WBI + 2048)
#define OFF_SW2 (OFF_SW1 + 1024)
#define OFF_WD2 (OFF_SW1 + 768)
#define OFF_TAG (OFF_SW2 + 1280)
#define OFF_NB (OFF_TAG + 768)
#define OFF_JNK (OFF_NB + 2048)
#define OFF_IDX (OFF_JNK + 2048)
#define OFF_RAW (OFF_IDX + 8192)
#define LDS_T (OFF_IDX + 16384)
constexpr int FOFF[4] = {SLOT, OFF_B + SLOT, 2 * SLOT, OFF_B + 2 * SLOT};
#define P_FLAG 3
#define P_POLL 6
#define P_ST 3
#define P_CHK 9
#define P_STAT 8

#define WS_FLAG 1024
#define WS_SUM 4096
#define WS_SIDE 65536
#define WS_FRAG (1 << 20)

#define LDS(T, a) (*(__attribute__((address_space(3))) T*)(size_t)(a))
#define LDSV(a) (*(volatile const __attribute__((address_space(3))) unsigned*)(size_t)(a))
__device__ __forceinline__ unsigned pk_bf16(float lo, float hi) {
    const f32x2 v = {lo, hi};
    return __builtin_bit_cast(unsigned, __builtin_convertvector(v, bf2));
}
__device__ __forceinline__ unsigned cvt_o(float lo, float hi) { unsigned r; asm volatile("v_cvt_pk_bf16_f32 %0, %1, %2" ST : "=v"(r) : "v"(lo), "v"(hi)); return r; }
__device__ __forceinline__ float exp_o(float a) { float r; asm volatile("v_exp_f32 %0, %1" : "=v"(r) : "v"(a)); return r; }
template <typename A, typename B> __device__ __forceinline__ f32x4 mf0(const A& a, const B& b) { f32x4 d; asm volatile("v_mfma_f32_16x16x32_bf16 %0, %1, %2, 0" ST : "=v"(d) : "v"(a), "v"(b)); return d; }
template <typename A, typename B> __device__ __forceinline__ void mfa(f32x4& d, const A& a, const B& b) { asm volatile("v_mfma_f32_16x16x32_bf16 %0, %1, %2, %0" ST : "+v"(d) : "v"(a), "v"(b)); }
__device__ __forceinline__ float lo_f(unsigned x) { return __uint_as_float(x << 16); }
__device__ __forceinline__ float hi_f(unsigned x) { return __uint_as_float(x & 0xffff0000u); }
__device__ __forceinline__ s16x4 tr_read(unsigned a) {
    typedef short v4s __attribute__((ext_vector_type(4)));
    return __builtin_amdgcn_ds_read_tr16_b64_v4i16((__attribute__((address_space(3))) v4s*)(size_t)a);
}
#define DMA(name, ins) __device__ __forceinline__ void name(unsigned voff, const void* sbase, unsigned ldsaddr) { \
    asm volatile("s_mov_b32 m0, %2\n\ts_nop 0\n\t" ins :: "v"(voff), "s"(sbase), "s"(ldsaddr) : "memory"); }
DMA(dma16, "global_load_lds_dwordx4 %0, %1") DMA(dma4, "global_load_lds_dword %0, %1")
DMA(dma16sc, "global_load_lds_dwordx4 %0, %1 sc1") DMA(dma4sc, "global_load_lds_dword %0, %1 sc1")
__device__ __forceinline__ void st16sc(unsigned voff, void* sbase, u32x4 v) {
    asm volatile("global_store_dwordx4 %0, %1, %2 sc1\n\ts_nop 1" :: "v"(voff), "v"(v), "s"(sbase) : "memory");
}
template <int O> __device__ __forceinline__ void set_m0(unsigned base) { asm volatile("s_add_u32 m0, %0, %1" :: "s"(base), "n"(O) : "memory", "scc"); }
template <int O> __device__ __forceinline__ void bdma16o(unsigned voff, __amdgpu_buffer_rsrc_t rs, unsigned soff) {
    asm volatile("buffer_load_dwordx4 %0, %1, %2 offen offset:%3 lds" :: "v"(voff), "s"(rs), "s"(soff), "n"(O) : "memory");
}
template <int O> __device__ __forceinline__ void dma16o(unsigned voff, const void* sbase) {
    asm volatile("global_load_lds_dwordx4 %0, %1 offset:%2" :: "v"(voff), "s"(sbase), "n"(O) : "memory");
}
template <int O> __device__ __forceinline__ void dma16sco(unsigned voff, const void* sbase) {
    asm volatile("global_load_lds_dwordx4 %0, %1 offset:%2 sc1" ST :: "v"(voff), "s"(sbase), "n"(O) : "memory");
}
#define WAITCNT(vm, lgkm) __builtin_amdgcn_s_waitcnt(((vm) & 15) | (((vm) >> 4) << 14) | 0x70 | ((lgkm) << 8))
#define WAITX(vm, x, m) { WAITCNT((vm) + (x), 0); asm volatile("s_cmp_eq_u32 %0, 0\n\ts_cbranch_scc1 1f\n\ts_waitcnt vmcnt(%1)\n1:" :: "s"(m), "n"(vm) : "memory", "scc"); }
#define BARRIER asm volatile("s_barrier" ST ::: "memory")
__device__ __forceinline__ float sum32(float v) { const u32x2 p = __builtin_amdgcn_permlane32_swap(__float_as_uint(v), __float_as_uint(v), false, false); return __uint_as_float(p[0]) + __uint_as_float(p[1]); }
__device__ __forceinline__ float sum16(float v) { const u32x2 p = __builtin_amdgcn_permlane16_swap(__float_as_uint(v), __float_as_uint(v), false, false); return __uint_as_float(p[0]) + __uint_as_float(p[1]); }
__device__ __forceinline__ float sel(unsigned m, float a, float b) {
    return __uint_as_float(__builtin_amdgcn_bitop3_b32(m, __float_as_uint(a), __float_as_uint(b), 0xca));
}
__device__ __forceinline__ void wait_set(unsigned f, unsigned a) {
    if (__builtin_expect(__builtin_amdgcn_ballot_w64(f == 0u) != 0ull, WSX)) {
#pragma unroll 1
        for (int it = 0; it < (1 << 20) && __builtin_amdgcn_ballot_w64(LDSV(a) == 0u) != 0ull; ++it) { }
    }
    asm volatile("" ::: "memory");
}

struct Com {
    char* ws;
    unsigned lane16, ldsb;
    int c, nq, rho, st;
};
typedef __attribute__((address_space(1))) char* gp;
__device__ __forceinline__ void deliver(const Com& c, float v, int part, unsigned s2, unsigned s8, double big, gp sums, gp outp) {
    const gp side = (gp)c.ws + (WS_SIDE + c.st * 2048 + c.c * 64);
    u32x2 mine = __builtin_bit_cast(u32x2, __builtin_fma((double)v, 0x1p42, big));
    mine[1] += 0x04004000u - 0x43380000u;
    const unsigned ab = __float_as_uint(v) << 1;
    if (__builtin_expect(ab - 0x61000000u >= 0x22000000u && ab != 0u, 0)) {
        mine = u32x2{0u, 0x04004000u + (0x40000u << part)};
        __hip_atomic_store((__attribute__((address_space(1))) float*)(side + part * 256 + s2 * 2), v, __ATOMIC_RELAXED, __HIP_MEMORY_SCOPE_AGENT);
        asm volatile("s_waitcnt vmcnt(0)" ::: "memory");
    }
    const unsigned long long mv = __builtin_bit_cast(unsigned long long, mine);
    const unsigned long long tot = mv + __hip_atomic_fetch_add((__attribute__((address_space(1))) unsigned long long*)(sums + s8), mv, __ATOMIC_RELAXED, __HIP_MEMORY_SCOPE_AGENT);
    const unsigned th = (unsigned)(tot >> 32);
    if (th >= 0x20000000u) {
        const u32x2 dv = {(unsigned)tot, (th & 0x3ffffu) | 0x43300000u};
        float r = (float)__builtin_fma(__builtin_bit_cast(double, dv), 0x1p-42, -0x1.2p10);
        if (__builtin_expect(th >= 0x20040000u, 0)) {
#pragma unroll 1
            for (int k = 0; k < 8; ++k)
                if ((th >> (18 + k)) & 1u) r += __hip_atomic_load((__attribute__((address_space(1))) float*)(side + k * 256 + s2 * 2), __ATOMIC_RELAXED, __HIP_MEMORY_SCOPE_AGENT);
        }
        *(__attribute__((address_space(1))) uint16_t*)(outp + s2) = (uint16_t)pk_bf16(r, r);
    }
}

struct Deep : Com {
    __amdgpu_buffer_rsrc_t rw1;
    const char* wsf; const char* wsh;
    unsigned ldsh;
    unsigned sG[4], sB, tb;
    unsigned m1, m3, m4;
    int unit;
    unsigned long long pmask;
    unsigned xa;
    unsigned one;
    unsigned tad, tag, pl;
    unsigned boff[4], ra[4], rb[4];
    u32x4 w2[2];
    f32x4 ac[4][4];
    bf16x8 fa[2][4];
    s16x4 lo[2][4], hi[2][4];
};
template <int Q> __device__ __forceinline__ void prepB(Deep& c) {
    if constexpr (Q == 0) c.sB = 24 * 32768; else c.sB = c.sG[(Q - 1) & 3] + ((Q - 1) >> 2) * (8 * 32768);
    PINS(c.sB);
    set_m0<OFF_B + (Q & 3) * SLOT>(c.ldsb);
}
template <int I> __device__ __forceinline__ void issueB(Deep& c) { bdma16o<I * 1024>(c.boff[I], c.rw1, c.sB); }
__device__ __forceinline__ void mkboff(Deep& d, int lane, int cw, int nq) {
    const unsigned b0 = (unsigned)(((cw & 1) * 64 + (cw >> 1) * 32 + (lane >> 3)) * 512 + nq * 128 + ((lane & 7) ^ ((lane >> 3) & 2)) * 16);
    d.boff[0] = b0; d.boff[1] = (b0 ^ 64u) + 3072; d.boff[2] = b0 + 6144; d.boff[3] = (b0 ^ 64u) + 9216;
#pragma unroll
    for (int k = 0; k < 4; ++k) PIN(d.boff[k]);
}
template <int P, int N> __device__ __forceinline__ void rd1(Deep& c) {
    constexpr int tab[12] = {0, 4, 5, 6, 7, 1, 2, 3, 8, 9, 10, 11}, k = tab[N < 12 ? N : 0];
    if constexpr (N >= 12) { }
    else if constexpr (k < 4) c.fa[P & 1][k] = LDS(const bf16x8, c.ra[k] + (P & 3) * SLOT);
    else if constexpr (k & 1) c.hi[P & 1][(k - 4) >> 1] = tr_read(c.rb[(k - 4) >> 1] + (P & 3) * SLOT + 512);
    else c.lo[P & 1][(k - 4) >> 1] = tr_read(c.rb[(k - 4) >> 1] + (P & 3) * SLOT);
    SB
}
template <int P, int M, int K> __device__ __forceinline__ void mk(Deep& c) {
    if constexpr (P >= 0) mfa(c.ac[M][K], CAT8(c.lo[P & 1][K], c.hi[P & 1][K]), c.fa[P & 1][M]);
    SB
}
#define DEF11 0
template <int P, int N> __device__ __forceinline__ void m1(Deep& c) { if constexpr (!(DEF11 && P == 11 && ((N >> 1) & 3) == 0)) mk<P, (N >> 1) & 3, (N >> 3) * 2 + (N & 1)>(c); }
template <int P, int M> __device__ __forceinline__ void m4(Deep& c) { mk<P, M, 0>(c); mk<P, M, 1>(c); mk<P, M, 2>(c); mk<P, M, 3>(c); }
#define E9 2
#define T9 0
#define TOP10 4
constexpr int dyoung(int S) { return S < 7 ? 8 : S < 9 ? 4 : E9; }
template <int U0, int I> __device__ __forceinline__ void dq(Deep& c) {
    constexpr int U = U0 + (I >> 2);
    if constexpr (U < NP) {
        if constexpr ((I & 3) == 0) { prepB<U>(c); asm volatile("s_nop 0"); }
        issueB<I & 3>(c);
    } else if constexpr (U == 15) {
        if constexpr ((I & 1) == 0) { set_m0<FOFF[2] + (I & 2) * 4096>(c.ldsh); asm volatile("s_nop 0"); }
        dma16sco<(I & 1) * 1024>(c.lane16, c.wsh + (I & 2) * 4096);
    } else {
        if constexpr ((I & 3) == 0) { set_m0<FOFF[U - NP]>(c.ldsb); asm volatile("s_nop 0"); }
        dma16sco<(I & 3) * 1024>(c.lane16, c.wsf + (U - NP) * 16384);
    }
    SB
}
template <int S, int I0, int I1> __device__ __forceinline__ void dissue(Deep& c) {
    if constexpr (I0 < I1 && I0 < 4 && S < 7) { dq<S + 3, I0>(c); dissue<S, I0 + 1, I1>(c); }
}
constexpr int cum(unsigned long long t, int n) { int k = 0; for (int i = 0; i < n; ++i) k += (t >> (4 * i)) & 15; return k; }
template <int U0, int I0, int I1> __device__ __forceinline__ void dqs(Deep& c) { if constexpr (I0 < I1) { dq<U0, I0>(c); dqs<U0, I0 + 1, I1>(c); } }
template <int P, int N0, int N1> __device__ __forceinline__ void rdn(Deep& c) { if constexpr (N0 < N1) { rd1<P, N0>(c); rdn<P, N0 + 1, N1>(c); } }
__device__ __forceinline__ unsigned tag_addr(int lane, int unit, int nq, int st, int xcd) {
    const unsigned l4 = (unsigned)lane * 4u;
    const unsigned a0 = (unsigned)(WS_FLAG + unit * 4) + l4 * 32u;
    const unsigned a1 = (((unsigned)(nq * 256) + l4 * 64u) & 768u) + (unsigned)(st * 4);
    const unsigned a2 = (unsigned)(WS_FLAG + xcd * 128 - 64) + l4;
    const unsigned lo = l4 < 32u ? a0 : a1, hi = l4 < 168u ? a2 : (unsigned)(st * 4);
    return l4 < 64u ? lo : hi;
}
__device__ __forceinline__ void tags(unsigned tad, const void* ws, unsigned lds, unsigned long long m) {
    asm volatile("s_mov_b32 m0, %2\n\ts_mov_b64 exec, %3\n\tglobal_load_lds_dword %0, %1 sc1\n\ts_mov_b64 exec, -1" :: "v"(tad), "s"(ws), "s"(lds), "s"(m) : "memory");
}
__device__ __forceinline__ void publish(Deep& c, unsigned long long m) {
    asm volatile("s_cmp_eq_u64 %3, 0\n\ts_cbranch_scc1 1f\n\ts_mov_b64 exec, %3\n\tglobal_atomic_add %0, %1, %2\n\ts_mov_b64 exec, -1\n1:"
                 :: "v"(c.tad), "v"(c.one), "s"(c.ws), "s"(m) : "memory", "scc");
}
__device__ __forceinline__ void raw(const Deep& d, unsigned lane16, int cw, const void* w_cross, const void* ws) {
    if (__builtin_expect(d.m1 == 0u, 1)) {
        const unsigned r0 = lane16 >> 6, u = (unsigned)((cw >> 1) * (NF * NF * 128) + d.unit * 128 + (cw & 1) * 64) + ((lane16 ^ (lane16 >> 3 & 0x20u)) & 0x30u);
        dma16(r0 * (NF * 128) + u, w_cross, OFF_RAW + cw * 2048);
        dma16(min(r0 + 16u, NF - 1u) * (NF * 128) + u, w_cross, OFF_RAW + cw * 2048 + 1024);
        dma4sc((lane16 >> 2) + (unsigned)(cw * 256), ws, OFF_NB + cw * 256);
    }
    SB
}
template <int W> __device__ __forceinline__ void produce(Deep& c) {
    if (c.m1 == 0u) {
        const int unit = c.unit;
        const unsigned p8 = (c.lane16 << 4) & 0x300u, e2 = (c.lane16 >> 5) & 0x1eu;
        const unsigned rb = (unsigned)(OFF_RAW + c.c * 2048) + e2, rs = rb + (p8 >> 1) + (c.lane16 & 0x20u);
        const unsigned dm = p8 == (c.lane16 & 0x300u) ? ~0u : 0u;
        const int m = unit < 24 ? 2 * (unit & 6) + (unit & 1) + ((unit >> 2) & 2) : 4 * (unit & 1) + 2;
        unsigned fo = (unsigned)(WS_FRAG + (c.c & 1) * 131072 + (c.c >> 1) * 2048 + (unit >> 4) * 1024 + m * 64) + (((e2 & 12u) << 1) | ((e2 >> 2) & 4u) | (e2 & 2u)) * 2048u + ((c.lane16 & 0x30u) ^ (unsigned)((m & 4) << 3));
        if (unit >= 24) fo += (c.lane16 << 1) & 0x200u;
        WAITCNT(W, 15);
        asm volatile("" ::: "memory");
        u32x4 fr[2];
#pragma unroll
        for (int z = 0; z < 2; ++z) {
#pragma unroll
            for (int d = 0; d < 4; ++d) {
                const unsigned rp = (d < 3 ? rs ^ (unsigned)(z * 32) : rb + z * 32) + (unsigned)(512 * d);
                fr[z][d] = LDS(const uint16_t, rp) | ((unsigned)LDS(const uint16_t, rp + 64) << 16);
            }
            fr[z][3] &= dm;
        }
        st16sc(fo, c.ws, fr[0]); st16sc(fo + 65536u, c.ws, fr[1]);
        if (unit >= 24) { st16sc(fo ^ 512u, c.ws, u32x4{0u, 0u, 0u, 0u}); st16sc((fo ^ 512u) + 65536u, c.ws, u32x4{0u, 0u, 0u, 0u}); }
    }
    SB
}
template <bool L, int I> __device__ __forceinline__ void part(Deep& c) {
#define PART(cmp) asm volatile(cmp " %0, %1\n\ts_cbranch_scc1 1f\n\tbuffer_load_dwordx4 %2, %3, %4 offen offset:%5 lds\n1:" \
                               :: "s"(c.tb), "n"(I), "v"(c.boff[I]), "s"(c.rw1), "s"(c.sB), "n"(I * 1024) : "memory", "scc")
    if constexpr (L) PART("s_cmp_gt_u32"); else PART("s_cmp_le_u32");
    SB
}
__device__ __forceinline__ void check_flags(Deep& c) {
    if (c.m3 == 0u) {
        const unsigned l4 = c.lane16 >> 2;
        const unsigned long long open = __builtin_amdgcn_ballot_w64(c.tag == c.pl) & (c.nq ? 0x7ffffff0000ull : 0x3ffffff0000ull);
        if (__builtin_expect(open != 0ull, 0)) {
#pragma unroll 1
            for (int it = 0; it < (1 << 22); ++it) {
                tags(c.tad, c.ws, OFF_TAG + 256, 0x7ffffff0000ull);
                asm volatile("s_waitcnt vmcnt(0)" ::: "memory");
                if ((__builtin_amdgcn_ballot_w64(LDSV(OFF_TAG + 256 + l4) == c.tag) & open) == 0ull) break;
                __builtin_amdgcn_s_sleep(2);
            }
        }
    }
    SB
}
__device__ __forceinline__ void fetch_status(Deep& c) {
    if (c.m4 == 0u) {
        if (c.nq) dma16sc(c.lane16, c.ws, OFF_NB + 1024);
        else tags(c.tad, c.ws, OFF_TAG + 512, 0xe00ull);
    }
    SB
}
constexpr bool two(int P, int S) { return P == S || P == S + 1; }
constexpr int xk(int P, int k) {
    int x = 0;
    if (k == 1 || k == 2 || k == 4) x += two(P, P_ST) + (two(P, P_POLL) && P + 1 < P_CHK);
    if (k == 4 && P_FLAG != P_ST) x += two(P, P_FLAG);
    if (k == 2 || k == 4) x += P == 9 ? P_STAT == 9 : two(P, P_STAT) && !(P == 8 && P_POLL >= P_STAT);
    return x;
}
constexpr int below(int P, int v) { int b = 0; for (int k = 0; k < 5; ++k) if (xk(P, k) < v && xk(P, k) > b) b = xk(P, k); return b; }
constexpr unsigned xbits(int k) {
    unsigned m = 0;
    for (int P = 0; P < 10; ++P) { int v = below(P, 99); for (int j = 0; v > 0; ++j, v = below(P, v)) if (xk(P, k) == v) m |= 1u << (3 * P + j); }
    return m;
}
template <int P> __device__ __forceinline__ void xwait(Deep& c) {
    constexpr int d = dyoung(P), a = below(P, 99), b = below(P, a), e = below(P, b), f = below(P, e);
    static_assert(f == 0, "");
    WAITCNT(d + a, 0);
#define XW(n) "s_bitcmp1_b32 %0, %" #n "\n\ts_cbranch_scc1 1f\n\ts_waitcnt vmcnt(%"
    if constexpr (e > 0) asm volatile(XW(1) "4)\n\t" XW(2) "5)\n\t" XW(3) "6)\n1:" :: "s"(c.xa), "n"(3 * P), "n"(3 * P + 1), "n"(3 * P + 2), "n"(d + b), "n"(d + e), "n"(d) : "memory", "scc");
    else if constexpr (b > 0) asm volatile(XW(1) "3)\n\t" XW(2) "4)\n1:" :: "s"(c.xa), "n"(3 * P), "n"(3 * P + 1), "n"(d + b), "n"(d) : "memory", "scc");
    else if constexpr (a > 0) asm volatile(XW(1) "2)\n1:" :: "s"(c.xa), "n"(3 * P), "n"(d) : "memory", "scc");
}
#define DTOP 2
#define DTOPB 4
#define A8 12
#define A9 12
#define A10 12
#define T8 0x842100
#define T10 0xaa
constexpr int adv(int P) { return P == 8 ? A8 : P == 9 ? A9 : P == 10 ? A10 : 0; }
constexpr int bits(unsigned t, int n) { return __builtin_popcount(n < 32 ? t & ((1u << n) - 1u) : t); }
template <int P, int N> __device__ __forceinline__ void drun(Deep& c) {
    constexpr int old = 16 - adv(P - 1), tot = old + adv(P), U0 = P == 8 ? 10 : 11, E = P == 8 ? 4 : P == 10 ? 8 : E9, I0 = P == 10 ? TOP10 : 0, per = old >= 12 ? 1 : old >= 6 ? 2 : old >= 4 ? 3 : 12;
    constexpr unsigned T = P == 8 ? T8 : P == 10 ? T10 : T9;
    if constexpr (P == 10 && N == 0) { if constexpr (E9 & 3) { prepB<11>(c); asm volatile("s_nop 0"); } dqs<U0, E9, TOP10>(c); }
    dqs<U0, I0 + bits(T, N), I0 + bits(T, N + 1)>(c);
    if constexpr (N == 0 && per == 12) rdn<P, 0, 12>(c);
    if constexpr (N < old) m1<P - 1, adv(P - 1) + N>(c); else m1<P, N - old>(c);
    if constexpr (per < 12) rdn<P, N * per, (N * per + per < 12 ? N * per + per : 12)>(c);
    if constexpr (N == 5) { TX(0) }
    if constexpr (N == 11) { TX(1) }
    if constexpr (N == 17) { TX(2) }
    if constexpr (N + 1 < tot) drun<P, N + 1>(c); else { dqs<U0, I0 + bits(T, tot), E>(c); TX(3) }
}
template <int P, int N> __device__ __forceinline__ void mrest(Deep& c) { if constexpr (N < 16) { m1<P, N>(c); mrest<P, N + 1>(c); } }
#define T11 0x1249
#define NE 6
#define TOP11 1
#define E11 0
#define R12 6
constexpr int mn(int a, int b) { return a < b ? a : b; }
constexpr bool VT = TB != 0x4444 || TBP != 4;
constexpr int TMIN = mn(mn(mn(TB & 15, (TB >> 4) & 15), mn((TB >> 8) & 15, TB >> 12)), TBP);
#define DT 0x2222222
#define DL 12
constexpr int dtop(int P) { return (P & 3) == 2 ? (VT ? TMIN : DTOPB) : (DT >> (4 * P)) & 15; }
template <int P, int N> __device__ __forceinline__ void dbody(Deep& c) {
    constexpr int top = dtop(P);
    m1<P - 1, N>(c);
    rd1<P, N>(c);
    if constexpr (VT && (P & 3) == 2) { if constexpr (N >= DL && N - DL + TMIN < 4) part<true, (N - DL + TMIN) & 3>(c); }
    else if constexpr (N >= DL) dissue<P, top + N - DL, (N == 15 ? 16 : top + N - DL + 1)>(c);
    if constexpr (N < 15) dbody<P, N + 1>(c);
}
template <int N> __device__ __forceinline__ void dlast(Deep& c) {
    dqs<14, TOP11 + bits(T11, N), TOP11 + bits(T11, N + 1)>(c);
    m1<11, N>(c);
    if constexpr (N == R12) { WAITCNT(TOP11 + bits(T11, N + 1), 15); SB }
    if constexpr (N >= R12 && N < R12 + 8) rd1<12, (N - R12 < 4 ? N - R12 + 1 : N - R12 + 4)>(c);
    if constexpr (N < 15) dlast<N + 1>(c); else dqs<14, TOP11 + bits(T11, 16), NE>(c);
}
template <int P> struct DStep {
    static __device__ __forceinline__ void run(Deep& c) {
        TSP(P)
        SB
        if constexpr (P == P_FLAG || P == P_ST) publish(c, P_FLAG == P_ST ? c.pmask : c.pmask & (P == P_ST ? 0x100ull : 0xffull));
        if constexpr (P == P_CHK) check_flags(c);
        if constexpr (P == P_STAT) fetch_status(c);
        if constexpr (P < 8) {
            dissue<P, 0, dtop(P)>(c);
            if constexpr (VT && (P & 3) == 2) {
                if constexpr (TMIN == 0) { prepB<P + 3>(c); part<false, 0>(c); }
                if constexpr (TMIN <= 1) part<false, 1>(c);
                if constexpr (TMIN <= 2) part<false, 2>(c);
                if constexpr (TMIN <= 3) part<false, 3>(c);
            }
            dbody<P, 0>(c);
        } else if constexpr (P <= 10) {
            drun<P, 0>(c);
            if constexpr (P == 10 && E11) { WAITCNT(4, 15); SB rdn<11, 1, 5>(c); rdn<11, 8, 12>(c); }
        } else {
            if constexpr (PU == 11) __builtin_amdgcn_s_setprio(2);
            rd1<11, 0>(c); rdn<11, 5, 8>(c);
            dqs<14, 0, TOP11>(c);
            if constexpr (!E11) { WAITCNT(4 + TOP11, 15); SB rdn<11, 1, 5>(c); rdn<11, 8, 12>(c); }
            mrest<10, A10>(c);
            rd1<12, 0>(c); rdn<12, 5, 8>(c);
            TS(c.ts[12])
            if constexpr (PU == 12) __builtin_amdgcn_s_setprio(2);
            dlast<0>(c);
        }
        if constexpr (P <= 10) {
            if constexpr (P == 4) {
                c.w2[0] = LDS(const u32x4, c.w2[0][0]); c.w2[1] = LDS(const u32x4, c.w2[1][0]);
            }
            if constexpr (P == P_POLL) { if (c.m3 == 0u) tags(c.tad, c.ws, OFF_TAG + 256, 0x7ffffff0000ull); }
            SB
            if constexpr (P == 10) WAITCNT(63, 0); else xwait<(P < 10 ? P : 0)>(c);
            TX(4)
            if constexpr (P == P_CHK - 1) { SB c.pl = LDSV(OFF_TAG + 256 + (c.lane16 >> 2)); SB }
            BARRIER;
            DStep<P + 1>::run(c);
        }
    }
};

struct Cross : Com {
    const char* tb[4]; const char* wsf;
    unsigned ga[40];
    unsigned re[2], rx[2], rx0[2], six, wf[2], pl, ph, fl;
    unsigned long long xm[2];
    f32x4 dg;
    s16x4 wa[2];
    bf16x8 wb[2], wr[2];
    u32x4 fe[2][2][2];
    f32x4 ew[NF], zs[4];
    u32x4 X[NP], Y[12], bp[4];
    float k0, k1, k2;
    unsigned am, wad[2];
    u32x4 on[4], s1, zq;
};
template <int I> __device__ __forceinline__ void issueA(Cross& c, unsigned off) { dma16o<I * 1024>(off, c.tb[I]); }
template <int K, int S, int N> __device__ __forceinline__ void sw1(Cross& c) {
    constexpr int a = 1 + 4 * K + (S == 1 ? 2 : (S == 3 ? 1 : 0)), b = 1 + 4 * K + (S == 0 ? 1 : (S == 2 ? 2 : 3)), Q = N >> 2, R = N & 3;
    float x = c.ew[2 * a + Q][R], y = c.ew[2 * b + Q][R];
    if (S >= 2) asm volatile("v_permlane32_swap_b32 %0, %1" ST : "+v"(x), "+v"(y)); else asm volatile("v_permlane16_swap_b32 %0, %1" ST : "+v"(x), "+v"(y));
    c.ew[2 * a + Q][R] = x; c.ew[2 * b + Q][R] = y;
}
template <int K, int S, int N0 = 0, int N1 = 8> __device__ __forceinline__ void swn(Cross& c) { if constexpr (N0 < N1) { sw1<K, S, N0>(c); swn<K, S, N0 + 1, N1>(c); } }
#define S8 24
#define S9 24
template <int N0, int N1> __device__ __forceinline__ void swl(Cross& c) { if constexpr (N0 < N1 && N0 < 32) { sw1<2, (N0 >> 3), (N0 & 7)>(c); swl<N0 + 1, N1>(c); } }
constexpr int sw0(int P) { return P <= 3 ? 0 : P <= 5 ? 8 : P == 6 ? 20 : P == 7 ? 32 : P == 8 ? 40 : P == 9 ? 40 + S8 : P == 10 ? 40 + S9 : 64; }
template <int P, int N0, int N1> __device__ __forceinline__ void tsw(Cross& c) {
    if constexpr (N0 < N1 && sw0(P) + N0 < sw0(P + 1)) { sw1<((sw0(P) + N0) >> 5), ((sw0(P) + N0) >> 3) & 3, (sw0(P) + N0) & 7>(c); tsw<P, N0 + 1, N1>(c); }
}
constexpr int fbuf(int P) { return P & 1; }
template <int P, int N> __device__ __forceinline__ void cmm(Cross& c) {
    if constexpr (P >= 0) {
        constexpr int B = fbuf(P);
        if constexpr (N < 2)
            asm volatile("v_smfmac_f32_16x16x64_bf16 %0, %1, %2, %3" : "+v"(c.zs[P ? (P - 1) >> 2 : 3])
                         : "v"(c.on[P ? (P - 1) & 3 : 0]), "v"(__builtin_shufflevector(c.fe[B][N][0], c.fe[B][N][1], 0, 1, 2, 3, 4, 5, 6, 7)), "v"(c.six));
        else if constexpr (N < 4) c.ew[2 * P + (N & 1)] = mf0(P ? c.wb[0] : c.wr[0], c.fe[B][0][N & 1]);
        else mfa(c.ew[2 * P + (N & 1)], P ? c.wb[1] : c.wr[1], c.fe[B][1][N & 1]);
    }
    SB
}
template <int P, int N> __device__ __forceinline__ void rdE(Cross& c) {
    c.fe[fbuf(P)][N >> 1][N & 1] = LDS(const u32x4, c.re[N >> 1] + (P & 3) * SLOT + (N & 1) * 2048);
    SB
}
template <int K> __device__ __forceinline__ void zpack(Cross& c) {
    float a = c.zs[K][0], b = c.zs[K][2];
    unsigned r;
    asm volatile("v_add_f32 %1, %1, %3\n\tv_add_f32 %2, %2, %4\n\tv_cvt_pk_bf16_f32 %0, %1, %2" : "=v"(r), "+v"(a), "+v"(b) : "v"(c.zs[K][1]), "v"(c.zs[K][3]));
    c.zq[K] = r;
    SB
}
template <int K> __device__ __forceinline__ void zclear(Cross& c) { c.zs[K] = f32x4{0.f, 0.f, 0.f, 0.f}; PIN(c.zs[K]); SB }
#define NJP 3
template <int P> __device__ __forceinline__ void rdX(Cross& c) {
    constexpr int k = 1 + ((P - 1) & ~3), a = ((P - 1) & 3) * 32, b = (P & 3) * 32;
    constexpr bool jp = (P - 1) / 4 < NJP;
#define RDX "s_mov_b64 exec, %6\n\tds_read2st64_b64 %0, %4 offset0:%7 offset1:%8\n\tds_read2st64_b64 %1, %4 offset0:%9 offset1:%10\n\t" \
            "ds_read2st64_b64 %2, %5 offset0:%7 offset1:%8\n\tds_read2st64_b64 %3, %5 offset0:%9 offset1:%10\n\ts_mov_b64 exec, -1"
#define RDY "s_mov_b64 exec, %5\n\tds_read_b128 %0, %4 offset:%6\n\tds_read_b128 %1, %4 offset:%7\n\tds_read_b128 %2, %4 offset:%8\n\tds_read_b128 %3, %4 offset:%9\n\ts_mov_b64 exec, -1"
    if constexpr (P == 0) { const u32x2 lo = LDS(const u32x2, c.rx0[0]), hi = LDS(const u32x2, c.rx0[1]); c.X[0] = u32x4{lo[0], lo[1], hi[0], hi[1]}; }
    else if constexpr ((P & 3) == 2 && jp)
        asm volatile(RDY : "=&v"(c.X[k]), "=&v"(c.X[k + 1]), "=&v"(c.X[k + 2]), "=&v"(c.X[k + 3]) : "v"(c.rx[0]), "s"(c.xm[0]), "n"(a * 512), "n"(a * 512 + 2048), "n"(b * 512), "n"(b * 512 + 2048) : "memory");
    else if constexpr ((P & 3) == 0 && jp)
        asm volatile(RDY : "+v"(c.X[k]), "+v"(c.X[k + 1]), "+v"(c.X[k + 2]), "+v"(c.X[k + 3]) : "v"(c.rx[0]), "s"(c.xm[1]), "n"(a * 512), "n"(a * 512 + 2048), "n"(b * 512), "n"(b * 512 + 2048) : "memory");
    else if constexpr ((P & 3) == 2)
        asm volatile(RDX : "=&v"(c.X[k]), "=&v"(c.X[k + 1]), "=&v"(c.X[k + 2]), "=&v"(c.X[k + 3]) : "v"(c.rx[0]), "v"(c.rx[1]), "s"(c.xm[0]), "n"(a), "n"(b), "n"(a + 4), "n"(b + 4) : "memory");
    else if constexpr ((P & 3) == 0)
        asm volatile(RDX : "+v"(c.X[k]), "+v"(c.X[k + 1]), "+v"(c.X[k + 2]), "+v"(c.X[k + 3]) : "v"(c.rx[0]), "v"(c.rx[1]), "s"(c.xm[1]), "n"(a), "n"(b), "n"(a + 4), "n"(b + 4) : "memory");
    SB
}
template <int K, int N0, int N1> __device__ __forceinline__ void xpair(Cross& c) {
    if constexpr (N0 < N1 && K < NJP) {
        constexpr int m = N0 >> 2, d = N0 & 3, s = 1 + 4 * K + (d & 2), e = (m & 2) + (d & 1);
        unsigned r;
        asm volatile("v_perm_b32 %0, %1, %2, %3" ST : "=v"(r) : "v"(c.X[s + 1][e]), "v"(c.X[s][e]), "s"((m & 1) ? c.ph : c.pl));
        c.Y[4 * K + m][d] = r;
        xpair<K, N0 + 1, N1>(c);
    }
}
#define C11 1
#define G8 0x5421
#define G8P(n) { if constexpr ((G8 & 15) == (n)) GQ(10, 0) if constexpr (((G8 >> 4) & 15) == (n)) GQ(10, 1) if constexpr (((G8 >> 8) & 15) == (n)) GQ(10, 2) if constexpr (((G8 >> 12) & 15) == (n)) GQ(10, 3) }
#define FPOS 0x01504241
#define FFL 4
#define BP 1
#define FI1(k, n) if (((FPOS >> (8 * (k))) & 255) == (n)) { if (!(k)) { set_m0<FOFF[0]>(c.ldsb); asm volatile("s_nop 0"); } dma16sco<(k) * 1024>(c.lane16, c.wsf); SB }
#define FI(n) FI1(0, n) FI1(1, n) FI1(2, n) FI1(3, n) if ((n) == BP) { BARRIER; SB } if ((n) == FFL) { WAITCNT(0, 15); SB LDS(unsigned, c.fl) = c.fl; SB }
#define GQ(Q, J) { if constexpr ((J) == 0) { set_m0<((Q) & 3) * SLOT>(c.ldsb); asm volatile("s_nop 0"); } issueA<J>(c, c.ga[4 * (Q) - 12 + (J)]); SB }
__device__ __forceinline__ void csetup(Cross& c) {
            const unsigned r6 = c.lane16 * 4u + (unsigned)c.rho * 256u;
            const unsigned am = (c.lane16 >> 8) + (unsigned)c.rho;
            c.wf[0] = (r6 & 0x3c0u) | (((am << 4) ^ ((r6 >> 3) & 0x20u)) & 0x30u) | SLOT;
            c.wf[1] = c.wf[0] + OFF_B;
            const unsigned wap = OFF_SW2 + (c.lane16 & 0x3c0u) + ((c.lane16 >> 1) & 0x18u);
            c.wa[0] = tr_read(wap); c.wa[1] = tr_read(wap + 32);
            const unsigned r2 = (c.lane16 >> 3) & 30u, s1 = OFF_SW1 + min(r2, 24u), sg = s1 + (am & 3u) * 52u;
#pragma unroll
            for (int k = 0; k < 4; ++k) {
                const unsigned a = (k < 3 ? sg : s1) + k * 208;
                const unsigned v = LDS(const uint16_t, a) | ((unsigned)LDS(const uint16_t, a + 26) << 16);
                c.s1[k] = r2 < 26u ? v : 0u;
            }
            PIN(c.s1);
            PIN(c.wf[0]); PIN(c.wf[1]); PIN(c.wa[0]); PIN(c.wa[1]);
}
template <int P> struct CStep {
    static __device__ __forceinline__ void run(Cross& c) {
        TSP(P)
        if constexpr (P == 7) { STAMP(6) }
        SB
        if constexpr (P == 1) {
#pragma unroll
            for (int k = 0; k < 2; ++k) c.wr[k] = CAT8(tr_read(c.wad[1] + k * 1024), tr_read(c.wad[1] + k * 1024 + 128));
            SB
            cmm<0, 0>(c); rdE<1, 0>(c); set_m0<0>(c.ldsb); cmm<0, 1>(c); rdE<1, 1>(c);
            issueA<0>(c, c.ga[4]); rdE<1, 2>(c); issueA<1>(c, c.ga[5]); rdE<1, 3>(c);
#pragma unroll
            for (int k = 0; k < 2; ++k) c.wb[k] = CAT8(tr_read(c.wad[0] + k * 1024), tr_read(c.wad[0] + k * 1024 + 128));
            SB
            cmm<0, 2>(c); cmm<0, 3>(c); issueA<2>(c, c.ga[6]); cmm<0, 4>(c); cmm<0, 5>(c); issueA<3>(c, c.ga[7]);
            PIN(c.wb[0]); PIN(c.wb[1]);
        } else if constexpr (P <= 8) {
            cmm<P - 1, 2>(c); rdE<P, 0>(c);
            cmm<P - 1, 3>(c); rdE<P, 1>(c);
            cmm<P - 1, 4>(c); rdE<P, 2>(c);
            cmm<P - 1, 5>(c); rdE<P, 3>(c);
            cmm<P - 1, 0>(c);
            if constexpr (P < 7) set_m0<((P + 3) & 3) * SLOT>(c.ldsb);
            cmm<P - 1, 1>(c);
            rdX<P>(c);
        }
        constexpr bool xw = P && !(P & 1);
#define GA(J) if constexpr (P < 7 && P != 1) issueA<J>(c, c.ga[4 * P + (J)]); SB
        if constexpr (P == 4 || P == 7) zclear<(P + 1) / 4>(c);
        if constexpr (P == 7) {
#pragma unroll
            for (int r = 0; r < 4; ++r) { c.bp[r][3] = cvt_o(c.ew[0][r], c.ew[1][r]); c.bp[r][0] = cvt_o(c.ew[2][r], c.ew[3][r]); }
            c.k0 = -2.0f * 0.7978845608028654f * 1.4426950408889634f / 64.0f; c.k1 = c.k0 * (0.044715f / 4096.0f); c.k2 = -64.0f / 1.4426950408889634f;
            c.am = (c.wf[0] & 16u) ? ~0u : 0u;
            c.fl = (c.lane16 >> 2) + (unsigned)(OFF_FL + 2048 + c.c * 256);
            PIN(c.k0); PIN(c.k1); PIN(c.k2); PIN(c.am); PIN(c.fl);
            SB
        }
        if constexpr (P == 2 || P == 6) zpack<(P == 2 ? 3 : 0)>(c);
        if constexpr (P == 8) {
            TX(0) G8P(0)
            cmm<8, 2>(c); cmm<8, 3>(c); cmm<8, 4>(c); cmm<8, 5>(c); cmm<8, 0>(c); cmm<8, 1>(c);
            G8P(1) TX(1)
            WAITCNT(63, 0); SB
            xpair<1, 0, 8>(c); SB G8P(2) xpair<1, 8, 16>(c); SB G8P(3)
            zpack<1>(c); TX(2)
            tsw<8, 0, S8 / 2>(c); SB G8P(4) tsw<8, S8 / 2, S8>(c); SB G8P(5)
        } else if constexpr (P == 9) {
            GQ(11, 0) rdE<9, 0>(c); rdE<9, 1>(c); rdE<9, 2>(c); rdE<9, 3>(c); tsw<9, 0, 2>(c); SB GQ(11, 1) tsw<9, 2, 6>(c); SB GQ(11, 2) TX(0)
            cmm<9, 2>(c); cmm<9, 3>(c); cmm<9, 4>(c); GQ(11, 3) cmm<9, 5>(c); cmm<9, 0>(c); cmm<9, 1>(c); TX(1)
            GQ(12, 0) tsw<9, 6, 10>(c); SB GQ(12, 1) tsw<9, 10, 14>(c); SB GQ(12, 2) TX(2) tsw<9, 14, 24>(c); SB GQ(12, 3)
        } else if constexpr (P == 10) {
            rdE<10, 0>(c); rdE<10, 1>(c); rdE<10, 2>(c); rdE<10, 3>(c);
            tsw<10, 0, 24>(c); SB
#pragma unroll
            for (int r = 0; r < 4; ++r) c.bp[r][1] = cvt_o(c.ew[10][r], c.ew[11][r]);
            SB
            cmm<10, 2>(c); cmm<10, 3>(c); cmm<10, 4>(c); cmm<10, 5>(c); rdX<10>(c); cmm<10, 0>(c); cmm<10, 1>(c); TX(0)
            WAITCNT(4, 15); asm volatile("" ::: "memory"); SB TX(1)
            rdE<11, 0>(c); rdE<11, 1>(c); rdE<11, 2>(c); rdE<11, 3>(c);
            swn<2, 0, 0, 8>(c); SB
            WAITCNT(63, 0); SB TX(2)
            if constexpr (C11) { cmm<11, 0>(c); cmm<11, 1>(c); cmm<11, 2>(c); cmm<11, 3>(c); cmm<11, 4>(c); cmm<11, 5>(c); }
        }
        else if constexpr (xw) { tsw<P, 0, 4>(c); WAITCNT(63, 0); GA(0) tsw<P, 4, 6>(c); GA(1) tsw<P, 6, 8>(c); GA(2) tsw<P, 8, 10>(c); GA(3) tsw<P, 10, 12>(c); }
        else { GA(0) GA(1) tsw<P, 0, 4>(c); GA(2) tsw<P, 4, 8>(c); GA(3) tsw<P, 8, 12>(c); }
        if constexpr (P == 4) { xpair<0, 0, 16>(c); SB }
        if constexpr (P == 3) csetup(c);
        SB
        TX(3)
        WAITCNT(P == 7 || P == 8 ? 4 : P < 10 ? 8 + (P == 0 && SMP == 2 ? 2 : 0) : 0, 0);
        TX(4)
        if constexpr (P == 10) { asm volatile("" ::: "memory"); SB rdE<12, 0>(c); rdE<12, 1>(c); rdE<12, 2>(c); rdE<12, 3>(c); FI(0x41) }
        else { BARRIER; CStep<P + 1>::run(c); }
    }
};

#define PFC 148
#define WPRE 5
#define ALN 4096
__global__ __launch_bounds__(512) __attribute__((aligned(ALN))) void k_main(const int* __restrict__ idx, const uint16_t* __restrict__ table,
        const uint16_t* __restrict__ w_bi, const uint16_t* __restrict__ w_deep1, const uint16_t* __restrict__ w_cross,
        unsigned* __restrict__ ws32, const uint16_t* __restrict__ se_w1, const uint16_t* __restrict__ se_w2,
        const uint16_t* __restrict__ w_deep2_, __hip_bfloat16* __restrict__ out_, unsigned long long* __restrict__ stamps) {
    SB
    asm volatile(".set .Lnext, 1\n\t.macro STUB1 n\n\t.if .Lnext == \\n\n.Lst\\n: s_cbranch_execz .Lback\n\t.endif\n\t.endm\n\t"
                 ".macro STUB\n\t.if . - _Z6k_mainPKiPKtS2_S2_S2_PjS2_S2_S2_P14__hip_bfloat16Py >= .Lnext * 4096\n\t"
                 "STUB1 1\n\tSTUB1 2\n\tSTUB1 3\n\tSTUB1 4\n\t.set .Lnext, .Lnext + 1\n\t.endif\n\t.endm");
    const int tid = threadIdx.x, lane = tid & 63;
    const int w = __builtin_amdgcn_readfirstlane(tid >> 6);
    const int bid = blockIdx.x;
    const int xcd = bid & 7, slot = bid >> 3;
    const int st = xcd * 8 + (slot >> 2), nq = slot & 3;
    const int s0 = st * 64;
    const int rho = (st + nq) & 3, cw = w & 3;
    const int l15 = lane & 15, g = lane >> 4;
    LDS(u32x2, OFF_FL + tid * 8) = u32x2{0u, 0u};
    unsigned long long pc;
    {
        const unsigned ln = min((unsigned)(4 + w * 4 + (lane & 3) + ((lane >> 2) & 1) * (32 + nq * 32)), (unsigned)PFC) * 128u;
        asm volatile("s_getpc_b64 %0\n\ts_mov_b32 m0, %2\n\ts_mov_b64 exec, 0xff\n\tglobal_load_lds_dword %1, %0\n\ts_mov_b64 exec, -1"
                     : "=&s"(pc) : "v"(ln), "n"(OFF_JNK) : "memory");
    }
    SB
    float gc0 = -2.0f * 0.7978845608028654f * 1.4426950408889634f, gc1 = gc0 * 0.044715f;
#define FIN_ST(c)
#define FIN_SA(c)
#define FINISH(c) STAMP(5) FIN_ST(c) FIN_SA(c) __builtin_amdgcn_endpgm(); __builtin_unreachable();
    unsigned lane16 = (unsigned)lane * 16u;
    int deep = w >> 2;
    Deep d;
    d.ldsb = (unsigned)(cw * 4096);
    unsigned o2 = min(lane16, 624u), im0 = (unsigned)(OFF_IDX + cw * 2048), io = (unsigned)((s0 + 16 * cw) * (NF * 4));
    mkboff(d, lane, cw, nq);
#pragma unroll
    for (int k = 0; k < 4; ++k) { d.sG[k] = (unsigned)(((k + rho) & 3) * (2 * 32768)); PINS(d.sG[k]); }
    d.unit = xcd + 8 * ((st & 7) >> 1);
    d.m1 = (unsigned)((nq ^ 1) | (st & 1) | ((d.unit + 32 - NF) >> 5));
    PIN(lane16); PIN(o2); PINS(im0); PINS(io); PINS(deep); PINS(d.ldsb); PINS(d.m1);
    asm volatile("s_mov_b32 m0, %0" :: "s"(im0) : "memory");
    SB
    asm volatile("" :: "s"(idx), "s"(table), "s"(se_w1), "s"(se_w2), "s"(w_bi), "s"(w_deep1), "s"(w_cross), "s"(ws32));
    asm volatile("s_cmp_lg_u32 %3, 0\n\ts_cbranch_scc1 1f\n\tglobal_load_lds_dwordx4 %0, %2\n\tglobal_load_lds_dwordx4 %1, %2 offset:1024\n1:"
                 :: "v"(lane16), "v"(o2), "s"((const char*)idx + io), "s"(deep) : "memory", "scc");
    SB
    typedef const __attribute__((address_space(4))) unsigned long long* kap;
    kap ka = (kap)__builtin_amdgcn_kernarg_segment_ptr();
    PINS(ka);
    const char* wd2 = (const char*)ka[8];
    const gp out = (gp)ka[9];
    unsigned long long w1p;
    asm volatile("s_mov_b64 %0, %1" : "=s"(w1p) : "s"(w_deep1));
    d.rw1 = __builtin_amdgcn_make_buffer_rsrc((void*)w1p, (short)0, NF * 32768, 0x00020000);
    if (deep) {
        const unsigned ta = min(lane16, 80u) * 8u + (unsigned)((bid * 4 + cw) * 768);
        asm volatile("s_mov_b32 m0, %2\n\ts_nop 0\n\tbuffer_load_dword %0, %1, 0 offen lds" :: "v"(ta), "s"(d.rw1), "n"(OFF_JNK) : "memory");
    }
#define PTS 2101376
#define PTW 5
#define PTO 0
    if (w == PTW) asm volatile("s_mov_b32 m0, %2\n\ts_mov_b64 exec, %3\n\tglobal_load_lds_dword %0, %1\n\ts_mov_b64 exec, -1" :: "v"(lane16 * (PTS / 16) + (unsigned)(PTO)), "s"(table), "n"(OFF_JNK), "s"(PT == 64 ? ~0ull : (1ull << (PT & 63)) - 1) : "memory");
    prepB<0>(d);
    BARRIER;
    Cross x;
    x.c = cw; x.nq = nq; x.rho = rho; x.st = st; x.lane16 = lane16; x.ws = (char*)ws32; x.ldsb = d.ldsb;
    unsigned srcA, srcB, ib[4], sm[3], late = d.m1 == 0u ? 1u : (L1M >> nq) & 1u;
    const char* sp;
    u32x2 ia[NP], ic[NP];
    if (deep) {
        if (TGE == 1) if (cw == 3) tags(tag_addr(lane, d.unit, nq, st, xcd), ws32, OFF_TAG, nq ? 0x7ffffff0000ull : 0x3ffffff0e00ull);
        raw(d, lane16, cw, w_cross, ws32); set_m0<OFF_B>(d.ldsb); asm volatile("s_nop 0");
        issueB<0>(d); issueB<1>(d); issueB<2>(d); issueB<3>(d);
        SB
        if (TGE == 0) if (cw == 3) tags(tag_addr(lane, d.unit, nq, st, xcd), ws32, OFF_TAG, nq ? 0x7ffffff0000ull : 0x3ffffff0e00ull);
        if ((cw ^ 2) + nq == 0 && lane < 32) st16sc(lane16, (char*)ws32 + WS_SUM + st * 512, u32x4{0u, 0u, 0u, 0u});
        SB
        if (late == 0u) { prepB<1>(d); asm volatile("s_nop 0"); issueB<0>(d); issueB<1>(d); issueB<2>(d); issueB<3>(d); }
        SB
    } else {
        Cross& c = x;
        srcA = (unsigned)(((lane & 7) ^ g) * 16); srcB = srcA ^ 64u;
        unsigned ib0 = (unsigned)(OFF_IDX + cw * 2048 + (lane >> 3) * 104);
#pragma unroll
        for (int k = 0; k < 4; ++k) { ib[k] = ib0 + (unsigned)(((k + rho) & 3) * 8); PIN(ib[k]); c.tb[k] = (const char*)table - k * 1024; PINS(c.tb[k]); }
        PIN(srcA); PIN(srcB); PIN(ib0);
        const int k = (l15 & 1) | ((l15 >> 2) & 2), r = 4 * cw + g;
        sm[0] = (unsigned)(cw * 2048 + (lane >> 1) * 128 + nq * 32 + (lane & 1) * 16);
        sm[1] = cw == 3 ? (unsigned)(nq * 128) + ((lane16 >> 2) & 124u) : (unsigned)min(cw * 64 + lane, 168) * 4u;
        sm[2] = (unsigned)((r < 13 ? r : 12) * (NF * 2) + k * 16 + (k == 3 ? 0 : ((l15 + 2 * rho) & 6) * 2));
        PIN(sm[0]); PIN(sm[1]); PIN(sm[2]);
        set_m0<0>(c.ldsb);
        SB
        WAITCNT(0, 15);
        asm volatile("" ::: "memory");
        STAMP(1)
        TP(c.tq[0])
        ia[0] = LDS(const u32x2, ib0 + 96); ic[0] = LDS(const u32x2, ib0 + 96 + 832);
#pragma unroll
        for (int q = 1; q < 3; ++q) { ia[q] = LDS(const u32x2, ib[q - 1]); ic[q] = LDS(const u32x2, ib[q - 1] + 832); }
        SB
#define GATHER(q) issueA<0>(c, ia[q][0] * 128u + srcA); issueA<1>(c, ic[q][0] * 128u + srcB); issueA<2>(c, ia[q][1] * 128u + srcA); issueA<3>(c, ic[q][1] * 128u + srcB);
        GATHER(0)
        set_m0<SLOT>(c.ldsb);
        SB
        GATHER(1)
#define SM0 asm volatile("s_mov_b32 m0, %2\n\ts_mov_b32 exec_hi, 0\n\tglobal_load_lds_dwordx4 %0, %1\n\ts_mov_b32 exec_hi, -1" :: "v"(sm[0]), "s"(w_bi), "s"(OFF_WBI + cw * 512) : "memory");
#define SM1 dma4(sm[1], sp, OFF_SW1 + cw * 256); dma4(sm[2], se_w2, OFF_SW2 + cw * 256);
        sp = cw == 3 ? wd2 : (const char*)se_w1;
        if (SMP == 0) { SM0 SM1 }
        TP(c.tq[1])
    }
    int crs = deep ^ 1;
    PINS(crs); PINS(deep);
    if (crs == 0) {
        Deep& c = d;
        int lane;
        asm volatile("v_mbcnt_lo_u32_b32 %0, -1, 0\n\tv_mbcnt_hi_u32_b32 %0, -1, %0" : "=v"(lane));
        const int l15 = lane & 15, g = lane >> 4, tid = w * 64 + lane;
        c.c = cw; c.nq = nq; c.rho = rho; c.st = st; c.lane16 = (unsigned)lane * 16u; c.ws = (char*)ws32;
        c.m3 = (unsigned)(cw ^ 3); c.m4 = c.m3 | min(c.m1, (unsigned)nq);
        c.tad = tag_addr(lane, c.unit, nq, st, xcd);
        PIN(c.tad);
        SB
        mkboff(d, lane, cw, nq);
        {
            const unsigned a0 = (unsigned)(cw * 4096 + (cw & 1) * 2048 + l15 * 128 + ((4 * (cw >> 1) + g) ^ (l15 >> 1)) * 16);
            const int q = l15 >> 2, p = l15 & 3;
            const unsigned b0 = (unsigned)(OFF_B + cw * 4096 + (8 * g + q) * 128 + ((p >> 1) ^ (q & 2) ^ ((g & 1) << 2)) * 16 + (p & 1) * 8);
#pragma unroll
            for (int k = 0; k < 4; ++k) { c.ra[k] = a0 ^ (unsigned)(k * 4096); c.rb[k] = b0 ^ (unsigned)(k * 32); PIN(c.ra[k]); PIN(c.rb[k]); }
#pragma unroll
            for (int k = 0; k < 16; ++k) { c.ac[k >> 2][k & 3] = f32x4{0.f, 0.f, 0.f, 0.f}; PIN(c.ac[k >> 2][k & 3]); }
        }
        unsigned exw[4], fl = (c.lane16 >> 2) + (unsigned)(OFF_FL + cw * 256), fr = c.lane16 + OFF_FL, s2 = c.lane16 >> 3, s8 = c.lane16 >> 1;
#pragma unroll
        for (int k = 1; k < 4; ++k) { exw[k] = (c.lane16 >> 2) + (unsigned)(OFF_B + (cw ^ k) * 4096); PIN(exw[k]); }
        double big = 0x1.8p52;
        f32x4 va = {0.f, 0.f, 0.f, 0.f};
        float gc2 = 1.0f / TRC;
        c.w2[0][0] = OFF_WD2 + g * 16; c.w2[1][0] = OFF_WD2 + 64 + g * 16; PIN(c.w2[0]); PIN(c.w2[1]);
        const unsigned dl = (unsigned)((l15 >> 2) ^ (2 * (g & 1))), on = (l15 & 1) ? 0x3f800000u : 0x3f80u;
        u32x4 unit = {dl == 0u ? on : 0u, dl == 0u ? on : 0u, dl == 1u ? on : 0u, dl == 1u ? on : 0u};
        unsigned uix = (l15 & 2) ? 0xddddeeeeu : 0xdddd4444u;
        u32x2 pr[4][4];
#pragma unroll
        for (int k = 0; k < 4; ++k) { pr[0][k] = u32x2{0u, 0u}; PIN(pr[0][k]); }
        PIN(uix);
        PIN(fl); PIN(fr); PIN(s2); PIN(s8); PIN(big); PIN(gc0); PIN(gc1); PIN(gc2); PIN(va); PIN(unit);
        c.wsf = c.ws + WS_FRAG + nq * 65536 + cw * 4096;
        c.wsh = c.ws + WS_FRAG + 32768 + nq * 65536 + cw * 2048; c.ldsh = (unsigned)(cw * 2048);
        PINS(c.wsh); PINS(c.ldsh);
        c.pmask = cw != 3 ? 0ull : (c.m1 == 0u ? 0x1ffull : 0x100ull);
        unsigned long long wmask = (cw ^ 3) + nq ? 0ull : 0xe00ull;
        unsigned m5 = c.m3 | c.m1;
        PINS(wmask); PINS(m5);
        constexpr unsigned xb1 = xbits(1), xb2 = xbits(2), xb3 = xbits(3), xb4 = xbits(4);
        c.xa = c.m1 == 0u ? (cw == 3 ? xb4 : xb3) : cw != 3 ? 0u : nq ? xb1 : xb2;
        c.one = 1u; PIN(c.one);
        PINS(c.pmask); PINS(c.m3); PINS(c.m4); PINS(c.wsf); PINS(c.xa);
        produce<5>(c);
#define B1L if (late != 0u) { prepB<1>(c); asm volatile("s_nop 0"); issueB<0>(c); issueB<1>(c); issueB<2>(c); issueB<3>(c); }
        if (B1P == 0) B1L
        if (VT) { c.tb = c.m1 == 0u ? TBP : (TB >> (4 * nq)) & 15; PINS(c.tb); }
        TP(c.tq[0])
        BARRIER;
        TP(c.tq[1])
        if (B1P == 1) B1L
        prepB<2>(c); asm volatile("s_nop 0"); issueB<0>(c); issueB<1>(c); issueB<2>(c); issueB<3>(c);
        TP(c.tq[2])
        WAITX(8, 2, c.m1)
        c.tag = LDSV(OFF_TAG + (c.lane16 >> 2));
        PIN(c.tag);
        TP(c.tq[3])
        BARRIER;
        TP(c.tq[4])
        gp sums = (gp)c.ws + (unsigned)(WS_SUM + (s0 + 16 * cw) * 8), outp = out + (unsigned)((s0 + 16 * cw) * 2);
        PINS(sums); PINS(outp);
        DStep<0>::run(c);
        TS(c.ts[13])
        u32x2 px[4][4];
#define EX2(e, k) { px[e][k][0] = cvt_o(c.ac[e][k][0], c.ac[e][k][1]); px[e][k][1] = cvt_o(c.ac[e][k][2], c.ac[e][k][3]); SB }
#define EXO(e, k) (((e) - 1) * 1024 + ((k) & 1) * 512)
#define WT1(e, k, d) { asm volatile("ds_write_addtid_b32 %0 offset:%1" :: "v"(px[e][k][d]), "n"(((k) < 2 ? 62464 : 32768) + EXO(e, k) + (d) * 256) : "memory"); SB }
#define WT(e, k) { asm volatile("ds_write_addtid_b32 %0 offset:%2\n\tds_write_addtid_b32 %1 offset:%3" ST :: "v"(px[e][k][0]), "v"(px[e][k][1]), \
            "n"(((k) < 2 ? 62464 : 32768) + EXO(e, k)), "n"(((k) < 2 ? 62464 : 32768) + EXO(e, k) + 256) : "memory"); SB }
#define W6(k) WT(3, k) WT(2, k) WT(1, k)
#define RD(h) { if (!(h)) f = LDSV(fr + 1024); asm volatile("" ::: "memory"); SB \
            _Pragma("unroll") for (int k = 2 * (h); k < 2 * (h) + 2; ++k) _Pragma("unroll") for (int e = 3; e > 0; --e) _Pragma("unroll") for (int d = 0; d < 2; ++d) \
                pr[e][k][d] = LDS(const unsigned, exw[e] + ((h) ? 0 : 3 * SLOT) + EXO(e, k) + d * 256); SB }
#define CK(h) { if (__builtin_expect(__builtin_amdgcn_ballot_w64(f == 0u) != 0ull, 0)) { \
            _Pragma("unroll 1") for (int it = 0; it < (1 << 20); ++it) { RD(0) RD(1) if (__builtin_amdgcn_ballot_w64(f == 0u) == 0ull) break; } } SB }
#define AD(k) asm volatile("v_smfmac_f32_16x16x64_bf16 %0, %1, %2, %3" ST : "+v"(c.ac[0][k]) : "v"(unit), \
            "v"(u32x8{pr[0][k][0], pr[0][k][1], pr[1][k][0], pr[1][k][1], pr[2][k][0], pr[2][k][1], pr[3][k][0], pr[3][k][1]}), "v"(uix));
#define G4(m, k, ins) asm volatile(ins : m(y[4 * k]), m(y[4 * k + 1]), m(y[4 * k + 2]), m(y[4 * k + 3]) \
            : "v"(c.ac[0][k][0]), "v"(c.ac[0][k][1]), "v"(c.ac[0][k][2]), "v"(c.ac[0][k][3]), "v"(gc0), "v"(gc1), "v"(gc2));
#define GA_ "v_mul_f32 %0, %4, %4\n\tv_mul_f32 %1, %5, %5\n\tv_mul_f32 %2, %6, %6\n\tv_mul_f32 %3, %7, %7\n\t" \
            "v_fma_f32 %0, %0, %9, %8\n\tv_fma_f32 %1, %1, %9, %8\n\tv_fma_f32 %2, %2, %9, %8\n\tv_fma_f32 %3, %3, %9, %8\n\t"
#define GM_ "v_mul_f32 %0, %0, %4\n\tv_mul_f32 %1, %1, %5\n\tv_mul_f32 %2, %2, %6\n\tv_mul_f32 %3, %3, %7\n\t"
#define GB_ GM_ "v_exp_f32 %0, %0\n\tv_exp_f32 %1, %1\n\tv_exp_f32 %2, %2\n\tv_exp_f32 %3, %3\n\t"
#define GC_ "v_fma_f32 %0, %0, %10, %10\n\tv_fma_f32 %1, %1, %10, %10\n\tv_fma_f32 %2, %2, %10, %10\n\tv_fma_f32 %3, %3, %10, %10\n\t" \
            "v_rcp_f32 %0, %0\n\tv_rcp_f32 %1, %1\n\tv_rcp_f32 %2, %2\n\tv_rcp_f32 %3, %3\n\t" GM_
#define GELU(k) { G4("=&v", k, GA_ GB_ GC_) }
#define GEL1(k, I0) { G4("=&v", k, GA_) I0 G4("+v", k, GB_ GC_) }
#define GEL2(k, I1, I2) { G4("=&v", k, GA_ GB_) I1 G4("+v", k, GC_) }
#define WDOT(q) asm volatile("v_smfmac_f32_16x16x64_bf16 %0, %1, %2, %3 abid:1" : "+v"(va) : "v"(c.w2[q]), \
            "v"(f32x8{y[8 * q], y[8 * q + 1], y[8 * q + 2], y[8 * q + 3], y[8 * q + 4], y[8 * q + 5], y[8 * q + 6], y[8 * q + 7]}), "v"(uix));
#define PROTO { watch = __builtin_amdgcn_ballot_w64(tw == c.tag) & wmask; \
            if (__builtin_expect(m5 == 0u, 0)) { \
                const u32x4 e4 = LDS(const u32x4, fr + (OFF_NB - OFF_FL)), l4 = LDS(const u32x4, fr + (OFF_NB + 1024 - OFF_FL)); \
                watch |= __builtin_amdgcn_ballot_w64(e4[0] == l4[0]) | __builtin_amdgcn_ballot_w64(e4[1] == l4[1]) | __builtin_amdgcn_ballot_w64(e4[2] == l4[2]) | __builtin_amdgcn_ballot_w64(e4[3] == l4[3]); \
            } \
            PINS(watch); } SB
#define FLA { LDS(unsigned, fl + 1024) = fl; SB }
#define FLB { LDS(unsigned, fl + 3072) = fl; SB }
        unsigned f;
        float y[16];
        unsigned long long watch = 0ull;
#define FW 4
#define FLP 1
#define WSP 1
#define PD 3
#define PR(n) { if ((n) == PU) __builtin_amdgcn_s_setprio(2); if ((n) == PD || (n) == PD2) __builtin_amdgcn_s_setprio(0); if ((n) == PU2) __builtin_amdgcn_s_setprio(2); SB }
#define PU2 -1
#define PD2 -1
#define FH(n) if ((n) == FW) { WAITCNT(0, 15); SB set_m0<OFF_B + 3 * SLOT - 62464>(c.ldsb); LDS(unsigned, fl + 3072) = fl; SB }
#define FD(k) { set_m0<FOFF[2 + (k)]>(c.ldsb); asm volatile("s_nop 0"); dma16sco<0>(c.lane16, c.wsf + 32768 + (k) * 16384); dma16sco<1024>(c.lane16, c.wsf + 32768 + (k) * 16384); \
            dma16sco<2048>(c.lane16, c.wsf + 32768 + (k) * 16384); dma16sco<3072>(c.lane16, c.wsf + 32768 + (k) * 16384); SB }
#define FS(n) if ((n) == FLP) { WAITCNT(0, 15); SB LDS(unsigned, fl) = fl; SB publish(c, c.pmask); SB TE(c.tq[3]) }
#define PP 4
#define PQ(n) if ((n) == PP) { PROTO }
        PR(0) FH(0)
        TE(c.tq[0])
        mk<12, 3, 0>(c); mk<12, 2, 0>(c); mk<12, 1, 0>(c); FH(1) mk<12, 3, 1>(c); mk<12, 2, 1>(c); mk<12, 1, 1>(c); FH(2)
        mk<12, 3, 2>(c); mk<12, 2, 2>(c); mk<12, 1, 2>(c); FH(3) EX2(3, 0) mk<12, 3, 3>(c); EX2(2, 0) mk<12, 2, 3>(c); EX2(1, 0) mk<12, 1, 3>(c); FH(4) PR(1)
#define OWN 1
#define OW(k) if (OWN) mk<12, 0, k>(c);
        WT(3, 0) EX2(3, 1) OW(0) WT(2, 0) EX2(2, 1) WT(1, 0) EX2(1, 1) OW(1) WT(3, 1) EX2(3, 2) WT(2, 1) EX2(2, 2) OW(2) WT(1, 1) set_m0<OFF_B - 32768>(c.ldsb); EX2(1, 2) WT(3, 2) EX2(3, 3) OW(3) WT(2, 2) EX2(2, 3) WT(1, 2) EX2(1, 3)
        W6(3) FLA
        TE(c.tq[1])
        PR(2) dqs<14, NE, 12>(c); PR(3)
        const unsigned tw = LDSV(fl + (OFF_TAG + 512 - OFF_FL - 768));
        SB
        if (DEF11) m4<11, 0>(c);
        RD(0) RD(1)
        if (!OWN) m4<12, 0>(c);
        PQ(2)
        PR(4) CK(0)
        TE(c.tq[2])
        AD(0) AD(1) AD(2) AD(3) asm volatile("s_nop 4");
        PR(5) PQ(4) FS(0)
        if (FLP == 1) GEL2(0, FS(1), ) else GELU(0)
        FS(2) PR(6) PQ(5)
        if (FLP == 3) GEL2(1, FS(3) PR(7), ) else GELU(1)
        FS(4) PR(8)
#define WDP 1
        if (WDP == 0) GEL1(2, WDOT(0)) else GELU(2)
        GELU(3)
        if (WDP == 1) WDOT(0)
        asm volatile("s_nop 0");
        WDOT(1)
        asm volatile("s_nop 4" : "+v"(va));
        TE(c.tq[4])
        const float v = va[0];
        TS(c.ts[14])
        if (__builtin_amdgcn_inverse_ballot_w64(0xffffull)) deliver(c, v, 2 * nq, s2, s8, big, sums, outp);
        if (__builtin_expect(watch != 0ull, 0)) {
            const u32x4 e4 = LDS(const u32x4, OFF_NB + c.lane16), l4 = LDS(const u32x4, OFF_NB + 1024 + c.lane16);
            bool opn[4];
#pragma unroll
            for (int k = 0; k < 4; ++k) opn[k] = c.m1 == 0u && e4[k] == l4[k];
            bool own = nq == 0 && lane >= 9 && lane < 12 && LDSV(OFF_TAG + 512 + lane * 4) == c.tag;
#pragma unroll 1
            for (int it = 0; it < (1 << 22) && __builtin_amdgcn_ballot_w64(opn[0] || opn[1] || opn[2] || opn[3] || own) != 0ull; ++it) {
                bool chg = false;
#pragma unroll
                for (int k = 0; k < 4; ++k)
                    if (opn[k] && __hip_atomic_load(ws32 + lane * 4 + k, __ATOMIC_RELAXED, __HIP_MEMORY_SCOPE_AGENT) != e4[k]) { opn[k] = false; chg = true; }
                if (own && __hip_atomic_load(ws32 + (c.tad >> 2), __ATOMIC_RELAXED, __HIP_MEMORY_SCOPE_AGENT) != c.tag) { own = false; chg = true; }
                if (__builtin_amdgcn_ballot_w64(chg) != 0ull) publish(c, c.pmask);
                else __builtin_amdgcn_s_sleep(2);
            }
        }
        FINISH(c)
    } else {
        Cross& c = x;
#pragma unroll
        for (int q = 3; q < NP; ++q) { ia[q] = LDS(const u32x2, ib[(q - 1) & 3] + ((q - 1) >> 2) * 32); ic[q] = LDS(const u32x2, ib[(q - 1) & 3] + ((q - 1) >> 2) * 32 + 832); }
#pragma unroll
        for (int q = 3; q < NP; ++q) {
            c.ga[4 * q - 12] = ia[q][0] * 128u + srcA; c.ga[4 * q - 11] = ic[q][0] * 128u + srcB;
            c.ga[4 * q - 10] = ia[q][1] * 128u + srcA; c.ga[4 * q - 9] = ic[q][1] * 128u + srcB;
        }
#pragma unroll
        for (int k = 0; k < 40; ++k) PIN(c.ga[k]);
        {
            const int G = 2 * g + rho;
            const unsigned ea = (unsigned)(cw * 4096 + l15 * 128);
            c.re[0] = ea + (unsigned)((g ^ (l15 >> 1)) * 16); c.re[1] = c.re[0] ^ 64u;
            c.rx[0] = ea + (unsigned)(((2 * nq + (g >> 1)) ^ (l15 >> 1)) * 16); c.rx[1] = c.rx[0] + 8u;
            c.rx0[0] = c.rx[0] + (unsigned)((rho & 1) * 2048 + (G & 2) * 4); c.rx0[1] = c.rx[0] + (unsigned)((~rho & 1) * 2048 + ((G + 1) & 2) * 4);
            c.six = (l15 & 1) ? 0xddddeeeeu : 0xdddd4444u;
            c.pl = 0x05040100u; c.ph = 0x07060302u; c.xm[0] = 0x0000ffff0000ffffull; c.xm[1] = 0xffff0000ffff0000ull;
            PINS(c.pl); PINS(c.ph); PINS(c.xm[0]); PINS(c.xm[1]);
#pragma unroll
            for (int k = 0; k < 4; ++k) { const unsigned o = (l15 >> 1) == 2 * k + (g >> 1) ? 0x3f803f80u : 0u; c.on[k] = u32x4{o, o, o, o}; PIN(c.on[k]); }
            PIN(c.re[0]); PIN(c.re[1]); PIN(c.rx[0]); PIN(c.rx[1]); PIN(c.rx0[0]); PIN(c.rx0[1]); PIN(c.six);
            zclear<3>(c); zclear<0>(c);
        }
        SB
        asm volatile("s_mov_b64 exec, 0\n\ts_cmp_eq_u32 %0, 0\n\ts_cbranch_scc1 .Lst1\n\ts_cmp_eq_u32 %0, 1\n\ts_cbranch_scc1 .Lst2\n\t"
                     "s_cmp_eq_u32 %0, 2\n\ts_cbranch_scc1 .Lst3\n\ts_branch .Lst4\n.Lback:\n\ts_mov_b64 exec, -1" :: "s"(cw) : "scc");
        BARRIER;
        if (SMP) SM0
        if (SMP == 1) { SM1 }
        set_m0<2 * SLOT>(c.ldsb);
        SB
        GATHER(2)
        if (SMP == 2) { SM1 }
        TP(c.tq[2])
        {
            const int q = (lane >> 2) & 3, p = lane & 3;
            c.wad[0] = (unsigned)(OFF_WBI + (8 * g + q) * 32 + p * 8); c.wad[1] = (unsigned)(OFF_WBI + (8 * g + q) * 32 + ((p + rho) & 3) * 8);
            PIN(c.wad[0]); PIN(c.wad[1]);
        }
        gp sums = (gp)c.ws + (unsigned)(WS_SUM + (s0 + 16 * cw) * 8), outp = out + (unsigned)((s0 + 16 * cw) * 2);
        PINS(sums); PINS(outp);
        c.wsf = c.ws + WS_FRAG + nq * 65536 + cw * 4096; PINS(c.wsf);
        TP(c.tq[3])
        WAITCNT(11, 0); BARRIER;
        TP(c.tq[4])
        __builtin_amdgcn_s_setprio(1);
        CStep<0>::run(c);
        TS(c.ts[11])
        u32x4 bq[4];
        u32x4 (&bp)[4] = c.bp;
        f32x2 T[6][8];
#define SPLIT(s0, s1, i0, i1) { _Pragma("unroll") for (int s = s0; s < s1; ++s) _Pragma("unroll") for (int i_ = i0; i_ < i1; ++i_) { \
            const f32x4 x = c.ew[2 * (1 + 4 * (s >> 1) + (i_ >> 1)) + (s & 1)]; T[s][i_] = f32x2{x[2 * (i_ & 1)], x[2 * (i_ & 1) + 1]}; } }
#define CB(B, i, h, k) { B[(2 * (i) + (h)) & 3][k] = cvt_o(T[2 * (k)][i][h], T[2 * (k) + 1][i][h]); }
#define SC(i, k) { asm volatile("v_pk_mul_f32 %0, %1, %0 op_sel_hi:[0,1]" : "+v"(T[2 * (k)][i]) : "v"(ao[k])); asm volatile("v_pk_mul_f32 %0, %1, %0 op_sel:[1,0]" : "+v"(T[2 * (k) + 1][i]) : "v"(ao[k])); }
        SPLIT(0, 4, 0, 8)
        SB
#define FLZ 8
#define SW3 6
#define SWA 4
#define FL1 3072
#define FLR 25
#define FLW 27
        STAMP(2)
        FI(0)
        if (!C11) { cmm<11, 0>(c); cmm<11, 1>(c); cmm<11, 2>(c); cmm<11, 3>(c); cmm<11, 4>(c); cmm<11, 5>(c); }
        cmm<12, 0>(c); cmm<12, 1>(c); cmm<12, 2>(c); cmm<12, 3>(c);
        rdX<12>(c);
        FI(0x42)
        zpack<2>(c);
        cmm<12, 4>(c); cmm<12, 5>(c);
        const f32x4 hp = mf0(c.s1, c.zq);
        TS(c.ts[12])
        FI(0x50) TH(0)
        asm volatile("s_nop 4");
        swl<8, 8 + SWA>(c);
        FI(1) TH(1)
        float hq[4];
        asm volatile("v_mul_f32 %0, %4, %4\n\tv_mul_f32 %1, %5, %5\n\tv_mul_f32 %2, %6, %6\n\tv_mul_f32 %3, %7, %7\n\t"
                     "v_fma_f32 %0, %0, %9, %8\n\tv_fma_f32 %1, %1, %9, %8\n\tv_fma_f32 %2, %2, %9, %8\n\tv_fma_f32 %3, %3, %9, %8\n\t"
                     "v_mul_f32 %0, %0, %4\n\tv_mul_f32 %1, %1, %5\n\tv_mul_f32 %2, %2, %6\n\tv_mul_f32 %3, %3, %7\n\t"
                     "v_exp_f32 %0, %0\n\tv_exp_f32 %1, %1\n\tv_exp_f32 %2, %2\n\tv_exp_f32 %3, %3\n\t"
                     "v_fma_f32 %0, %0, %10, %10\n\tv_fma_f32 %1, %1, %10, %10\n\tv_fma_f32 %2, %2, %10, %10\n\tv_fma_f32 %3, %3, %10, %10\n\t"
                     "v_rcp_f32 %0, %0\n\tv_rcp_f32 %1, %1\n\tv_rcp_f32 %2, %2\n\tv_rcp_f32 %3, %3\n\t"
                     "v_mul_f32 %0, %0, %4\n\tv_mul_f32 %1, %1, %5\n\tv_mul_f32 %2, %2, %6\n\tv_mul_f32 %3, %3, %7\n\t"
                     "v_cvt_pk_bf16_f32 %0, %0, %1\n\tv_cvt_pk_bf16_f32 %1, %2, %3"
                     : "=&v"(hq[0]), "=&v"(hq[1]), "=&v"(hq[2]), "=&v"(hq[3]) : "v"(hp[0]), "v"(hp[1]), "v"(hp[2]), "v"(hp[3]), "v"(c.k0), "v"(c.k1), "v"(c.k2));
        FI(2) TH(2)
        const f32x2 hb = {hq[0], hq[1]};
        swl<8 + SWA, 10 + SWA>(c);
        f32x4 av[2];
        asm volatile("v_mfma_f32_16x16x16_bf16 %0, %1, %2, 0" : "=&v"(av[0]) : "v"(c.wa[0]), "v"(hb));
        asm volatile("v_mfma_f32_16x16x16_bf16 %0, %1, %2, 0" : "=&v"(av[1]) : "v"(c.wa[1]), "v"(hb));
        FI(3)
        swl<10 + SWA, 18 + SWA>(c);
        TH(3) FI(4)
        TS(c.ts[13])
        SB
        float as[8];
#pragma unroll
        for (int r = 0; r < 8; ++r) as[r] = exp_o(av[r >> 2][r & 3]);
        asm volatile("v_add_f32 %0, 1.0, %0\n\tv_add_f32 %1, 1.0, %1\n\tv_add_f32 %2, 1.0, %2\n\tv_add_f32 %3, 1.0, %3\n\tv_add_f32 %4, 1.0, %4\n\tv_add_f32 %5, 1.0, %5\n\tv_add_f32 %6, 1.0, %6\n\tv_add_f32 %7, 1.0, %7\n\t"
                     "v_rcp_f32 %0, %0\n\tv_rcp_f32 %1, %1\n\tv_rcp_f32 %2, %2\n\tv_rcp_f32 %3, %3\n\tv_rcp_f32 %4, %4\n\tv_rcp_f32 %5, %5\n\tv_rcp_f32 %6, %6\n\tv_rcp_f32 %7, %7"
                     : "+v"(as[0]), "+v"(as[1]), "+v"(as[2]), "+v"(as[3]), "+v"(as[4]), "+v"(as[5]), "+v"(as[6]), "+v"(as[7]));
        FI(5) TH(4)
        unsigned flg = LDSV(c.wf[1] + (OFF_FL + 2048 - OFF_B - SLOT));
        SB
        swl<18 + SWA, 18 + SWA + SW3>(c);
        FI(6)
        TG(c.tq[0]) TF(0)
        SB
        WAITCNT(63, 0);
        wait_set(flg, c.wf[1] + (OFF_FL + 2048 - OFF_B - SLOT));
        SB TF(1)
#define WSEQ(n) LDS(const u32x4, c.wf[((n) >> 4) & 1] + ((n) >> 5) * SLOT + (((n) & 15) ^ ((~(n) >> 2) & 1)) * 1024)
        u32x4 wq[64];
#pragma unroll
        for (int r = 0; r < WPRE; ++r) wq[r] = WSEQ(r);
        SB TF(2)
        swl<18 + SWA + SW3, 32>(c);
        SPLIT(4, 6, 0, 8)
        CB(bp, 0, 0, 2) CB(bp, 0, 1, 2)
        f32x2 ao[4] = {{as[0], as[1]}, {as[2], as[3]}, {as[4], as[5]}, {as[6], as[7]}};
        {
            f32x2 m[4] = {{c.ew[0][0], c.ew[0][1]}, {c.ew[1][0], c.ew[1][1]}, {c.ew[0][2], c.ew[0][3]}, {c.ew[1][2], c.ew[1][3]}};
#pragma unroll
            for (int r = 0; r < 4; r += 2) {
                asm volatile("v_pk_mul_f32 %0, %1, %0 op_sel_hi:[0,1]" : "+v"(m[r]) : "v"(ao[3]));
                asm volatile("v_pk_mul_f32 %0, %1, %0 op_sel:[1,0]" : "+v"(m[r + 1]) : "v"(ao[3]));
            }
            SC(0, 0) SC(0, 1) SC(0, 2) TF(3)
#pragma unroll
            for (int r = 0; r < 4; ++r) bq[r][3] = cvt_o(m[r & 2][r & 1], m[(r & 2) + 1][r & 1]);
        }
        ao[3][0] = sel(c.am, as[7], as[6]);
        PIN(ao[3]);
        SB
        FI(8)
        TG(c.tq[1]) TF(4)
        f32x4 u[2][4];
        float S1[7][16];
#define SQ(n) (((n) >> 1) + 4 * ((n) & 1))
#define FM(n, h, k) if ((k) < NJP) { asm volatile("v_pk_fma_f32 %0, %1, %2, %3" ST : "+v"(T[2 * (k) + (h)][SQ(n)]) : "v"(f32x2{u[h][2 + ((k) >> 1)][2 * ((k) & 1)], u[h][2 + ((k) >> 1)][2 * ((k) & 1) + 1]}), "v"(ao[k]), "v"(f32x2{u[h][(k) >> 1][2 * ((k) & 1)], u[h][(k) >> 1][2 * ((k) & 1) + 1]})); }
#define F1(n, h, s) if ((s) >= 2 * NJP) { float r_; \
            if ((s) < 6) { r_ = T[(s) % 6][SQ(n)][h]; asm volatile("v_fma_f32 %0, %1, %2, %3" : "+v"(r_) : "v"(u[h][2 + ((s) >> 2)][(s) & 3]), "v"(ao[(s) >> 1][(s) & 1]), "v"(u[h][(s) >> 2][(s) & 3])); } \
            else { asm volatile("v_fma_f32 %0, %1, %2, %3" : "=v"(r_) : "v"(u[h][3][2]), "v"(ao[3][0]), "v"(u[h][1][2]), "v"(u[h][3][3]), "v"(u[h][1][3])); } S1[s][2 * SQ(n) + (h)] = r_; }
#define TT8(s, q) f32x8{T[s][2 * (q)][0], T[s][2 * (q)][1], T[s][2 * (q) + 1][0], T[s][2 * (q) + 1][1], T[s][2 * (q) + 4][0], T[s][2 * (q) + 4][1], T[s][2 * (q) + 5][0], T[s][2 * (q) + 5][1]}
#define SS8(s, q) f32x8{S1[s][4 * (q)], S1[s][4 * (q) + 1], S1[s][4 * (q) + 2], S1[s][4 * (q) + 3], S1[s][4 * (q) + 8], S1[s][4 * (q) + 9], S1[s][4 * (q) + 10], S1[s][4 * (q) + 11]}
#define DOT1(a_, b_) asm volatile("v_smfmac_f32_16x16x64_bf16 %0, %1, %2, %3 abid:1" : "+v"(c.dg) : "v"(a_), "v"(b_), "v"(c.six));
#define DOT(q, s) { if ((s) == 6) { DOT1(c.X[0], (SS8(6, q))) } else if ((s) < 2 * NJP) { DOT1(c.Y[(4 * ((s) >> 1) + ((s) & 1) + 2 * (q)) % 12], (TT8((s) % 6, q))) } \
            else { DOT1(c.X[1 + 4 * (((s) % 6) >> 1) + ((s) & 1) + 2 * (q)], (SS8(s, q))) } }
#define DPOS 0x0600000504070301ull
#define DPOS2 0x20
#define DP(n, j) if (n >= 4 && n < 6) { constexpr int s_ = (((DPOS | DPOS2) >> (4 * ((8 * n + j) & 15))) & 15); if (s_) DOT(0, (s_ + 6) % 7) }
#define XQ(n, j) (((n) & 3) * 4 + ((j) > 5 ? 4 : (j) > 3 ? 2 : (j) - 2))
#define PJ(n, j) { constexpr int i = SQ(n), i1 = SQ((n + 1) & 7), np = (n + 7) & 7, z = 8 * n + j, h = j >> 2; \
                constexpr bool nx = n < 7, pv = n >= 1, al = nx && n != 1; \
                SB \
                if (z == FLW + 4 * NE - 16) { wait_set(flg, c.wf[1] + (OFF_FL - OFF_B - SLOT)); SB } \
                if (z == FLW - 16) { wait_set(flg, c.wf[1] + (OFF_FL + FL1 - OFF_B - SLOT)); SB } \
                if (!(j & 1)) { if (z + WPRE <= 64) WAITCNT(63, WPRE - 2); SB } \
                if (j & 2) u[h][(j & 3) ^ h ^ 1] = mf0(wq[z], bq[(2 * i + h) & 3]); else u[h][(j & 3) ^ h ^ 1] = mf0(wq[z], bp[(2 * i + h) & 3]); \
                SB \
                if (z == FLR + 4 * NE - 16) flg = LDSV(c.wf[1] + (OFF_FL - OFF_B - SLOT)); \
                if (z == FLR - 16) flg = LDSV(c.wf[1] + (OFF_FL + FL1 - OFF_B - SLOT)); \
                if (z + WPRE < 64) wq[(z + WPRE) & 63] = WSEQ(z + WPRE); \
                SB \
                if (j == 0) { CB(bq, i, 0, 0) DP(n, 0) CB(bq, i, 0, 1) } \
                if (j == 1) { CB(bq, i, 0, 2) DP(n, 1) CB(bq, i, 1, 0) CB(bq, i, 1, 1) } \
                if (n >= 4 && (j == 2 || j == 3 || j == 5)) xpair<2, XQ(n, j), XQ(n, j + 1)>(c); \
                if (j == 2) { if (pv) F1(np, 1, 4) CB(bq, i, 1, 2) DP(n, 2) if (pv) { F1(np, 1, 0) F1(np, 1, 1) F1(np, 1, 2) F1(np, 1, 3) } } \
                if (j == 3) { if (pv) F1(np, 1, 5) DP(n, 3) if (pv) F1(np, 1, 6) } \
                if (j == 4) { if (al) CB(bp, i1, 0, 0) DP(n, 4) if (al) CB(bp, i1, 0, 1) } \
                if (j == 5) { F1(n, 0, 4) DP(n, 5) F1(n, 0, 5) F1(n, 0, 6) } \
                if (j == 6) { if (nx) CB(bp, i1, 0, 2) DP(n, 6) if (al) CB(bp, i1, 1, 0) F1(n, 0, 0) F1(n, 0, 1) } \
                if (j == 7) { \
                    F1(n, 0, 2) F1(n, 0, 3) \
                    if (al) CB(bp, i1, 1, 1) \
                    if (nx) CB(bp, i1, 1, 2) \
                    FM(n, 0, 0) FM(n, 0, 1) FM(n, 0, 2) \
                    if (nx) { SC(i1, 0) SC(i1, 1) SC(i1, 2) } else asm volatile("s_nop 3"); \
                    FM(n, 1, 0) FM(n, 1, 1) FM(n, 1, 2) \
                    if (n == 3) { f32x2 a_, b_; asm volatile("v_mov_b64 %0, 0\n\tv_mov_b64 %1, 0" : "=v"(a_), "=v"(b_)); c.dg = f32x4{a_[0], a_[1], b_[0], b_[1]}; } \
                } }
        static_assert(FLW + WPRE <= 32 && FLZ <= FLR && FLR < FLW, "");
#define PN(n) TI(n) PJ(n, 0) PJ(n, 1) PJ(n, 2) PJ(n, 3) PJ(n, 4) PJ(n, 5) PJ(n, 6) PJ(n, 7)
        PN(0)
        PN(1)
        TG(c.tq[2])
        PN(2)
        PN(3)
        TG(c.tq[3])
        PN(4) PN(5) PN(6) PN(7) TI(8)
        unsigned s2;
        asm volatile("v_mbcnt_lo_u32_b32 %0, -1, 0\n\tv_mbcnt_hi_u32_b32 %0, -1, %0" : "=v"(s2));
        F1(7, 1, 0) F1(7, 1, 1) F1(7, 1, 2) F1(7, 1, 3)
        DOT(1, 0) F1(7, 1, 4) F1(7, 1, 5) DOT(1, 2) F1(7, 1, 6) DOT(1, 1)
        asm volatile("v_lshlrev_b32 %0, 1, %0\n\tv_and_b32 %0, 30, %0" : "+v"(s2));
        DOT(1, 3) DOT(1, 4)
        unsigned s8 = s2 * 4u;
        double big = 0x1.8p52;
        PIN(s8); PIN(big);
        SB
        DOT(1, 5) DOT(1, 6)
        const f32x4 dg = c.dg;
        float v, v2;
        asm volatile("s_mov_b64 vcc, %6\n\ts_nop 6\n\tv_cndmask_b32 %0, %2, %3, vcc\n\tv_cndmask_b32 %1, %4, %5, vcc\n\tv_cndmask_b32 %0, %0, %1, %7"
                     : "=&v"(v), "=&v"(v2) : "v"(dg[0]), "v"(dg[1]), "v"(dg[2]), "v"(dg[3]), "s"(0xaaaaaaaaaaaaaaaaull), "s"(0xccccccccccccccccull) : "vcc");
        TG(c.tq[4])
        STAMP(4)
        TS(c.ts[14])
        if (__builtin_amdgcn_inverse_ballot_w64(0xf0000f0000f0000full)) deliver(c, v, 2 * nq + 1, s2, s8, big, sums, outp);
        const int ln = __builtin_amdgcn_mbcnt_hi(~0u, __builtin_amdgcn_mbcnt_lo(~0u, 0u));
        const int lane = ln, tid = w * 64 + ln;
        (void)lane; (void)tid;
        FINISH(c)
    }
}

extern "C" void kernel_launch(void* const* d_in, const int* in_sizes, int n_in,
                              void* d_out, int out_size, void* d_ws, size_t ws_size,
                              hipStream_t stream) {
    const int* idx = (const int*)d_in[0];
    const uint16_t* table = (const uint16_t*)d_in[1];
    const uint16_t* se_w1 = (const uint16_t*)d_in[2];
    const uint16_t* se_w2 = (const uint16_t*)d_in[3];
    const uint16_t* w_bi = (const uint16_t*)d_in[4];
    const uint16_t* w_cross = (const uint16_t*)d_in[5];
    const uint16_t* w_deep1 = (const uint16_t*)d_in[6];
    const uint16_t* w_deep2 = (const uint16_t*)d_in[7];
    (void)hipFuncSetAttribute((const void*)k_main, hipFuncAttributeMaxDynamicSharedMemorySize, LDS_T);
    k_main<<<256, 512, LDS_T, stream>>>(idx, table, w_bi, w_deep1, w_cross, (unsigned*)d_ws, se_w1, se_w2, w_deep2, (__hip_bfloat16*)d_out, (unsigned long long*)((char*)d_ws + (128ull << 20)));
}
```

```cpp
#include <hip/hip_runtime.h>
#include <hip/hip_bf16.h>
#include <stdint.h>

#define EXM 1
#define PU 0
#define WSX 0
#define TB 0x2344
#define TBP 4
#define SMP 0
#define PRO 4
#define B1P 0
#define L1M 8
#define TGE 0
#define PT 7
#define NF 26
#define TRC 1.0028f
#define NP 13
#define PROD_CLASS(slot) ((((slot) & 3) == 1) && (((slot) >> 2) & 1) == 0)
#define STAMP(k)
#define TS(x)
#define TSP(P)
#define TX(k)
#define TP(x)
#define TEM 0x3ff
#define TE(x)
#define TI(n)
#define TF(k)
#define TH(k)
#define TG(x) TE(x)
using bf16x8 = __attribute__((ext_vector_type(8))) short;
using f32x4 = __attribute__((ext_vector_type(4))) float;
using f32x8 = __attribute__((ext_vector_type(8))) float;
using f32x2 = __attribute__((ext_vector_type(2))) float;
using s16x4 = __attribute__((ext_vector_type(4))) short;
using u32x4 = __attribute__((ext_vector_type(4))) unsigned;
using u32x8 = __attribute__((ext_vector_type(8))) unsigned;
using u32x2 = __attribute__((ext_vector_type(2))) unsigned;
using bf2 = __attribute__((ext_vector_type(2))) __bf16;
#define ST "\n\tSTUB"
#define ST_ "STUB\n\t"
#define PIN(x) asm volatile("" : "+v"(x))
#define PINS(x) asm volatile("" : "+s"(x))
#define SB __builtin_amdgcn_sched_barrier(0);
#define CAT8(a, b) __builtin_shufflevector((a), (b), 0, 1, 2, 3, 4, 5, 6, 7)

#define SLOT 16384
#define OFF_B 65536
#define OFF_FL 131072
#define OFF_WBI (OFF_FL + 4096)
#define OFF_SW1 (OFF_WBI + 2048)
#define OFF_SW2 (OFF_SW1 + 1024)
#define OFF_WD2 (PRO & 2 ? OFF_SW2 + 1024 : OFF_SW1 + 768)
#define OFF_TAG (OFF_SW2 + 1280)
#define OFF_NB (OFF_TAG + 768)
#define OFF_JNK (OFF_NB + 2048)
#define OFF_IDX (OFF_JNK + 2048)
#define OFF_RAW (OFF_IDX + 8192)
#define LDS_T (OFF_IDX + 16384)
constexpr int FOFF[4] = {SLOT, OFF_B + SLOT, 2 * SLOT, OFF_B + 2 * SLOT};
#define P_FLAG 3
#define P_POLL 6
#define P_ST 3
#define P_CHK 9
#define P_STAT 8

#define WS_FLAG 1024
#define WS_SUM 4096
#define WS_SIDE 65536
#define WS_FRAG (1 << 20)

#define LDS(T, a) (*(__attribute__((address_space(3))) T*)(size_t)(a))
#define LDSV(a) (*(volatile const __attribute__((address_space(3))) unsigned*)(size_t)(a))
__device__ __forceinline__ unsigned pk_bf16(float lo, float hi) {
    const f32x2 v = {lo, hi};
    return __builtin_bit_cast(unsigned, __builtin_convertvector(v, bf2));
}
__device__ __forceinline__ unsigned cvt_o(float lo, float hi) { unsigned r; asm volatile("v_cvt_pk_bf16_f32 %0, %1, %2" ST : "=v"(r) : "v"(lo), "v"(hi)); return r; }
__device__ __forceinline__ float exp_o(float a) { float r; asm volatile("v_exp_f32 %0, %1" : "=v"(r) : "v"(a)); return r; }
template <typename A, typename B> __device__ __forceinline__ f32x4 mf0(const A& a, const B& b) { f32x4 d; asm volatile("v_mfma_f32_16x16x32_bf16 %0, %1, %2, 0" ST : "=v"(d) : "v"(a), "v"(b)); return d; }
template <typename A, typename B> __device__ __forceinline__ void mfa(f32x4& d, const A& a, const B& b) { asm volatile("v_mfma_f32_16x16x32_bf16 %0, %1, %2, %0" ST : "+v"(d) : "v"(a), "v"(b)); }
__device__ __forceinline__ float lo_f(unsigned x) { return __uint_as_float(x << 16); }
__device__ __forceinline__ float hi_f(unsigned x) { return __uint_as_float(x & 0xffff0000u); }
__device__ __forceinline__ s16x4 tr_read(unsigned a) {
    typedef short v4s __attribute__((ext_vector_type(4)));
    return __builtin_amdgcn_ds_read_tr16_b64_v4i16((__attribute__((address_space(3))) v4s*)(size_t)a);
}
#define DMA(name, ins) __device__ __forceinline__ void name(unsigned voff, const void* sbase, unsigned ldsaddr) { \
    asm volatile("s_mov_b32 m0, %2\n\ts_nop 0\n\t" ins :: "v"(voff), "s"(sbase), "s"(ldsaddr) : "memory"); }
DMA(dma16, "global_load_lds_dwordx4 %0, %1") DMA(dma4, "global_load_lds_dword %0, %1")
DMA(dma16sc, "global_load_lds_dwordx4 %0, %1 sc1") DMA(dma4sc, "global_load_lds_dword %0, %1 sc1")
__device__ __forceinline__ void st16sc(unsigned voff, void* sbase, u32x4 v) {
    asm volatile("global_store_dwordx4 %0, %1, %2 sc1\n\ts_nop 1" :: "v"(voff), "v"(v), "s"(sbase) : "memory");
}
template <int O> __device__ __forceinline__ void set_m0(unsigned base) { asm volatile("s_add_u32 m0, %0, %1" :: "s"(base), "n"(O) : "memory", "scc"); }
template <int O> __device__ __forceinline__ void bdma16o(unsigned voff, __amdgpu_buffer_rsrc_t rs, unsigned soff) {
    asm volatile("buffer_load_dwordx4 %0, %1, %2 offen offset:%3 lds" :: "v"(voff), "s"(rs), "s"(soff), "n"(O) : "memory");
}
template <int O> __device__ __forceinline__ void dma16o(unsigned voff, const void* sbase) {
    asm volatile("global_load_lds_dwordx4 %0, %1 offset:%2" :: "v"(voff), "s"(sbase), "n"(O) : "memory");
}
template <int O> __device__ __forceinline__ void dma16sco(unsigned voff, const void* sbase) {
    asm volatile("global_load_lds_dwordx4 %0, %1 offset:%2 sc1" ST :: "v"(voff), "s"(sbase), "n"(O) : "memory");
}
#define WAITCNT(vm, lgkm) __builtin_amdgcn_s_waitcnt(((vm) & 15) | (((vm) >> 4) << 14) | 0x70 | ((lgkm) << 8))
#define WAITX(vm, x, m) { WAITCNT((vm) + (x), 0); asm volatile("s_cmp_eq_u32 %0, 0\n\ts_cbranch_scc1 1f\n\ts_waitcnt vmcnt(%1)\n1:" :: "s"(m), "n"(vm) : "memory", "scc"); }
#define BARRIER asm volatile("s_barrier" ST ::: "memory")
extern "C" __device__ void snop(short) __asm("llvm.amdgcn.s.nop");
#define USE(x) asm volatile("" :: "v"(x))
__device__ __forceinline__ float sum32(float v) { const u32x2 p = __builtin_amdgcn_permlane32_swap(__float_as_uint(v), __float_as_uint(v), false, false); return __uint_as_float(p[0]) + __uint_as_float(p[1]); }
__device__ __forceinline__ float sum16(float v) { const u32x2 p = __builtin_amdgcn_permlane16_swap(__float_as_uint(v), __float_as_uint(v), false, false); return __uint_as_float(p[0]) + __uint_as_float(p[1]); }
__device__ __forceinline__ float sel(unsigned m, float a, float b) {
    return __uint_as_float(__builtin_amdgcn_bitop3_b32(m, __float_as_uint(a), __float_as_uint(b), 0xca));
}
#define SLOW(b)
template <int b> __device__ __forceinline__ void wait_set(unsigned f, unsigned a, unsigned& slow) {
    if (__builtin_expect(__builtin_amdgcn_ballot_w64(f == 0u) != 0ull, (WSX >> b) & 1)) {
        SLOW(b)
        while (__builtin_amdgcn_ballot_w64(LDSV(a) == 0u) != 0ull) { }
    }
    asm volatile("" ::: "memory");
}

struct Com {
    char* ws;
    unsigned lane16, ldsb, slow = 0;
    int c, nq, rho, st;
};
typedef __attribute__((address_space(1))) char* gp;
struct Fin { unsigned kr, kl; double c0; unsigned long long last, lim; };
__device__ __forceinline__ void fin(Fin& k) {
    k.kr = 0x7d000000u; k.kl = 0xde000000u; k.c0 = -0x1.1p11; k.last = 0x1c00000000000000ull; k.lim = 0x1c04000000000000ull;
    PINS(k.kr); PINS(k.kl); PINS(k.c0); PINS(k.last); PINS(k.lim);
}
__device__ __forceinline__ void deliver(const Com& c, float v, double big, int part, unsigned s8, Fin k, gp sums, gp outp) {
    double mine = __builtin_fma((double)v, 0x1p-969, big);
    const bool far = (__float_as_uint(v) << 1) + k.kr < k.kl;
    if (__builtin_expect(__builtin_amdgcn_ballot_w64(far) != 0ull, 0)) {
        if (far) {
            mine = __builtin_bit_cast(double, u32x2{0u, 0x04004000u + (0x40000u << part)});
            __hip_atomic_store((__attribute__((address_space(1))) float*)((gp)c.ws + (WS_SIDE + c.st * 2048 + c.c * 64 + part * 256) + (s8 >> 1)), v, __ATOMIC_RELAXED, __HIP_MEMORY_SCOPE_AGENT);
        }
        asm volatile("s_waitcnt vmcnt(0)" ::: "memory");
        k.lim = 0ull;
    }
    SB
    const unsigned long long ret = __hip_atomic_fetch_add((__attribute__((address_space(1))) unsigned long long*)(sums + s8), __builtin_bit_cast(unsigned long long, mine), __ATOMIC_RELAXED, __HIP_MEMORY_SCOPE_AGENT);
    SB
    double own = __builtin_fma(mine, 0x1p969, k.c0);
    unsigned s2 = s8 >> 2;
    PIN(own); PIN(s2);
    SB
    own = __builtin_fma(__builtin_bit_cast(double, ret), 0x1p585, own);
    SB
    asm volatile("v_cmpx_le_u64 vcc, %0, %1" :: "s"(k.last), "v"(ret), "v"(own) : "vcc");
    SB
    float r = (float)own;
    asm volatile("" :: "v"(r));
    SB
    if (__builtin_expect(__builtin_amdgcn_ballot_w64(ret >= k.lim) != 0ull, 0)) {
        const unsigned long long tot = ret + __builtin_bit_cast(unsigned long long, mine);
        const unsigned th = (unsigned)(tot >> 32);
        r = (float)__builtin_fma(__builtin_bit_cast(double, tot & 0xfc03ffffffffffffull), 0x1p521, -0x1.2p10);
#pragma unroll 1
        for (int q = 0; q < 8; ++q)
            if ((th >> (18 + q)) & 1u) r += __hip_atomic_load((__attribute__((address_space(1))) float*)((gp)c.ws + (WS_SIDE + c.st * 2048 + c.c * 64 + q * 256) + (s8 >> 1)), __ATOMIC_RELAXED, __HIP_MEMORY_SCOPE_AGENT);
    }
    *(__attribute__((address_space(1))) uint16_t*)(outp + s2) = (uint16_t)pk_bf16(r, r);
}
#define BIG 0x1.04p-959

struct Deep : Com {
    __amdgpu_buffer_rsrc_t rw1;
    const char* wsf[2]; const char* wsh[2];
    unsigned ldsh;
    unsigned sG[4], sB, tb;
    unsigned m1, m3, m4;
    int unit;
    unsigned long long pmask;
    unsigned xa;
    unsigned one;
    unsigned tad, tag, pl;
    unsigned boff[4], ra[4], rb[4];
    u32x4 w2[2];
    f32x4 ac[4][4];
    bf16x8 fa[2][4];
    s16x4 lo[2][4], hi[2][4];
};
template <int Q> __device__ __forceinline__ void prepB(Deep& c) {
    set_m0<OFF_B + (Q & 3) * SLOT>(c.ldsb);
    SB
    if constexpr (Q == 0) c.sB = 24 * 32768; else c.sB = c.sG[(Q - 1) & 3] + ((Q - 1) >> 2) * (8 * 32768);
    asm volatile("" :: "s"(c.sB));
    SB
}
template <int I> __device__ __forceinline__ void issueB(Deep& c) { bdma16o<I * 1024>(c.boff[I], c.rw1, c.sB); }
__device__ __forceinline__ void mkboff(Deep& d, int lane, int cw, int nq) {
    const unsigned b0 = (unsigned)(((cw & 1) * 64 + (cw >> 1) * 32 + (lane >> 3)) * 512 + nq * 128 + ((lane & 7) ^ ((lane >> 3) & 2)) * 16);
    d.boff[0] = b0; d.boff[1] = (b0 ^ 64u) + 3072; d.boff[2] = b0 + 6144; d.boff[3] = (b0 ^ 64u) + 9216;
#pragma unroll
    for (int k = 0; k < 4; ++k) PIN(d.boff[k]);
}
template <int P, int N> __device__ __forceinline__ void rd1(Deep& c) {
    constexpr int tab[12] = {0, 4, 5, 6, 7, 1, 2, 3, 8, 9, 10, 11}, k = tab[N < 12 ? N : 0];
    if constexpr (N >= 12) { }
    else if constexpr (k < 4) c.fa[P & 1][k] = LDS(const bf16x8, c.ra[k] + (P & 3) * SLOT);
    else if constexpr (k & 1) c.hi[P & 1][(k - 4) >> 1] = tr_read(c.rb[(k - 4) >> 1] + (P & 3) * SLOT + 512);
    else c.lo[P & 1][(k - 4) >> 1] = tr_read(c.rb[(k - 4) >> 1] + (P & 3) * SLOT);
    SB
}
template <int P, int M, int K> __device__ __forceinline__ void mk(Deep& c) {
    if constexpr (P >= 0) mfa(c.ac[M][K], CAT8(c.lo[P & 1][K], c.hi[P & 1][K]), c.fa[P & 1][M]);
    SB
}
#define DEF11 0
template <int P, int N> __device__ __forceinline__ void m1(Deep& c) { if constexpr (!(DEF11 && P == 11 && ((N >> 1) & 3) == 0)) mk<P, (N >> 1) & 3, (N >> 3) * 2 + (N & 1)>(c); }
template <int P, int M> __device__ __forceinline__ void m4(Deep& c) { mk<P, M, 0>(c); mk<P, M, 1>(c); mk<P, M, 2>(c); mk<P, M, 3>(c); }
#define E9 2
#define T9 0
#define TOP10 4
constexpr int dyoung(int S) { return S < 7 ? 8 : S < 9 ? 4 : E9; }
template <int U0, int I> __device__ __forceinline__ void dq(Deep& c) {
    constexpr int U = U0 + (I >> 2);
    if constexpr (U < NP) {
        if constexpr ((I & 3) == 0 && U >= 10) prepB<U>(c);
        issueB<I & 3>(c);
        if constexpr (U == 12 && (I & 3) == 3) set_m0<FOFF[1]>(c.ldsb);
    } else if constexpr (U == 15) {
        dma16sco<(I & 1) * 1024>(c.lane16, c.wsh[(I >> 1) & 1]);
        if constexpr ((I & 3) == 3) set_m0<FOFF[3]>(c.ldsb);
    } else {
        dma16sco<(I & 3) * 1024>(c.lane16, c.wsf[U == 16]);
        if constexpr (U == 14 && (I & 3) == 3) set_m0<FOFF[2]>(c.ldsh);
    }
    SB
}
template <int S, int I0, int I1> __device__ __forceinline__ void dissue(Deep& c) {
    if constexpr (I0 < I1 && I0 < 4 && S < 7) { dq<S + 3, I0>(c); dissue<S, I0 + 1, I1>(c); }
}
constexpr int cum(unsigned long long t, int n) { int k = 0; for (int i = 0; i < n; ++i) k += (t >> (4 * i)) & 15; return k; }
template <int U0, int I0, int I1> __device__ __forceinline__ void dqs(Deep& c) { if constexpr (I0 < I1) { dq<U0, I0>(c); dqs<U0, I0 + 1, I1>(c); } }
template <int P, int N0, int N1> __device__ __forceinline__ void rdn(Deep& c) { if constexpr (N0 < N1) { rd1<P, N0>(c); rdn<P, N0 + 1, N1>(c); } }
__device__ __forceinline__ unsigned tag_addr(int lane, int unit, int nq, int st, int xcd) {
    const unsigned l4 = (unsigned)lane * 4u;
    const unsigned a0 = (unsigned)(WS_FLAG + unit * 4) + l4 * 32u;
    const unsigned a1 = (((unsigned)(nq * 256) + l4 * 64u) & 768u) + (unsigned)(st * 4);
    const unsigned a2 = (unsigned)(WS_FLAG + xcd * 128 - 64) + l4;
    const unsigned lo = l4 < 32u ? a0 : a1, hi = l4 < 168u ? a2 : (unsigned)(st * 4);
    return l4 < 64u ? lo : hi;
}
__device__ __forceinline__ void tags(unsigned tad, const void* ws, unsigned lds, unsigned long long m) {
    asm volatile("s_mov_b32 m0, %2\n\ts_mov_b64 exec, %3\n\tglobal_load_lds_dword %0, %1 sc1\n\ts_mov_b64 exec, -1" :: "v"(tad), "s"(ws), "s"(lds), "s"(m) : "memory");
}
__device__ __forceinline__ void publish(Deep& c, unsigned long long m) {
    asm volatile("s_cmp_eq_u64 %3, 0\n\ts_cbranch_scc1 1f\n\ts_mov_b64 exec, %3\n\tglobal_atomic_add %0, %1, %2\n\ts_mov_b64 exec, -1\n1:"
                 :: "v"(c.tad), "v"(c.one), "s"(c.ws), "s"(m) : "memory", "scc");
}
__device__ __forceinline__ void rawo(const Deep& d, unsigned lane16, int cw, unsigned (&ro)[3]) {
    const unsigned r0 = (lane16 >> 6) & 15u, u = (unsigned)((cw >> 1) * (NF * NF * 128) + d.unit * 128 + (cw & 1) * 64) + ((lane16 ^ (lane16 >> 3 & 0x20u)) & 0x30u);
    ro[0] = r0 * (NF * 128) + u; ro[1] = min(r0 + 16u, NF - 1u) * (NF * 128) + u - 1024u; ro[2] = (lane16 >> 2) + (unsigned)(cw * 256);
}
__device__ __forceinline__ void raw(const Deep& d, unsigned lane16, int cw, const void* w_cross, const void* ws, unsigned (&ro)[3]) {
    if (__builtin_expect(d.m1 == 0u, 1)) {
        if (!(PRO & 4)) rawo(d, lane16, cw, ro);
        dma16(ro[0], w_cross, OFF_RAW + cw * 2048);
        dma16o<1024>(ro[1], w_cross);
        dma4sc(ro[2], ws, OFF_NB + cw * 256);
    }
    SB
}
template <int W> __device__ __forceinline__ void produce(Deep& c) {
    if (c.m1 == 0u) {
        const int unit = c.unit;
        const unsigned p8 = (c.lane16 << 4) & 0x300u, e2 = (c.lane16 >> 5) & 0x1eu;
        const unsigned rb = (unsigned)(OFF_RAW + c.c * 2048) + e2, rs = rb + (p8 >> 1) + (c.lane16 & 0x20u);
        const unsigned dm = p8 == (c.lane16 & 0x300u) ? ~0u : 0u;
        const int m = unit < 24 ? 2 * (unit & 6) + (unit & 1) + ((unit >> 2) & 2) : 4 * (unit & 1) + 2;
        unsigned fo = (unsigned)(WS_FRAG + (c.c & 1) * 131072 + (c.c >> 1) * 2048 + (unit >> 4) * 1024 + m * 64) + (((e2 & 12u) << 1) | ((e2 >> 2) & 4u) | (e2 & 2u)) * 2048u + ((c.lane16 & 0x30u) ^ (unsigned)((m & 4) << 3));
        if (unit >= 24) fo += (c.lane16 << 1) & 0x200u;
        WAITCNT(W, 15);
        asm volatile("" ::: "memory");
        u32x4 fr[2];
#pragma unroll
        for (int z = 0; z < 2; ++z) {
#pragma unroll
            for (int d = 0; d < 4; ++d) {
                const unsigned rp = (d < 3 ? rs ^ (unsigned)(z * 32) : rb + z * 32) + (unsigned)(512 * d);
                fr[z][d] = LDS(const uint16_t, rp) | ((unsigned)LDS(const uint16_t, rp + 64) << 16);
            }
            fr[z][3] &= dm;
        }
        st16sc(fo, c.ws, fr[0]); st16sc(fo + 65536u, c.ws, fr[1]);
        if (unit >= 24) { st16sc(fo ^ 512u, c.ws, u32x4{0u, 0u, 0u, 0u}); st16sc((fo ^ 512u) + 65536u, c.ws, u32x4{0u, 0u, 0u, 0u}); }
    }
    SB
}
template <bool L, int I> __device__ __forceinline__ void part(Deep& c) {
#define PART(cmp) asm volatile(cmp " %0, %1\n\ts_cbranch_scc1 1f\n\tbuffer_load_dwordx4 %2, %3, %4 offen offset:%5 lds\n1:" \
                               :: "s"(c.tb), "n"(I), "v"(c.boff[I]), "s"(c.rw1), "s"(c.sB), "n"(I * 1024) : "memory")
    SB
    if constexpr (L) PART("s_cmp_gt_u32"); else PART("s_cmp_le_u32");
    SB
}
__device__ __forceinline__ void check_flags(Deep& c) {
    if (c.m3 == 0u) {
        const unsigned l4 = c.lane16 >> 2;
        const unsigned long long open = __builtin_amdgcn_ballot_w64(c.tag == c.pl) & (c.nq ? 0x7ffffff0000ull : 0x3ffffff0000ull);
        if (__builtin_expect(open != 0ull, 0)) {
            unsigned& slow = c.slow; (void)slow; SLOW(4)
#pragma unroll 1
            for (int it = 0; it < (1 << 22); ++it) {
                tags(c.tad, c.ws, OFF_TAG + 256, 0x7ffffff0000ull);
                asm volatile("s_waitcnt vmcnt(0)" ::: "memory");
                if ((__builtin_amdgcn_ballot_w64(LDSV(OFF_TAG + 256 + l4) == c.tag) & open) == 0ull) break;
                __builtin_amdgcn_s_sleep(2);
            }
        }
    }
    SB
}
__device__ __forceinline__ void fetch_status(Deep& c) {
    if (c.m4 == 0u) {
        if (c.nq) dma16sc(c.lane16, c.ws, OFF_NB + 1024);
        else tags(c.tad, c.ws, OFF_TAG + 512, 0xe00ull);
    }
    SB
}
constexpr bool two(int P, int S) { return P == S || P == S + 1; }
constexpr int xk(int P, int k) {
    int x = 0;
    if (k == 1 || k == 2 || k == 4) x += two(P, P_ST) + (two(P, P_POLL) && P + 1 < P_CHK);
    if (k == 4 && P_FLAG != P_ST) x += two(P, P_FLAG);
    if (k == 2 || k == 4) x += P == 9 ? P_STAT == 9 : two(P, P_STAT) && !(P == 8 && P_POLL >= P_STAT);
    return x;
}
constexpr int below(int P, int v) { int b = 0; for (int k = 0; k < 5; ++k) if (xk(P, k) < v && xk(P, k) > b) b = xk(P, k); return b; }
constexpr unsigned xbits(int k) {
    unsigned m = 0;
    for (int P = 0; P < 10; ++P) { int v = below(P, 99); for (int j = 0; v > 0; ++j, v = below(P, v)) if (xk(P, k) == v) m |= 1u << (3 * P + j); }
    return m;
}
template <int P> __device__ __forceinline__ void xwait(Deep& c) {
    constexpr int d = dyoung(P), a = below(P, 99), b = below(P, a), e = below(P, b), f = below(P, e);
    static_assert(f == 0, "");
    WAITCNT(d + a, 0);
#define XW(n) "s_bitcmp1_b32 %0, %" #n "\n\ts_cbranch_scc1 1f\n\ts_waitcnt vmcnt(%"
    if constexpr (e > 0) asm volatile(XW(1) "4)\n\t" XW(2) "5)\n\t" XW(3) "6)\n1:" :: "s"(c.xa), "n"(3 * P), "n"(3 * P + 1), "n"(3 * P + 2), "n"(d + b), "n"(d + e), "n"(d) : "memory", "scc");
    else if constexpr (b > 0) asm volatile(XW(1) "3)\n\t" XW(2) "4)\n1:" :: "s"(c.xa), "n"(3 * P), "n"(3 * P + 1), "n"(d + b), "n"(d) : "memory", "scc");
    else if constexpr (a > 0) asm volatile(XW(1) "2)\n1:" :: "s"(c.xa), "n"(3 * P), "n"(d) : "memory", "scc");
}
#define DTOP 2
#define DTOPB 4
#define A8 12
#define A9 12
#define A10 12
#define T8 0x842100
#define T10 0xaa
constexpr int adv(int P) { return P == 8 ? A8 : P == 9 ? A9 : P == 10 ? A10 : 0; }
constexpr int bits(unsigned t, int n) { return __builtin_popcount(n < 32 ? t & ((1u << n) - 1u) : t); }
template <int P, int N> __device__ __forceinline__ void drun(Deep& c) {
    constexpr int old = 16 - adv(P - 1), tot = old + adv(P), U0 = P == 8 ? 10 : 11, E = P == 8 ? 4 : P == 10 ? 8 : E9, I0 = P == 10 ? TOP10 : 0, per = old >= 12 ? 1 : old >= 6 ? 2 : old >= 4 ? 3 : 12;
    constexpr unsigned T = P == 8 ? T8 : P == 10 ? T10 : T9;
    if constexpr (P == 10 && N == 0) dqs<U0, E9, TOP10>(c);
    dqs<U0, I0 + bits(T, N), I0 + bits(T, N + 1)>(c);
    if constexpr (N == 0 && per == 12) rdn<P, 0, 12>(c);
    if constexpr (N < old) m1<P - 1, adv(P - 1) + N>(c); else m1<P, N - old>(c);
    if constexpr (per < 12) rdn<P, N * per, (N * per + per < 12 ? N * per + per : 12)>(c);
    if constexpr (N == 5) { TX(0) }
    if constexpr (N == 11) { TX(1) }
    if constexpr (N == 17) { TX(2) }
    if constexpr (N + 1 < tot) drun<P, N + 1>(c); else { dqs<U0, I0 + bits(T, tot), E>(c); TX(3) }
}
template <int P, int N> __device__ __forceinline__ void mrest(Deep& c) { if constexpr (N < 16) { m1<P, N>(c); mrest<P, N + 1>(c); } }
#define T11 0x1249
#define NE 6
#define TOP11 1
#define E11 0
#define R12 6
constexpr int mn(int a, int b) { return a < b ? a : b; }
constexpr bool VT = TB != 0x4444 || TBP != 4;
constexpr int TMIN = mn(mn(mn(TB & 15, (TB >> 4) & 15), mn((TB >> 8) & 15, TB >> 12)), TBP);
#define DT 0x2222222
#define DL 12
constexpr int dtop(int P) { return (P & 3) == 2 ? (VT ? TMIN : DTOPB) : (DT >> (4 * P)) & 15; }
template <int P, int N> __device__ __forceinline__ void dbody(Deep& c) {
    constexpr int top = dtop(P);
    m1<P - 1, N>(c);
    rd1<P, N>(c);
    if constexpr (VT && (P & 3) == 2) { if constexpr (N >= DL && N - DL + TMIN < 4) part<true, (N - DL + TMIN) & 3>(c); }
    else if constexpr (N >= DL) dissue<P, top + N - DL, (N == 15 ? 16 : top + N - DL + 1)>(c);
    if constexpr (N < 15) dbody<P, N + 1>(c);
}
template <int N> __device__ __forceinline__ void dlast(Deep& c) {
    dqs<14, TOP11 + bits(T11, N), TOP11 + bits(T11, N + 1)>(c);
    m1<11, N>(c);
    if constexpr (N == R12) { WAITCNT(TOP11 + bits(T11, N + 1), 15); SB }
    if constexpr (N >= R12 && N < R12 + 8) rd1<12, (N - R12 < 4 ? N - R12 + 1 : N - R12 + 4)>(c);
    if constexpr (N < 15) dlast<N + 1>(c); else dqs<14, TOP11 + bits(T11, 16), NE>(c);
}
template <int P> struct DStep {
    static __device__ __forceinline__ void run(Deep& c) {
        TSP(P)
        SB
        if constexpr (P == P_FLAG || P == P_ST) publish(c, P_FLAG == P_ST ? c.pmask : c.pmask & (P == P_ST ? 0x100ull : 0xffull));
        if constexpr (P == P_CHK) check_flags(c);
        if constexpr (P == P_STAT) fetch_status(c);
        if constexpr (P < 8) {
            dissue<P, 0, dtop(P)>(c);
            if constexpr (VT && (P & 3) == 2) {
                if constexpr (TMIN == 0) part<false, 0>(c);
                if constexpr (TMIN <= 1) part<false, 1>(c);
                if constexpr (TMIN <= 2) part<false, 2>(c);
                if constexpr (TMIN <= 3) part<false, 3>(c);
            }
            dbody<P, 0>(c);
        } else if constexpr (P <= 10) {
            drun<P, 0>(c);
            if constexpr (P == 10 && E11) { WAITCNT(4, 15); SB rdn<11, 1, 5>(c); rdn<11, 8, 12>(c); }
        } else {
            if constexpr (PU == 11) __builtin_amdgcn_s_setprio(2);
            rd1<11, 0>(c); rdn<11, 5, 8>(c);
            dqs<14, 0, TOP11>(c);
            if constexpr (!E11) { WAITCNT(4 + TOP11, 15); SB rdn<11, 1, 5>(c); rdn<11, 8, 12>(c); }
            mrest<10, A10>(c);
            rd1<12, 0>(c); rdn<12, 5, 8>(c);
            TS(c.ts[12])
            if constexpr (PU == 12) __builtin_amdgcn_s_setprio(2);
            dlast<0>(c);
        }
        if constexpr (P <= 10) {
            if constexpr (P == 4) {
                c.w2[0] = LDS(const u32x4, c.w2[0][0]); c.w2[1] = LDS(const u32x4, c.w2[1][0]);
            }
            if constexpr (P == P_POLL) { if (c.m3 == 0u) tags(c.tad, c.ws, OFF_TAG + 256, 0x7ffffff0000ull); }
            SB
            if constexpr (P < 6) prepB<P + 4>(c);
#define NPT 0x10000000000ull
            if constexpr ((NPT >> (4 * P)) & 15) asm volatile(".rept %0\n\ts_nop 0\n\t.endr" :: "n"((int)((NPT >> (4 * P)) & 15)));
            if constexpr (P == 10) WAITCNT(63, 0); else xwait<(P < 10 ? P : 0)>(c);
            TX(4)
            if constexpr (P == P_CHK - 1) { SB c.pl = LDSV(OFF_TAG + 256 + (c.lane16 >> 2)); SB }
            BARRIER;
            DStep<P + 1>::run(c);
        }
    }
};

struct Cross : Com {
    const char* tb[4]; const char* wsf;
    unsigned ga[40];
    unsigned re[2], rx[2], rx0[2], six, wf[2], pl, ph, fl;
    unsigned long long xm[2];
    f32x4 dg;
    s16x4 wa[2];
    bf16x8 wb[2], wr[2];
    u32x4 fe[2][2][2];
    f32x4 ew[NF], zs[4];
    u32x4 X[NP], Y[12], bp[4];
    float k0, k1, k2;
    unsigned am, wad[2];
    u32x4 on[4], s1, zq;
};
template <int I> __device__ __forceinline__ void issueA(Cross& c, unsigned off) { dma16o<I * 1024>(off, c.tb[I]); }
template <int K, int S, int N> __device__ __forceinline__ void sw1(Cross& c) {
    constexpr int a = 1 + 4 * K + (S == 1 ? 2 : (S == 3 ? 1 : 0)), b = 1 + 4 * K + (S == 0 ? 1 : (S == 2 ? 2 : 3)), Q = N >> 2, R = N & 3;
    float x = c.ew[2 * a + Q][R], y = c.ew[2 * b + Q][R];
    if (S >= 2) asm volatile("v_permlane32_swap_b32 %0, %1" ST : "+v"(x), "+v"(y)); else asm volatile("v_permlane16_swap_b32 %0, %1" ST : "+v"(x), "+v"(y));
    c.ew[2 * a + Q][R] = x; c.ew[2 * b + Q][R] = y;
}
template <int K, int S, int N0 = 0, int N1 = 8> __device__ __forceinline__ void swn(Cross& c) { if constexpr (N0 < N1) { sw1<K, S, N0>(c); swn<K, S, N0 + 1, N1>(c); } }
#define S8 24
#define S9 24
template <int N0, int N1> __device__ __forceinline__ void swl(Cross& c) { if constexpr (N0 < N1 && N0 < 32) { sw1<2, (N0 >> 3), (N0 & 7)>(c); swl<N0 + 1, N1>(c); } }
constexpr int sw0(int P) { return P <= 3 ? 0 : P <= 5 ? 8 : P == 6 ? 20 : P == 7 ? 32 : P == 8 ? 40 : P == 9 ? 40 + S8 : P == 10 ? 40 + S9 : 64; }
template <int P, int N0, int N1> __device__ __forceinline__ void tsw(Cross& c) {
    if constexpr (N0 < N1 && sw0(P) + N0 < sw0(P + 1)) { sw1<((sw0(P) + N0) >> 5), ((sw0(P) + N0) >> 3) & 3, (sw0(P) + N0) & 7>(c); tsw<P, N0 + 1, N1>(c); }
}
constexpr int fbuf(int P) { return P & 1; }
template <int P, int N> __device__ __forceinline__ void cmm(Cross& c) {
    if constexpr (P >= 0) {
        constexpr int B = fbuf(P);
        if constexpr (N < 2)
            asm volatile("v_smfmac_f32_16x16x64_bf16 %0, %1, %2, %3" :: "v"(c.zs[P ? (P - 1) >> 2 : 3]),
                         "v"(c.on[P ? (P - 1) & 3 : 0]), "v"(__builtin_shufflevector(c.fe[B][N][0], c.fe[B][N][1], 0, 1, 2, 3, 4, 5, 6, 7)), "v"(c.six));
        else if constexpr (N < 4) c.ew[2 * P + (N & 1)] = mf0(P ? c.wb[0] : c.wr[0], c.fe[B][0][N & 1]);
        else mfa(c.ew[2 * P + (N & 1)], P ? c.wb[1] : c.wr[1], c.fe[B][1][N & 1]);
    }
    SB
}
template <int P, int N> __device__ __forceinline__ void rdE(Cross& c) {
    c.fe[fbuf(P)][N >> 1][N & 1] = LDS(const u32x4, c.re[N >> 1] + (P & 3) * SLOT + (N & 1) * 2048);
    SB
}
template <int K> __device__ __forceinline__ void zpack(Cross& c) {
#define ZP "v_add_f32 %1, %1, %2\n\tv_add_f32 %3, %3, %4\n\tv_cvt_pk_bf16_f32 %0, %1, %3"
    if constexpr (K == 2) asm volatile(ZP :: "v"(c.zq[2]), "v"(c.zs[K][0]), "v"(c.zs[K][1]), "v"(c.zs[K][2]), "v"(c.zs[K][3]));
    else {
        unsigned r;
        asm volatile(ZP : "=v"(r) : "v"(c.zs[K][0]), "v"(c.zs[K][1]), "v"(c.zs[K][2]), "v"(c.zs[K][3]));
        c.zq[K] = r;
    }
    SB
}
template <int K> __device__ __forceinline__ void zclear(Cross& c) { c.zs[K] = f32x4{0.f, 0.f, 0.f, 0.f}; PIN(c.zs[K]); SB }
#define NJP 3
template <int P> __device__ __forceinline__ void rdX(Cross& c) {
    constexpr int k = 1 + ((P - 1) & ~3), a = ((P - 1) & 3) * 32, b = (P & 3) * 32;
    constexpr bool jp = (P - 1) / 4 < NJP;
#define RDX "s_mov_b64 exec, %6\n\tds_read2st64_b64 %0, %4 offset0:%7 offset1:%8\n\tds_read2st64_b64 %1, %4 offset0:%9 offset1:%10\n\t" \
            "ds_read2st64_b64 %2, %5 offset0:%7 offset1:%8\n\tds_read2st64_b64 %3, %5 offset0:%9 offset1:%10\n\ts_mov_b64 exec, -1"
#define RDY "s_mov_b64 exec, %5\n\tds_read_b128 %0, %4 offset:%6\n\tds_read_b128 %1, %4 offset:%7\n\tds_read_b128 %2, %4 offset:%8\n\tds_read_b128 %3, %4 offset:%9\n\ts_mov_b64 exec, -1"
    if constexpr (P == 0) { const u32x2 lo = LDS(const u32x2, c.rx0[0]), hi = LDS(const u32x2, c.rx0[1]); c.X[0] = u32x4{lo[0], lo[1], hi[0], hi[1]}; }
    else if constexpr ((P & 3) == 2 && jp)
        asm volatile(RDY : "=&v"(c.X[k]), "=&v"(c.X[k + 1]), "=&v"(c.X[k + 2]), "=&v"(c.X[k + 3]) : "v"(c.rx[0]), "s"(c.xm[0]), "n"(a * 512), "n"(a * 512 + 2048), "n"(b * 512), "n"(b * 512 + 2048) : "memory");
    else if constexpr ((P & 3) == 0 && jp)
        asm volatile(RDY : "+v"(c.X[k]), "+v"(c.X[k + 1]), "+v"(c.X[k + 2]), "+v"(c.X[k + 3]) : "v"(c.rx[0]), "s"(c.xm[1]), "n"(a * 512), "n"(a * 512 + 2048), "n"(b * 512), "n"(b * 512 + 2048) : "memory");
    else if constexpr ((P & 3) == 2)
        asm volatile(RDX : "=&v"(c.X[k]), "=&v"(c.X[k + 1]), "=&v"(c.X[k + 2]), "=&v"(c.X[k + 3]) : "v"(c.rx[0]), "v"(c.rx[1]), "s"(c.xm[0]), "n"(a), "n"(b), "n"(a + 4), "n"(b + 4) : "memory");
    else if constexpr ((P & 3) == 0)
        asm volatile(RDX : "+v"(c.X[k]), "+v"(c.X[k + 1]), "+v"(c.X[k + 2]), "+v"(c.X[k + 3]) : "v"(c.rx[0]), "v"(c.rx[1]), "s"(c.xm[1]), "n"(a), "n"(b), "n"(a + 4), "n"(b + 4) : "memory");
    SB
}
template <int K, int N0, int N1> __device__ __forceinline__ void xpair(Cross& c) {
    if constexpr (N0 < N1 && K < NJP) {
        constexpr int m = N0 >> 2, d = N0 & 3, s = 1 + 4 * K + (d & 2), e = (m & 2) + (d & 1);
        unsigned r;
        asm volatile("v_perm_b32 %0, %1, %2, %3" ST : "=v"(r) : "v"(c.X[s + 1][e]), "v"(c.X[s][e]), "s"((m & 1) ? c.ph : c.pl));
        c.Y[4 * K + m][d] = r;
        xpair<K, N0 + 1, N1>(c);
    }
}
#define C11 1
#define G8 0x5421
#define G8P(n) { if constexpr ((G8 & 15) == (n)) GQ(10, 0) if constexpr (((G8 >> 4) & 15) == (n)) GQ(10, 1) if constexpr (((G8 >> 8) & 15) == (n)) GQ(10, 2) if constexpr (((G8 >> 12) & 15) == (n)) GQ(10, 3) }
#define FPOS 0x01504241
#define FFL 4
#define BP 1
#define FI1(k, n) if (((FPOS >> (8 * (k))) & 255) == (n)) { dma16sco<(k) * 1024>(c.lane16, c.wsf); SB }
#define FI(n) FI1(0, n) FI1(1, n) FI1(2, n) FI1(3, n) if ((n) == BP) { asm volatile("" ::: "memory"); SB __builtin_amdgcn_s_barrier(); SB asm volatile("" ::: "memory"); SB } if ((n) == FFL) { WAITCNT(0, 15); SB LDS(unsigned, c.fl) = c.fl; SB }
#define GQ(Q, J) { issueA<J>(c, c.ga[4 * (Q) - 12 + (J)]); if constexpr ((J) == 3) set_m0<(((Q) + 1) & 3) * SLOT>(c.ldsb); SB }
static_assert(FOFF[0] == SLOT, "");
__device__ __forceinline__ void csetup(Cross& c) {
            const unsigned r6 = c.lane16 * 4u + (unsigned)c.rho * 256u;
            const unsigned am = (c.lane16 >> 8) + (unsigned)c.rho;
            c.wf[0] = (r6 & 0x3c0u) | (((am << 4) ^ ((r6 >> 3) & 0x20u)) & 0x30u) | SLOT;
            c.wf[1] = c.wf[0] + OFF_B;
            const unsigned wap = OFF_SW2 + (c.lane16 & 0x3c0u) + ((c.lane16 >> 1) & 0x18u);
            c.wa[0] = tr_read(wap); c.wa[1] = tr_read(wap + 32);
            const unsigned r2 = (c.lane16 >> 3) & 30u, s1 = OFF_SW1 + min(r2, 24u), sg = s1 + (am & 3u) * 52u;
#pragma unroll
            for (int k = 0; k < 4; ++k) {
                const unsigned a = (k < 3 ? sg : s1) + k * 208;
                const unsigned v = LDS(const uint16_t, a) | ((unsigned)LDS(const uint16_t, a + 26) << 16);
                c.s1[k] = r2 < 26u ? v : 0u;
            }
            PIN(c.s1);
            PIN(c.wf[0]); PIN(c.wf[1]); PIN(c.wa[0]); PIN(c.wa[1]);
}
template <int P> struct CStep {
    static __device__ __forceinline__ void run(Cross& c) {
        TSP(P)
        if constexpr (P == 7) { STAMP(6) }
        SB
        if constexpr (P == 1) {
#pragma unroll
            for (int k = 0; k < 2; ++k) c.wr[k] = CAT8(tr_read(c.wad[1] + k * 1024), tr_read(c.wad[1] + k * 1024 + 128));
            SB
            cmm<0, 0>(c); rdE<1, 0>(c); set_m0<0>(c.ldsb); cmm<0, 1>(c); rdE<1, 1>(c);
            issueA<0>(c, c.ga[4]); rdE<1, 2>(c); issueA<1>(c, c.ga[5]); rdE<1, 3>(c);
#pragma unroll
            for (int k = 0; k < 2; ++k) c.wb[k] = CAT8(tr_read(c.wad[0] + k * 1024), tr_read(c.wad[0] + k * 1024 + 128));
            SB
            cmm<0, 2>(c); cmm<0, 3>(c); issueA<2>(c, c.ga[6]); cmm<0, 4>(c); cmm<0, 5>(c); issueA<3>(c, c.ga[7]);
            PIN(c.wb[0]); PIN(c.wb[1]);
        } else if constexpr (P <= 8) {
            cmm<P - 1, 2>(c); rdE<P, 0>(c);
            cmm<P - 1, 3>(c); rdE<P, 1>(c);
            cmm<P - 1, 4>(c); rdE<P, 2>(c);
            cmm<P - 1, 5>(c); rdE<P, 3>(c);
            cmm<P - 1, 0>(c);
            if constexpr (P < 8) set_m0<((P + 3) & 3) * SLOT>(c.ldsb);
            cmm<P - 1, 1>(c);
            rdX<P>(c);
        }
        constexpr bool xw = P && !(P & 1);
#define GA(J) if constexpr (P < 7 && P != 1) issueA<J>(c, c.ga[4 * P + (J)]); SB
        if constexpr (P == 4 || P == 7) zclear<(P + 1) / 4>(c);
        if constexpr (P == 7) {
#pragma unroll
            for (int r = 0; r < 4; ++r) { c.bp[r][3] = cvt_o(c.ew[0][r], c.ew[1][r]); c.bp[r][0] = cvt_o(c.ew[2][r], c.ew[3][r]); }
            c.k0 = -2.0f * 0.7978845608028654f * 1.4426950408889634f / 64.0f; c.k1 = c.k0 * (0.044715f / 4096.0f); c.k2 = -64.0f / 1.4426950408889634f;
            c.am = (c.wf[0] & 16u) ? ~0u : 0u;
            c.fl = (c.lane16 >> 2) + (unsigned)(OFF_FL + 2048 + c.c * 256);
            PIN(c.k0); PIN(c.k1); PIN(c.k2); PIN(c.am); PIN(c.fl);
            SB
        }
        if constexpr (P == 2 || P == 6) zpack<(P == 2 ? 3 : 0)>(c);
        if constexpr (P == 8) {
            TX(0) G8P(0)
            cmm<8, 2>(c); cmm<8, 3>(c); cmm<8, 4>(c); cmm<8, 5>(c); cmm<8, 0>(c); cmm<8, 1>(c);
            G8P(1) TX(1)
            WAITCNT(63, 0); SB
            xpair<1, 0, 8>(c); SB G8P(2) xpair<1, 8, 16>(c); SB G8P(3)
            zpack<1>(c); TX(2)
            tsw<8, 0, S8 / 2>(c); SB G8P(4) tsw<8, S8 / 2, S8>(c); SB G8P(5)
        } else if constexpr (P == 9) {
            GQ(11, 0) rdE<9, 0>(c); rdE<9, 1>(c); rdE<9, 2>(c); rdE<9, 3>(c); tsw<9, 0, 2>(c); SB GQ(11, 1) tsw<9, 2, 6>(c); SB GQ(11, 2) TX(0)
            cmm<9, 2>(c); cmm<9, 3>(c); cmm<9, 4>(c); GQ(11, 3) cmm<9, 5>(c); cmm<9, 0>(c); cmm<9, 1>(c); TX(1)
            GQ(12, 0) tsw<9, 6, 10>(c); SB GQ(12, 1) tsw<9, 10, 14>(c); SB GQ(12, 2) TX(2) tsw<9, 14, 24>(c); SB GQ(12, 3)
        } else if constexpr (P == 10) {
            { unsigned z_; asm volatile("" : "=v"(z_)); c.zq[2] = z_; SB }
            rdE<10, 0>(c); rdE<10, 1>(c); PIN(c.zq); SB rdE<10, 2>(c); rdE<10, 3>(c);
            tsw<10, 0, 24>(c); SB
#pragma unroll
            for (int r = 0; r < 4; ++r) c.bp[r][1] = cvt_o(c.ew[10][r], c.ew[11][r]);
            SB
            cmm<10, 2>(c); cmm<10, 3>(c); cmm<10, 4>(c); cmm<10, 5>(c); rdX<10>(c); cmm<10, 0>(c); cmm<10, 1>(c); TX(0)
            WAITCNT(4, 15); asm volatile("" ::: "memory"); SB TX(1)
            rdE<11, 0>(c); rdE<11, 1>(c); rdE<11, 2>(c); rdE<11, 3>(c);
            swn<2, 0, 0, 8>(c); SB
            WAITCNT(63, 0); SB TX(2)
            cmm<11, 0>(c); cmm<11, 1>(c);
            asm volatile("v_mfma_f32_16x16x32_bf16 %0, %2, %4, 0\n\tv_mfma_f32_16x16x32_bf16 %1, %2, %5, 0\n\tv_mfma_f32_16x16x32_bf16 %0, %3, %6, %0\n\tv_mfma_f32_16x16x32_bf16 %1, %3, %7, %1"
                         : "=&v"(c.ew[22]), "=&v"(c.ew[23]) : "v"(c.wb[0]), "v"(c.wb[1]), "v"(c.fe[1][0][0]), "v"(c.fe[1][0][1]), "v"(c.fe[1][1][0]), "v"(c.fe[1][1][1]));
            SB
        }
        else if constexpr (xw) { tsw<P, 0, 4>(c); WAITCNT(63, 0); GA(0) tsw<P, 4, 6>(c); GA(1) tsw<P, 6, 8>(c); GA(2) tsw<P, 8, 10>(c); GA(3) tsw<P, 10, 12>(c); }
        else { GA(0) GA(1) tsw<P, 0, 4>(c); GA(2) tsw<P, 4, 8>(c); GA(3) tsw<P, 8, 12>(c); }
        if constexpr (P == 4) { xpair<0, 0, 16>(c); SB }
        if constexpr (P == 3) csetup(c);
        SB
        TX(3)
        WAITCNT(P == 7 || P == 8 ? 4 : P < 10 ? 8 + (P == 0 && SMP == 2 ? 2 : 0) : 0, 0);
        TX(4)
        if constexpr (P == 10) { asm volatile("" ::: "memory"); SB rdE<12, 0>(c); rdE<12, 1>(c); rdE<12, 2>(c); rdE<12, 3>(c); FI(0x41) }
        else { BARRIER; CStep<P + 1>::run(c); }
    }
};

#define PFC 147
#define WPRE 5
#define ALN 4096
__global__ __launch_bounds__(512) __attribute__((aligned(ALN))) void k_main(const int* __restrict__ idx, const uint16_t* __restrict__ table,
        const uint16_t* __restrict__ w_bi, const uint16_t* __restrict__ w_deep1, const uint16_t* __restrict__ w_cross,
        unsigned* __restrict__ ws32, const uint16_t* __restrict__ se_w1, const uint16_t* __restrict__ se_w2,
        const uint16_t* __restrict__ w_deep2_, __hip_bfloat16* __restrict__ out_, unsigned long long* __restrict__ stamps) {
    SB
    asm volatile(".set .Lnext, 1\n\t.macro STUB1 n\n\t.if .Lnext == \\n\n.Lst\\n: s_cbranch_execz .Lback\n\t.endif\n\t.endm\n\t"
                 ".macro STUB\n\t.if . - _Z6k_mainPKiPKtS2_S2_S2_PjS2_S2_S2_P14__hip_bfloat16Py >= .Lnext * 4096\n\t"
                 "STUB1 1\n\tSTUB1 2\n\tSTUB1 3\n\tSTUB1 4\n\t.set .Lnext, .Lnext + 1\n\t.endif\n\t.endm\n\t"
                 ".macro PLACE m, k\n\t.rept ((\\k - (. - _Z6k_mainPKiPKtS2_S2_S2_PjS2_S2_S2_P14__hip_bfloat16Py)) & (\\m - 1)) / 4\n\ts_nop 0\n\t.endr\n\t.endm");
    const int tid = threadIdx.x, lane = tid & 63;
    const int w = __builtin_amdgcn_readfirstlane(tid >> 6);
    const int bid = blockIdx.x;
    const int xcd = bid & 7, slot = bid >> 3;
    const int st = xcd * 8 + (slot >> 2), nq = slot & 3;
    const int s0 = st * 64;
    const int rho = (st + nq) & 3, cw = w & 3;
    const int l15 = lane & 15, g = lane >> 4;
    LDS(u32x2, OFF_FL + tid * 8) = u32x2{0u, 0u};
    unsigned long long pc;
    {
        const unsigned ln = min((unsigned)(4 + w * 4 + (lane & 3) + ((lane >> 2) & 1) * (32 + nq * 32)), (unsigned)PFC) * 128u;
        asm volatile("s_getpc_b64 %0\n\ts_mov_b32 m0, %2\n\ts_mov_b64 exec, 0xff\n\tglobal_load_lds_dword %1, %0\n\ts_mov_b64 exec, -1"
                     : "=&s"(pc) : "v"(ln), "n"(OFF_JNK) : "memory");
    }
    SB
    float gc0 = -2.0f * 0.7978845608028654f * 1.4426950408889634f, gc1 = gc0 * 0.044715f;
#define FIN_ST(c)
#define FIN_SA(c)
#define ALL
#define DA 0x7a0
#define PADE if (sizeof(c) == sizeof(Cross)) asm volatile("s_endpgm\n\tPLACE 4096, %0" :: "n"(DA) : "memory");
#define FINISH(c) STAMP(5) FIN_ST(c) FIN_SA(c) PADE __builtin_amdgcn_endpgm(); __builtin_unreachable();
    unsigned lane16 = (unsigned)lane * 16u;
    int deep = w >> 2;
    Deep d;
    d.ldsb = (unsigned)(cw * 4096);
    unsigned o2 = min(lane16, 624u), im0 = (unsigned)(OFF_IDX + cw * 2048), io = (unsigned)((s0 + 16 * cw) * (NF * 4));
#define DSET mkboff(d, lane, cw, nq); \
    _Pragma("unroll") for (int k = 0; k < 4; ++k) { d.sG[k] = (unsigned)(((k + rho) & 3) * (2 * 32768)); PINS(d.sG[k]); } \
    d.unit = xcd + 8 * ((st & 7) >> 1);         \
    d.m1 = (unsigned)((nq ^ 1) | (st & 1) | ((d.unit + 32 - NF) >> 5)); PINS(d.m1); PINS(d.ldsb); \
    if (PRO & 4) { soon = min(d.m1, (unsigned)(nq ^ 3)); PINS(soon); }
    unsigned soon;
    if (!(PRO & 1)) { DSET }
    PIN(lane16); PIN(o2); PINS(im0); PINS(io); PINS(deep);
    asm volatile("s_mov_b32 m0, %0" :: "s"(im0) : "memory");
    SB
    asm volatile("" :: "s"(idx), "s"(table), "s"(se_w1), "s"(se_w2), "s"(w_bi), "s"(w_deep1), "s"(w_cross), "s"(ws32));
    asm volatile("s_cmp_lg_u32 %3, 0\n\ts_cbranch_scc1 1f\n\tglobal_load_lds_dwordx4 %0, %2\n\tglobal_load_lds_dwordx4 %1, %2 offset:1024\n1:"
                 :: "v"(lane16), "v"(o2), "s"((const char*)idx + io), "s"(deep) : "memory", "scc");
    SB
    typedef const __attribute__((address_space(4))) unsigned long long* kap;
    kap ka = (kap)__builtin_amdgcn_kernarg_segment_ptr();
    PINS(ka);
    const char* wd2;
    gp out;
    if (!(PRO & 2)) { wd2 = (const char*)ka[8]; out = (gp)ka[9]; }
    unsigned long long w1p;
    asm volatile("s_mov_b64 %0, %1" : "=s"(w1p) : "s"(w_deep1));
    d.rw1 = __builtin_amdgcn_make_buffer_rsrc((void*)w1p, (short)0, NF * 32768, 0x00020000);
    if (deep) {
        const unsigned ta = min(lane16, 80u) * 8u + (unsigned)((bid * 4 + cw) * 768);
        asm volatile("s_mov_b32 m0, %2\n\ts_nop 0\n\tbuffer_load_dword %0, %1, 0 offen lds" :: "v"(ta), "s"(d.rw1), "n"(OFF_JNK) : "memory");
    }
#define PTS 2101376
#define PTW 5
#define PTO 0
    if (w == PTW) asm volatile("s_mov_b32 m0, %2\n\ts_mov_b64 exec, %3\n\tglobal_load_lds_dword %0, %1\n\ts_mov_b64 exec, -1" :: "v"(lane16 * (PTS / 16) + (unsigned)(PTO)), "s"(table), "n"(OFF_JNK), "s"(PT == 64 ? ~0ull : (1ull << (PT & 63)) - 1) : "memory");
    if (PRO & 1) { SB DSET }
    unsigned ro[3], dp = (unsigned)(deep ^ 1) | d.m1;
    if (PRO & 4) {
        PINS(dp);
        asm volatile("" : "=v"(ro[0]), "=v"(ro[1]), "=v"(ro[2]));
        if (dp == 0u) { rawo(d, lane16, cw, ro); PIN(ro[0]); PIN(ro[1]); PIN(ro[2]); }
        PINS(d.m1); PINS(deep);
        SB
    }
    BARRIER;
    Cross x;
    x.c = cw; x.nq = nq; x.rho = rho; x.st = st; x.lane16 = lane16; x.ws = (char*)ws32; x.ldsb = d.ldsb;
    unsigned srcA, srcB, ib[4], sm[3];
    if (!(PRO & 4)) soon = d.m1 == 0u ? 0u : (~L1M >> nq) & 1u;
    const char* sp;
    u32x2 ia[NP], ic[NP];
    if (deep) {
        if (TGE == 1) if (cw == 3) tags(tag_addr(lane, d.unit, nq, st, xcd), ws32, OFF_TAG, nq ? 0x7ffffff0000ull : 0x3ffffff0e00ull);
        raw(d, lane16, cw, w_cross, ws32, ro); prepB<0>(d);
        issueB<0>(d); issueB<1>(d); issueB<2>(d); issueB<3>(d);
        SB
        if (TGE == 0) if (cw == 3) tags(tag_addr(lane, d.unit, nq, st, xcd), ws32, OFF_TAG, nq ? 0x7ffffff0000ull : 0x3ffffff0e00ull);
        if ((cw ^ 2) + nq == 0 && lane < 32) st16sc(lane16, (char*)ws32 + WS_SUM + st * 512, u32x4{0u, 0u, 0u, 0u});
        SB
        if (soon != 0u) { prepB<1>(d); snop(0); SB issueB<0>(d); issueB<1>(d); issueB<2>(d); issueB<3>(d); }
        SB
    } else {
        Cross& c = x;
        srcA = (unsigned)(((lane & 7) ^ g) * 16); srcB = srcA ^ 64u;
        unsigned ib0 = (unsigned)(OFF_IDX + cw * 2048 + (lane >> 3) * 104);
#pragma unroll
        for (int k = 0; k < 4; ++k) { ib[k] = ib0 + (unsigned)(((k + rho) & 3) * 8); PIN(ib[k]); c.tb[k] = (const char*)table - k * 1024; PINS(c.tb[k]); }
        PIN(srcA); PIN(srcB); PIN(ib0);
        const int k = (l15 & 1) | ((l15 >> 2) & 2), r = 4 * cw + g;
        sm[0] = (unsigned)(cw * 2048 + (lane >> 1) * 128 + nq * 32 + (lane & 1) * 16);
        sm[1] = cw == 3 && !(PRO & 2) ? (unsigned)(nq * 128) + ((lane16 >> 2) & 124u) : (unsigned)min(cw * 64 + lane, 168) * 4u;
        sm[2] = (unsigned)((r < 13 ? r : 12) * (NF * 2) + k * 16 + (k == 3 ? 0 : ((l15 + 2 * rho) & 6) * 2));
        PIN(sm[0]); PIN(sm[1]); PIN(sm[2]);
        set_m0<0>(c.ldsb);
        SB
        WAITCNT(0, 15);
        asm volatile("" ::: "memory");
        STAMP(1)
        TP(c.tq[0])
        ia[0] = LDS(const u32x2, ib0 + 96); ic[0] = LDS(const u32x2, ib0 + 96 + 832);
#pragma unroll
        for (int q = 1; q < 3; ++q) { ia[q] = LDS(const u32x2, ib[q - 1]); ic[q] = LDS(const u32x2, ib[q - 1] + 832); }
        SB
#define GATHER(q) issueA<0>(c, ia[q][0] * 128u + srcA); issueA<1>(c, ic[q][0] * 128u + srcB); issueA<2>(c, ia[q][1] * 128u + srcA); issueA<3>(c, ic[q][1] * 128u + srcB);
        GATHER(0)
        set_m0<SLOT>(c.ldsb);
        SB
        GATHER(1)
#define SM0 asm volatile("s_mov_b32 m0, %2\n\ts_mov_b32 exec_hi, 0\n\tglobal_load_lds_dwordx4 %0, %1\n\ts_mov_b32 exec_hi, -1" :: "v"(sm[0]), "s"(w_bi), "s"(OFF_WBI + cw * 512) : "memory");
#define SM1 dma4(sm[1], sp, OFF_SW1 + cw * 256); dma4(sm[2], se_w2, OFF_SW2 + cw * 256);
        sp = cw == 3 && !(PRO & 2) ? wd2 : (const char*)se_w1;
        SM0 SM1
        set_m0<2 * SLOT>(c.ldsb);
        TP(c.tq[1])
    }
    if (PRO & 4) PINS(d.m1);
    int crs = deep ^ 1;
    PINS(crs); PINS(deep);
    if (crs == 0) {
        Deep& c = d;
        int lane;
        asm volatile("v_mbcnt_lo_u32_b32 %0, -1, 0\n\tv_mbcnt_hi_u32_b32 %0, -1, %0" : "=v"(lane));
        const int l15 = lane & 15, g = lane >> 4, tid = w * 64 + lane;
        c.c = cw; c.nq = nq; c.rho = rho; c.st = st; c.lane16 = (unsigned)lane * 16u; c.ws = (char*)ws32;
        c.m3 = (unsigned)(cw ^ 3); c.m4 = c.m3 | min(c.m1, (unsigned)nq);
        c.tad = tag_addr(lane, c.unit, nq, st, xcd);
        PIN(c.tad);
        SB
        mkboff(d, lane, cw, nq);
        {
            const unsigned a0 = (unsigned)(cw * 4096 + (cw & 1) * 2048 + l15 * 128 + ((4 * (cw >> 1) + g) ^ (l15 >> 1)) * 16);
            const int q = l15 >> 2, p = l15 & 3;
            const unsigned b0 = (unsigned)(OFF_B + cw * 4096 + (8 * g + q) * 128 + ((p >> 1) ^ (q & 2) ^ ((g & 1) << 2)) * 16 + (p & 1) * 8);
#pragma unroll
            for (int k = 0; k < 4; ++k) { c.ra[k] = a0 ^ (unsigned)(k * 4096); c.rb[k] = b0 ^ (unsigned)(k * 32); PIN(c.ra[k]); PIN(c.rb[k]); }
#pragma unroll
            for (int k = 0; k < 16; ++k) { c.ac[k >> 2][k & 3] = f32x4{0.f, 0.f, 0.f, 0.f}; PIN(c.ac[k >> 2][k & 3]); }
        }
        unsigned exw[4], fl = (c.lane16 >> 2) + (unsigned)(OFF_FL + cw * 256), fr = c.lane16 + OFF_FL, s8 = c.lane16 >> 1;
        Fin fk; fin(fk);
#pragma unroll
        for (int k = 1; k < 4; ++k) { exw[k] = (c.lane16 >> 2) + (unsigned)(OFF_B + (cw ^ k) * 4096); PIN(exw[k]); }
        double big = BIG;
        f32x4 va = {0.f, 0.f, 0.f, 0.f};
        float gc2 = 1.0f / TRC;
        f32x2 gcp = {gc0, gc1}, gcq = {gc2, gc2};
        c.w2[0][0] = OFF_WD2 + g * 16; c.w2[1][0] = OFF_WD2 + 64 + g * 16; PIN(c.w2[0]); PIN(c.w2[1]);
        const unsigned dl = (unsigned)((l15 >> 2) ^ (2 * (g & 1))), on = (l15 & 1) ? 0x3f800000u : 0x3f80u;
        u32x4 unit = {dl == 0u ? on : 0u, dl == 0u ? on : 0u, dl == 1u ? on : 0u, dl == 1u ? on : 0u};
        unsigned uix = (l15 & 2) ? 0xddddeeeeu : 0xdddd4444u;
        u32x2 pr[4][4];
#pragma unroll
        for (int k = 0; k < 4; ++k) { pr[0][k] = u32x2{0u, 0u}; PIN(pr[0][k]); }
        PIN(uix);
        PIN(fl); PIN(fr); PIN(s8); PIN(big); PIN(gcp); PIN(gcq); gc0 = gcp[0]; gc1 = gcp[1]; gc2 = gcq[0]; PIN(va); PIN(unit);
        c.wsf[0] = c.ws + WS_FRAG + 16384 + nq * 65536 + cw * 4096; c.wsf[1] = c.wsf[0] + 32768;
        c.wsh[0] = c.ws + WS_FRAG + 32768 + nq * 65536 + cw * 2048; c.wsh[1] = c.wsh[0] + 8192; c.ldsh = (unsigned)(cw * 2048);
        PINS(c.wsh[0]); PINS(c.wsh[1]); PINS(c.wsf[0]); PINS(c.wsf[1]); PINS(c.ldsh);
        c.pmask = cw != 3 ? 0ull : (c.m1 == 0u ? 0x1ffull : 0x100ull);
        unsigned long long wmask = (cw ^ 3) + nq ? 0ull : 0xe00ull;
        unsigned m5 = c.m3 | c.m1;
        PINS(wmask); PINS(m5);
        constexpr unsigned xb1 = xbits(1), xb2 = xbits(2), xb3 = xbits(3), xb4 = xbits(4);
        c.xa = c.m1 == 0u ? (cw == 3 ? xb4 : xb3) : cw != 3 ? 0u : nq ? xb1 : xb2;
        c.one = 1u; PIN(c.one);
        PINS(c.pmask); PINS(c.m3); PINS(c.m4); PINS(c.xa);
        if (PRO & 4) PINS(c.m1);
        produce<5>(c);
        if (PRO & 4) PINS(soon);
#define B1L if (soon == 0u) { prepB<1>(c); snop(0); SB issueB<0>(c); issueB<1>(c); issueB<2>(c); issueB<3>(c); }
        B1L
        prepB<2>(c);
        if (VT) { c.tb = c.m1 == 0u ? TBP : (TB >> (4 * nq)) & 15; PINS(c.tb); }
        TP(c.tq[0])
        BARRIER;
        TP(c.tq[1])
        issueB<0>(c); issueB<1>(c); issueB<2>(c); issueB<3>(c);
        if (PRO & 2) {
            SB PINS(ka);
            wd2 = (const char*)ka[8]; out = (gp)ka[9];
            if (cw == 0) dma4((unsigned)(nq * 128) + ((c.lane16 >> 2) & 124u), wd2, OFF_WD2);
            SB
        }
        prepB<3>(c);
        TP(c.tq[2])
        gp sums = (gp)c.ws + (unsigned)(WS_SUM + (s0 + 16 * cw) * 8), outp = out + (unsigned)((s0 + 16 * cw) * 2);
        unsigned tga = OFF_TAG + (c.lane16 >> 2);
        PINS(sums); PINS(outp); PIN(tga);
        SB
#define DB 24
        asm volatile("PLACE 32, %0" :: "n"(DB));
        SB
        WAITX(8, 2, c.m1)
        c.tag = LDSV(tga);
        PIN(c.tag);
        TP(c.tq[3])
        BARRIER;
        TP(c.tq[4])
        SB
        DStep<0>::run(c);
        TS(c.ts[13])
        u32x2 px[4][4];
#define BX 0
#define EX2(e, k) if (BX & 1) { px[e][k][0] = cvt_o(c.ac[e][k][0], c.ac[e][k][1]); px[e][k][1] = cvt_o(c.ac[e][k][2], c.ac[e][k][3]); SB } else { px[e][k][0] = pk_bf16(c.ac[e][k][0], c.ac[e][k][1]); px[e][k][1] = pk_bf16(c.ac[e][k][2], c.ac[e][k][3]); USE(px[e][k][0]); USE(px[e][k][1]); SB }
#define EXO(e, k) (((e) - 1) * 1024 + ((k) & 1) * 512)
#define WT1(e, k, d) { asm volatile("ds_write_addtid_b32 %0 offset:%1" :: "v"(px[e][k][d]), "n"(((k) < 2 ? 62464 : 32768) + EXO(e, k) + (d) * 256) : "memory"); SB }
#define WT(e, k) { asm volatile("ds_write_addtid_b32 %0 offset:%2\n\tds_write_addtid_b32 %1 offset:%3" ST :: "v"(px[e][k][0]), "v"(px[e][k][1]), \
            "n"(((k) < 2 ? 62464 : 32768) + EXO(e, k)), "n"(((k) < 2 ? 62464 : 32768) + EXO(e, k) + 256) : "memory"); SB }
#define W6(k) WT(3, k) WT(2, k) WT(1, k)
#define RD(h_) { constexpr int h = (h_) & 1; if (!(h_)) f = LDSV(fr + 1024); asm volatile("" ::: "memory"); SB \
            _Pragma("unroll") for (int k = 2 * (h); k < 2 * (h) + 2; ++k) _Pragma("unroll") for (int e = 3; e > 0; --e) _Pragma("unroll") for (int d = 0; d < 2; ++d) \
                pr[e][k][d] = LDS(const unsigned, exw[e] + ((h) ? 0 : 3 * SLOT) + EXO(e, k) + d * 256); SB }
#define CK(h) { if (__builtin_expect(__builtin_amdgcn_ballot_w64(f == 0u) != 0ull, 0)) { unsigned& slow = c.slow; (void)slow; SLOW(3) \
            _Pragma("unroll 1") for (int it = 0; it < (1 << 20); ++it) { RD(0) RD(1) if (__builtin_amdgcn_ballot_w64(f == 0u) == 0ull) break; } } SB }
#define AD(k) asm volatile("v_smfmac_f32_16x16x64_bf16 %0, %1, %2, %3" ST : "+v"(c.ac[0][k]) : "v"(unit), \
            "v"(u32x8{pr[0][k][0], pr[0][k][1], pr[1][k][0], pr[1][k][1], pr[2][k][0], pr[2][k][1], pr[3][k][0], pr[3][k][1]}), "v"(uix));
#define G4(m, k, ins) asm volatile(ins : m(y[4 * k]), m(y[4 * k + 1]), m(y[4 * k + 2]), m(y[4 * k + 3]) \
            : "v"(c.ac[0][k][0]), "v"(c.ac[0][k][1]), "v"(c.ac[0][k][2]), "v"(c.ac[0][k][3]), "v"(gc0), "v"(gc1), "v"(gc2));
#define GA_ "v_mul_f32 %0, %4, %4\n\tv_mul_f32 %1, %5, %5\n\tv_mul_f32 %2, %6, %6\n\tv_mul_f32 %3, %7, %7\n\t" \
            "v_fma_f32 %0, %0, %9, %8\n\tv_fma_f32 %1, %1, %9, %8\n\tv_fma_f32 %2, %2, %9, %8\n\tv_fma_f32 %3, %3, %9, %8\n\t" ST_
#define GM_ "v_mul_f32 %0, %0, %4\n\tv_mul_f32 %1, %1, %5\n\tv_mul_f32 %2, %2, %6\n\tv_mul_f32 %3, %3, %7\n\t" ST_
#define GB_ GM_ "v_exp_f32 %0, %0\n\tv_exp_f32 %1, %1\n\tv_exp_f32 %2, %2\n\tv_exp_f32 %3, %3\n\t"
#define GC_ ST_ "v_fma_f32 %0, %0, %10, %10\n\tv_fma_f32 %1, %1, %10, %10\n\tv_fma_f32 %2, %2, %10, %10\n\tv_fma_f32 %3, %3, %10, %10\n\t" ST_ \
            "v_rcp_f32 %0, %0\n\tv_rcp_f32 %1, %1\n\tv_rcp_f32 %2, %2\n\tv_rcp_f32 %3, %3\n\t" GM_
#define GELU(k) { G4("=&v", k, GA_ GB_ GC_) }
#define GEL1(k, I0) { G4("=&v", k, GA_) I0 G4("+v", k, GB_ GC_) }
#define GEL2(k, I1, I2) { if (k < 2 || PU2 == 2 * k + 6) { G4("=&v", k, GA_ GB_) I1 G4("+v", k, GC_) } else GELU(k) }
#define WDOT(q) asm volatile("v_smfmac_f32_16x16x64_bf16 %0, %1, %2, %3 abid:1" :: "v"(va), "v"(c.w2[q]), \
            "v"(f32x8{y[8 * q], y[8 * q + 1], y[8 * q + 2], y[8 * q + 3], y[8 * q + 4], y[8 * q + 5], y[8 * q + 6], y[8 * q + 7]}), "v"(uix));
#define PROTO { watch = __builtin_amdgcn_ballot_w64(tw == c.tag) & wmask; \
            if (__builtin_expect(m5 == 0u, 0)) { \
                const u32x4 e4 = LDS(const u32x4, fr + (OFF_NB - OFF_FL)), l4 = LDS(const u32x4, fr + (OFF_NB + 1024 - OFF_FL)); \
                watch |= __builtin_amdgcn_ballot_w64(e4[0] == l4[0]) | __builtin_amdgcn_ballot_w64(e4[1] == l4[1]) | __builtin_amdgcn_ballot_w64(e4[2] == l4[2]) | __builtin_amdgcn_ballot_w64(e4[3] == l4[3]); \
            } \
            PINS(watch); } SB
#define FLA { LDS(unsigned, fl + 1024) = fl; SB }
#define FLB { LDS(unsigned, fl + 3072) = fl; SB }
        unsigned f = 0;
        float y[16];
        unsigned long long watch = 0ull;
#define FW 4
#define FLP 1
#define WSP 1
#define PD 3
#define PR(n) { if ((n) == PU) __builtin_amdgcn_s_setprio(2); if ((n) == PD || (n) == PD2) __builtin_amdgcn_s_setprio(0); if ((n) == PU2) __builtin_amdgcn_s_setprio(2); SB }
#define PU2 13
#define PD2 -1
#define FH(n) if ((n) == FW) { WAITCNT(0, 15); SB set_m0<OFF_B + 3 * SLOT - 62464>(c.ldsb); LDS(unsigned, fl + 3072) = fl; SB }
#define FS(n) if ((n) == FLP) { WAITCNT(0, 15); SB LDS(unsigned, fl) = fl; SB publish(c, c.pmask); SB TE(c.tq[3]) }
#define PP 4
#define PQ(n) if ((n) == PP) { PROTO }
        PR(0) FH(0)
        TE(c.tq[0])
        mk<12, 3, 0>(c); mk<12, 2, 0>(c); mk<12, 1, 0>(c); FH(1) mk<12, 3, 1>(c); mk<12, 2, 1>(c); mk<12, 1, 1>(c); FH(2)
        mk<12, 3, 2>(c); mk<12, 2, 2>(c); mk<12, 1, 2>(c); FH(3) EX2(3, 0) mk<12, 3, 3>(c); EX2(2, 0) mk<12, 2, 3>(c); EX2(1, 0) mk<12, 1, 3>(c); FH(4) PR(1)
#define OWN 4
#define OW(k) if ((k) < OWN) mk<12, 0, k>(c);
#define OX(k) if ((k) >= OWN) mk<12, 0, k>(c);
        WT(3, 0) EX2(3, 1) OW(0) WT(2, 0) EX2(2, 1) WT(1, 0) EX2(1, 1) OW(1) WT(3, 1) EX2(3, 2) WT(2, 1) EX2(2, 2) OW(2) WT(1, 1) set_m0<OFF_B - 32768>(c.ldsb); EX2(1, 2) WT(3, 2) EX2(3, 3) OW(3) WT(2, 2) EX2(2, 3) WT(1, 2) EX2(1, 3)
        W6(3) set_m0<FOFF[2] + 8192>(c.ldsh); SB FLA
        TE(c.tq[1])
        static_assert(NE == 6 && WSP == 1, "");
        PR(2) dqs<14, NE, 8>(c);
        const unsigned tw = LDSV(fl + (OFF_TAG + 512 - OFF_FL - 768));
        SB
#define CKL 2
#define FRP 12
        dqs<14, 8, FRP>(c);
        if (CKL == 3) asm volatile("ds_read_b32 %0, %1 offset:1024" : "=v"(f) : "v"(fr) : "memory");
        dqs<14, FRP, 12>(c); PR(3)
        if (DEF11) m4<11, 0>(c);
        OX(0) OX(1) OX(2) OX(3)
        PQ(2) PR(9)
#define R2(n, a, o) "ds_read2st64_b32 %" #n ", %" #a " offset0:" #o " offset1:" #o "+1\n\t"
#define R3(n0, n1, n2, o) R2(n0, 16, o+8) R2(n1, 15, o+4) R2(n2, 14, o) ST_
        if (CKL == 1) {
            asm volatile("1:\n\tds_read_b32 %0, %13 offset:1024\n\t" R3(1, 2, 3, 192) R3(4, 5, 6, 194) R3(7, 8, 9, 0) R3(10, 11, 12, 2)
                         "s_waitcnt lgkmcnt(12)\n\tv_cmp_eq_u32 vcc, 0, %0\n\ts_cbranch_vccnz 1b"
                         : "=&v"(f), "=&v"(pr[3][0]), "=&v"(pr[2][0]), "=&v"(pr[1][0]), "=&v"(pr[3][1]), "=&v"(pr[2][1]), "=&v"(pr[1][1]),
                           "=&v"(pr[3][2]), "=&v"(pr[2][2]), "=&v"(pr[1][2]), "=&v"(pr[3][3]), "=&v"(pr[2][3]), "=&v"(pr[1][3])
                         : "v"(fr), "v"(exw[1]), "v"(exw[2]), "v"(exw[3]) : "memory", "vcc");
            PR(4)
        }
        else if (CKL == 3) {
            asm volatile("1:\n\ts_waitcnt lgkmcnt(0)\n\tv_cmp_eq_u32 vcc, 0, %0\n\tds_read_b32 %0, %13 offset:1024\n\ts_cbranch_vccnz 1b\n\t"
                         R3(1, 2, 3, 192) R3(4, 5, 6, 194) R3(7, 8, 9, 0) R3(10, 11, 12, 2)
                         : "+&v"(f), "=&v"(pr[3][0]), "=&v"(pr[2][0]), "=&v"(pr[1][0]), "=&v"(pr[3][1]), "=&v"(pr[2][1]), "=&v"(pr[1][1]),
                           "=&v"(pr[3][2]), "=&v"(pr[2][2]), "=&v"(pr[1][2]), "=&v"(pr[3][3]), "=&v"(pr[2][3]), "=&v"(pr[1][3])
                         : "v"(fr), "v"(exw[1]), "v"(exw[2]), "v"(exw[3]) : "memory", "vcc");
            PR(4) PQ(3)
        }
        else if (CKL == 2) { while (__builtin_amdgcn_ballot_w64(LDSV(fr + 1024) == 0u) != 0ull) { } RD(2) RD(1) PR(4) }
        else { RD(0) RD(1) PR(4) CK(0) }
        TE(c.tq[2])
#define AW(n) if (CKL & 1) { SB WAITCNT(63, n); SB }
#define ADN 0
        AW(9) AD(0) USE(f); AW(6) AD(1) AW(3) AD(2) AW(0) AD(3) snop(ADN); SB
        PR(5) PQ(4) FS(0)
        if (FLP == 1) GEL2(0, FS(1), ) else GELU(0)
        FS(2) PR(6) PQ(5)
        if (FLP == 3) GEL2(1, FS(3) PR(7), ) else GELU(1)
        FS(4) PR(8)
#define WDP 1
#define GP 0
#define PK2(op, a, b, m) op " %0, %0, " a m "\n\t" op " %1, %1, " b m "\n\t"
#define T4(op) op " %2, %2\n\t" op " %3, %3\n\t" op " %4, %4\n\t" op " %5, %5\n\t"
#define GPK(k, I) { f32x2 p0, p1; asm volatile("" : "=v"(p0), "=v"(p1)); I \
            asm volatile("v_pk_mul_f32 %0, %6, %6\n\tv_pk_mul_f32 %1, %7, %7\n\t" PK2("v_pk_fma_f32", "%8, %8", "%8, %8", " op_sel:[0,1,0] op_sel_hi:[1,1,0]") PK2("v_pk_mul_f32", "%6", "%7", "") \
                T4("v_exp_f32") ST_ PK2("v_pk_fma_f32", "%9, %9", "%9, %9", " op_sel_hi:[1,0,0]") T4("v_rcp_f32") ST_ PK2("v_pk_mul_f32", "%6", "%7", "") \
                :: "v"(p0), "v"(p1), "v"(p0[0]), "v"(p0[1]), "v"(p1[0]), "v"(p1[1]), "v"(f32x2{c.ac[0][k][0], c.ac[0][k][1]}), "v"(f32x2{c.ac[0][k][2], c.ac[0][k][3]}), "v"(gcp), "v"(gcq)); \
            y[4 * k] = p0[0]; y[4 * k + 1] = p0[1]; y[4 * k + 2] = p1[0]; y[4 * k + 3] = p1[1]; }
        if (GP & 4) GPK(2, ) else GEL2(2, PR(10), )
        PR(11)
        if (GP & 8) GPK(3, ) else GEL2(3, PR(12), )
        PR(13)
        if (GP & 8) { snop(0); SB }
        WDOT(0)
        WDOT(1)
        asm volatile("s_mov_b64 exec, 0xffff" : "+v"(va));
        snop(6);
        SB
        TE(c.tq[4])
        const float v = va[0];
        TS(c.ts[14])
        deliver(c, v, big, 2 * nq, s8, fk, sums, outp);
        ALL
        if (__builtin_expect(watch != 0ull, 0)) {
            asm volatile("s_mov_b64 exec, -1" ::: "memory");
            unsigned& slow = c.slow; (void)slow; SLOW(5)
            const u32x4 e4 = LDS(const u32x4, OFF_NB + c.lane16), l4 = LDS(const u32x4, OFF_NB + 1024 + c.lane16);
            bool opn[4];
#pragma unroll
            for (int k = 0; k < 4; ++k) opn[k] = c.m1 == 0u && e4[k] == l4[k];
            bool own = nq == 0 && lane >= 9 && lane < 12 && LDSV(OFF_TAG + 512 + lane * 4) == c.tag;
#pragma unroll 1
            for (int it = 0; it < (1 << 22) && __builtin_amdgcn_ballot_w64(opn[0] || opn[1] || opn[2] || opn[3] || own) != 0ull; ++it) {
                bool chg = false;
#pragma unroll
                for (int k = 0; k < 4; ++k)
                    if (opn[k] && __hip_atomic_load(ws32 + lane * 4 + k, __ATOMIC_RELAXED, __HIP_MEMORY_SCOPE_AGENT) != e4[k]) { opn[k] = false; chg = true; }
                if (own && __hip_atomic_load(ws32 + (c.tad >> 2), __ATOMIC_RELAXED, __HIP_MEMORY_SCOPE_AGENT) != c.tag) { own = false; chg = true; }
                if (__builtin_amdgcn_ballot_w64(chg) != 0ull) publish(c, c.pmask);
                else __builtin_amdgcn_s_sleep(2);
            }
        }
        FINISH(c)
    } else {
        Cross& c = x;
#pragma unroll
        for (int q = 3; q < NP; ++q) { ia[q] = LDS(const u32x2, ib[(q - 1) & 3] + ((q - 1) >> 2) * 32); ic[q] = LDS(const u32x2, ib[(q - 1) & 3] + ((q - 1) >> 2) * 32 + 832); }
#pragma unroll
        for (int q = 3; q < NP; ++q) {
            c.ga[4 * q - 12] = ia[q][0] * 128u + srcA; c.ga[4 * q - 11] = ic[q][0] * 128u + srcB;
            c.ga[4 * q - 10] = ia[q][1] * 128u + srcA; c.ga[4 * q - 9] = ic[q][1] * 128u + srcB;
        }
#pragma unroll
        for (int k = 0; k < 40; ++k) PIN(c.ga[k]);
        {
            const int G = 2 * g + rho;
            const unsigned ea = (unsigned)(cw * 4096 + l15 * 128);
            c.re[0] = ea + (unsigned)((g ^ (l15 >> 1)) * 16); c.re[1] = c.re[0] ^ 64u;
            c.rx[0] = ea + (unsigned)(((2 * nq + (g >> 1)) ^ (l15 >> 1)) * 16); c.rx[1] = c.rx[0] + 8u;
            c.rx0[0] = c.rx[0] + (unsigned)((rho & 1) * 2048 + (G & 2) * 4); c.rx0[1] = c.rx[0] + (unsigned)((~rho & 1) * 2048 + ((G + 1) & 2) * 4);
            c.six = (l15 & 1) ? 0xddddeeeeu : 0xdddd4444u;
            c.pl = 0x05040100u; c.ph = 0x07060302u; c.xm[0] = 0x0000ffff0000ffffull; c.xm[1] = 0xffff0000ffff0000ull;
            PINS(c.pl); PINS(c.ph); PINS(c.xm[0]); PINS(c.xm[1]);
#pragma unroll
            for (int k = 0; k < 4; ++k) { const unsigned o = (l15 >> 1) == 2 * k + (g >> 1) ? 0x3f803f80u : 0u; c.on[k] = u32x4{o, o, o, o}; PIN(c.on[k]); }
            PIN(c.re[0]); PIN(c.re[1]); PIN(c.rx[0]); PIN(c.rx[1]); PIN(c.rx0[0]); PIN(c.rx0[1]); PIN(c.six);
            zclear<3>(c); zclear<0>(c);
        }
        SB
        asm volatile("s_mov_b64 exec, 0\n\ts_cmp_eq_u32 %0, 0\n\ts_cbranch_scc1 .Lst1\n\ts_cmp_eq_u32 %0, 1\n\ts_cbranch_scc1 .Lst2\n\t"
                     "s_cmp_eq_u32 %0, 2\n\ts_cbranch_scc1 .Lst3\n\ts_branch .Lst4\n.Lback:\n\ts_mov_b64 exec, -1" :: "s"(cw) : "scc");
        static_assert(SMP == 0, "");
        BARRIER;
        SB
        GATHER(2)
        if (PRO & 2) { SB PINS(ka); out = (gp)ka[9]; }
        if (SMP == 2) { SM1 }
        TP(c.tq[2])
        {
            const int q = (lane >> 2) & 3, p = lane & 3;
            c.wad[0] = (unsigned)(OFF_WBI + (8 * g + q) * 32 + p * 8); c.wad[1] = (unsigned)(OFF_WBI + (8 * g + q) * 32 + ((p + rho) & 3) * 8);
            PIN(c.wad[0]); PIN(c.wad[1]);
        }
        gp sums = (gp)c.ws + (unsigned)(WS_SUM + (s0 + 16 * cw) * 8), outp = out + (unsigned)((s0 + 16 * cw) * 2);
        PINS(sums); PINS(outp);
        c.wsf = c.ws + WS_FRAG + nq * 65536 + cw * 4096; PINS(c.wsf);
        unsigned long long act = 0xf0000f0000f0000full, m1 = 0xaaaaaaaaaaaaaaaaull, m2 = 0xccccccccccccccccull;
        double bigs = BIG;
        PINS(act); PINS(m1); PINS(m2); PINS(bigs);
        Fin fk; fin(fk);
        TP(c.tq[3])
        WAITCNT(11, 0); BARRIER;
        TP(c.tq[4])
        __builtin_amdgcn_s_setprio(1);
        CStep<0>::run(c);
        TS(c.ts[11])
        u32x4 bq[4];
        u32x4 (&bp)[4] = c.bp;
        f32x2 T[6][8];
#define SPLIT(s0, s1, i0, i1) { _Pragma("unroll") for (int s = s0; s < s1; ++s) _Pragma("unroll") for (int i_ = i0; i_ < i1; ++i_) { \
            const f32x4 x = c.ew[2 * (1 + 4 * (s >> 1) + (i_ >> 1)) + (s & 1)]; T[s][i_] = f32x2{x[2 * (i_ & 1)], x[2 * (i_ & 1) + 1]}; } }
#define CB(B, i, h, k) { B[(2 * (i) + (h)) & 3][k] = cvt_o(T[2 * (k)][i][h], T[2 * (k) + 1][i][h]); }
#define SC(i, k) { asm volatile("v_pk_mul_f32 %0, %1, %0 op_sel_hi:[0,1]" : "+v"(T[2 * (k)][i]) : "v"(ao[k])); asm volatile("v_pk_mul_f32 %0, %1, %0 op_sel:[1,0]" : "+v"(T[2 * (k) + 1][i]) : "v"(ao[k])); }
        SPLIT(0, 4, 0, 8)
        SB
#define FLZ 8
#define SW3 6
#define SWA 8
#define FL1 3072
#define FLR 25
#define FLW 27
        STAMP(2)
        FI(0)
#define HO 1
#define M45 asm volatile("v_mfma_f32_16x16x32_bf16 %0, %2, %3, %0\n\tv_mfma_f32_16x16x32_bf16 %1, %2, %4, %1" :: "v"(c.ew[24]), "v"(c.ew[25]), "v"(c.wb[1]), "v"(c.fe[0][1][0]), "v"(c.fe[0][1][1]));
        cmm<12, 0>(c); cmm<12, 2>(c); cmm<12, 3>(c); cmm<12, 1>(c); M45
        rdX<12>(c);
        zpack<2>(c);
        FI(0x42)
        swl<8, 9>(c);
        const f32x4 hp = mf0(c.s1, c.zq);
        TS(c.ts[12])
        FI(0x50) TH(0)
        f32x2 hb;
        float hq[2];
        asm volatile("" : "=v"(hb), "=v"(hq[0]), "=v"(hq[1]));
        if (HO == 0) snop(5);
        SB
        swl<8 + HO, 8 + SWA>(c);
        FI(1) TH(1)
        asm volatile("v_mul_f32 %0, %4, %4\n\tv_mul_f32 %1, %5, %5\n\tv_mul_f32 %2, %6, %6\n\tv_mul_f32 %3, %7, %7\n\t"
                     "v_fma_f32 %0, %0, %9, %8\n\tv_fma_f32 %1, %1, %9, %8\n\tv_fma_f32 %2, %2, %9, %8\n\tv_fma_f32 %3, %3, %9, %8\n\t"
                     "v_mul_f32 %0, %0, %4\n\tv_mul_f32 %1, %1, %5\n\tv_mul_f32 %2, %2, %6\n\tv_mul_f32 %3, %3, %7\n\t"
                     "v_exp_f32 %0, %0\n\tv_exp_f32 %1, %1\n\tv_exp_f32 %2, %2\n\tv_exp_f32 %3, %3\n\t"
                     "v_fma_f32 %0, %0, %10, %10\n\tv_fma_f32 %1, %1, %10, %10\n\tv_fma_f32 %2, %2, %10, %10\n\tv_fma_f32 %3, %3, %10, %10\n\t"
                     "v_rcp_f32 %0, %0\n\tv_rcp_f32 %1, %1\n\tv_rcp_f32 %2, %2\n\tv_rcp_f32 %3, %3\n\t"
                     "v_mul_f32 %0, %0, %4\n\tv_mul_f32 %1, %1, %5\n\tv_mul_f32 %2, %2, %6\n\tv_mul_f32 %3, %3, %7\n\t"
                     "v_cvt_pk_bf16_f32 %0, %0, %1\n\tv_cvt_pk_bf16_f32 %1, %2, %3"
                     :: "v"(hb[0]), "v"(hb[1]), "v"(hq[0]), "v"(hq[1]), "v"(hp[0]), "v"(hp[1]), "v"(hp[2]), "v"(hp[3]), "v"(c.k0), "v"(c.k1), "v"(c.k2));
        FI(2) TH(2)
        swl<8 + SWA, 10 + SWA>(c);
        f32x4 av[2];
        asm volatile("v_mfma_f32_16x16x16_bf16 %0, %1, %2, 0" : "=&v"(av[0]) : "v"(c.wa[0]), "v"(hb));
        asm volatile("v_mfma_f32_16x16x16_bf16 %0, %1, %2, 0" : "=&v"(av[1]) : "v"(c.wa[1]), "v"(hb));
        FI(3)
        swl<10 + SWA, 18 + SWA>(c);
        TH(3) FI(4)
        TS(c.ts[13])
        SB
        float as[8];
        asm volatile("v_exp_f32 %0, %8\n\tv_exp_f32 %1, %9\n\tv_exp_f32 %2, %10\n\tv_exp_f32 %3, %11\n\tv_exp_f32 %4, %12\n\tv_exp_f32 %5, %13\n\tv_exp_f32 %6, %14\n\tv_exp_f32 %7, %15\n\t"
                     "v_add_f32 %0, 1.0, %0\n\tv_add_f32 %1, 1.0, %1\n\tv_add_f32 %2, 1.0, %2\n\tv_add_f32 %3, 1.0, %3\n\tv_add_f32 %4, 1.0, %4\n\tv_add_f32 %5, 1.0, %5\n\tv_add_f32 %6, 1.0, %6\n\tv_add_f32 %7, 1.0, %7\n\t"
                     "v_rcp_f32 %0, %0\n\tv_rcp_f32 %1, %1\n\tv_rcp_f32 %2, %2\n\tv_rcp_f32 %3, %3\n\tv_rcp_f32 %4, %4\n\tv_rcp_f32 %5, %5\n\tv_rcp_f32 %6, %6\n\tv_rcp_f32 %7, %7"
                     : "=&v"(as[0]), "=&v"(as[1]), "=&v"(as[2]), "=&v"(as[3]), "=&v"(as[4]), "=&v"(as[5]), "=&v"(as[6]), "=&v"(as[7])
                     : "v"(av[0][0]), "v"(av[0][1]), "v"(av[0][2]), "v"(av[0][3]), "v"(av[1][0]), "v"(av[1][1]), "v"(av[1][2]), "v"(av[1][3]));
        FI(5) TH(4)
        unsigned flg = LDSV(c.wf[1] + (OFF_FL + 2048 - OFF_B - SLOT));
        SB
        swl<18 + SWA, 18 + SWA + SW3>(c);
        FI(6)
        TG(c.tq[0]) TF(0)
        SB
        WAITCNT(63, 0);
        SB
        wait_set<0>(flg, c.wf[1] + (OFF_FL + 2048 - OFF_B - SLOT), c.slow);
        SB TF(1)
#define WSEQ(n) LDS(const u32x4, c.wf[((n) >> 4) & 1] + ((n) >> 5) * SLOT + (((n) & 15) ^ ((~(n) >> 2) & 1)) * 1024)
        u32x4 wq[64];
#pragma unroll
        for (int r = 0; r < WPRE; ++r) wq[r] = WSEQ(r);
        SB TF(2)
        swl<18 + SWA + SW3, 32>(c);
        SPLIT(4, 6, 0, 8)
        CB(bp, 0, 0, 2) CB(bp, 0, 1, 2)
        f32x2 ao[4] = {{as[0], as[1]}, {as[2], as[3]}, {as[4], as[5]}, {as[6], as[7]}};
        {
            f32x2 m[4] = {{c.ew[0][0], c.ew[0][1]}, {c.ew[1][0], c.ew[1][1]}, {c.ew[0][2], c.ew[0][3]}, {c.ew[1][2], c.ew[1][3]}};
#pragma unroll
            for (int r = 0; r < 4; r += 2) {
                asm volatile("v_pk_mul_f32 %0, %1, %0 op_sel_hi:[0,1]" : "+v"(m[r]) : "v"(ao[3]));
                asm volatile("v_pk_mul_f32 %0, %1, %0 op_sel:[1,0]" : "+v"(m[r + 1]) : "v"(ao[3]));
            }
            SC(0, 0) SC(0, 1) SC(0, 2) TF(3)
#pragma unroll
            for (int r = 0; r < 4; ++r) bq[r][3] = cvt_o(m[r & 2][r & 1], m[(r & 2) + 1][r & 1]);
        }
        ao[3][0] = sel(c.am, as[7], as[6]);
        PIN(ao[3]);
        SB
        FI(8)
        TG(c.tq[1]) TF(4)
        f32x4 u[2][4];
        float S1[7][16];
#define SQ(n) (((n) >> 1) + 4 * ((n) & 1))
#define FM(n, h, k) if ((k) < NJP) { asm volatile("v_pk_fma_f32 %0, %1, %2, %3" ST : "+v"(T[2 * (k) + (h)][SQ(n)]) : "v"(f32x2{u[h][2 + ((k) >> 1)][2 * ((k) & 1)], u[h][2 + ((k) >> 1)][2 * ((k) & 1) + 1]}), "v"(ao[k]), "v"(f32x2{u[h][(k) >> 1][2 * ((k) & 1)], u[h][(k) >> 1][2 * ((k) & 1) + 1]})); }
#define F1(n, h, s) if ((s) >= 2 * NJP) { float r_; \
            if ((s) < 6) { r_ = T[(s) % 6][SQ(n)][h]; asm volatile("v_fma_f32 %0, %1, %2, %3" : "+v"(r_) : "v"(u[h][2 + ((s) >> 2)][(s) & 3]), "v"(ao[(s) >> 1][(s) & 1]), "v"(u[h][(s) >> 2][(s) & 3])); } \
            else { asm volatile("v_fma_f32 %0, %1, %2, %3" : "=v"(r_) : "v"(u[h][3][2]), "v"(ao[3][0]), "v"(u[h][1][2]), "v"(u[h][3][3]), "v"(u[h][1][3])); } S1[s][2 * SQ(n) + (h)] = r_; }
#define FS1(h, k, d) { asm volatile("v_fma_f32 %0, %1, %2, %3" :: "v"(T[2 * (k) + (h)][7][d]), "v"(u[h][2 + ((k) >> 1)][2 * ((k) & 1) + (d)]), "v"(ao[k][d]), "v"(u[h][(k) >> 1][2 * ((k) & 1) + (d)])); }
#define LI 1
#define TT8(s, q) f32x8{T[s][2 * (q)][0], T[s][2 * (q)][1], T[s][2 * (q) + 1][0], T[s][2 * (q) + 1][1], T[s][2 * (q) + 4][0], T[s][2 * (q) + 4][1], T[s][2 * (q) + 5][0], T[s][2 * (q) + 5][1]}
#define SS8(s, q) f32x8{S1[s][4 * (q)], S1[s][4 * (q) + 1], S1[s][4 * (q) + 2], S1[s][4 * (q) + 3], S1[s][4 * (q) + 8], S1[s][4 * (q) + 9], S1[s][4 * (q) + 10], S1[s][4 * (q) + 11]}
#define DOT1(a_, b_) asm volatile("v_smfmac_f32_16x16x64_bf16 %0, %1, %2, %3 abid:1" : "+v"(c.dg) : "v"(a_), "v"(b_), "v"(c.six));
#define DOT(q, s) { if ((s) == 6) { DOT1(c.X[0], (SS8(6, q))) } else if ((s) < 2 * NJP) { DOT1(c.Y[(4 * ((s) >> 1) + ((s) & 1) + 2 * (q)) % 12], (TT8((s) % 6, q))) } \
            else { DOT1(c.X[1 + 4 * (((s) % 6) >> 1) + ((s) & 1) + 2 * (q)], (SS8(s, q))) } }
#define DPOS 0x0600000504070301ull
#define DPOS2 0x20
#define DP(n, j) if (n >= 4 && n < 6) { constexpr int s_ = (((DPOS | DPOS2) >> (4 * ((8 * n + j) & 15))) & 15); if (s_) DOT(0, (s_ + 6) % 7) }
#define XQ(n, j) (((n) & 3) * 4 + ((j) > 5 ? 4 : (j) > 3 ? 2 : (j) - 2))
#define PJ(n, j) { constexpr int i = SQ(n), i1 = SQ((n + 1) & 7), np = (n + 7) & 7, z = 8 * n + j, h = j >> 2; \
                constexpr bool nx = n < 7, pv = n >= 1, al = nx && n != 1; \
                SB \
                if (z == FLW + 4 * NE - 16) { wait_set<2>(flg, c.wf[1] + (OFF_FL - OFF_B - SLOT), c.slow); SB } \
                if (z == FLW - 16) { wait_set<1>(flg, c.wf[1] + (OFF_FL + FL1 - OFF_B - SLOT), c.slow); SB } \
                if (!(j & 1)) { if (z + WPRE <= 64) WAITCNT(63, WPRE - 2); SB } \
                if (j & 2) u[h][(j & 3) ^ h ^ 1] = mf0(wq[z], bq[(2 * i + h) & 3]); else u[h][(j & 3) ^ h ^ 1] = mf0(wq[z], bp[(2 * i + h) & 3]); \
                SB \
                if (z == FLR + 4 * NE - 16) flg = LDSV(c.wf[1] + (OFF_FL - OFF_B - SLOT)); \
                if (z == FLR - 16) flg = LDSV(c.wf[1] + (OFF_FL + FL1 - OFF_B - SLOT)); \
                if (z + WPRE < 64) wq[(z + WPRE) & 63] = WSEQ(z + WPRE); \
                SB \
                if (j == 0) { CB(bq, i, 0, 0) DP(n, 0) CB(bq, i, 0, 1) } \
                if (j == 1) { CB(bq, i, 0, 2) DP(n, 1) CB(bq, i, 1, 0) CB(bq, i, 1, 1) } \
                if (n >= 4 && (j == 2 || j == 3 || j == 5)) xpair<2, XQ(n, j), XQ(n, j + 1)>(c); \
                if (j == 2) { if (pv) F1(np, 1, 4) CB(bq, i, 1, 2) DP(n, 2) if (pv) { F1(np, 1, 0) F1(np, 1, 1) F1(np, 1, 2) F1(np, 1, 3) } } \
                if (j == 3) { if (pv) F1(np, 1, 5) DP(n, 3) if (pv) F1(np, 1, 6) } \
                if (j == 4) { if (al) CB(bp, i1, 0, 0) DP(n, 4) if (al) CB(bp, i1, 0, 1) } \
                if (j == 5) { F1(n, 0, 4) DP(n, 5) F1(n, 0, 5) F1(n, 0, 6) } \
                if (j == 6) { if (nx) CB(bp, i1, 0, 2) DP(n, 6) if (al) CB(bp, i1, 1, 0) F1(n, 0, 0) F1(n, 0, 1) } \
                if (LI && n == 7) { if (j == 4 || j > 5) { FS1(0, j == 4 ? 2 : j & 1, 0) FS1(0, j == 4 ? 2 : j & 1, 1) } } \
                else if (j == 7) { \
                    F1(n, 0, 2) F1(n, 0, 3) \
                    if (al) CB(bp, i1, 1, 1) \
                    if (nx) CB(bp, i1, 1, 2) \
                    FM(n, 0, 0) FM(n, 0, 1) FM(n, 0, 2) \
                    if (nx) { SC(i1, 0) SC(i1, 1) SC(i1, 2) } else asm volatile("s_nop 3"); \
                    FM(n, 1, 0) FM(n, 1, 1) FM(n, 1, 2) \
                    if (n == 3) { f32x2 a_, b_; asm volatile("v_mov_b64 %0, 0\n\tv_mov_b64 %1, 0" : "=v"(a_), "=v"(b_)); c.dg = f32x4{a_[0], a_[1], b_[0], b_[1]}; } \
                } }
        static_assert(FLW + WPRE <= 32 && FLZ <= FLR && FLR < FLW, "");
#define CP 9
#define PN(n) TI(n) if (n == CP) { __builtin_amdgcn_s_setprio(0); SB } PJ(n, 0) PJ(n, 1) PJ(n, 2) PJ(n, 3) PJ(n, 4) PJ(n, 5) PJ(n, 6) PJ(n, 7)
        PN(0)
        PN(1)
        TG(c.tq[2])
        PN(2)
        PN(3)
        TG(c.tq[3])
        PN(4) PN(5) PN(6) PN(7) TI(8)
        unsigned s8;
        double big;
#define SU0 { SB s8 = __builtin_amdgcn_mbcnt_lo((unsigned)act, 0u); asm volatile("" :: "v"(s8)); SB }
#define SU1 { SB s8 = __builtin_amdgcn_mbcnt_hi((unsigned)(act >> 32), s8); asm volatile("" :: "v"(s8)); SB }
#define SU2 { SB s8 <<= 3; asm volatile("" :: "v"(s8)); SB }
#define SU3 { SB big = bigs; PIN(big); SB }
#undef DOT1
#define DOT1(a_, b_) asm volatile("v_smfmac_f32_16x16x64_bf16 %0, %1, %2, %3 abid:1" :: "v"(c.dg), "v"(a_), "v"(b_), "v"(c.six));
        if (LI) {
            DOT(1, 4) SU0 FS1(1, 0, 0) DOT(1, 0) FS1(1, 0, 1) FS1(1, 1, 0) DOT(1, 2) FS1(1, 1, 1) FS1(1, 2, 0) DOT(1, 1) FS1(1, 2, 1) F1(7, 1, 6)
            DOT(1, 3) SU1 DOT(1, 5) SU2 DOT(1, 6) SU3
        } else {
            SU0 SU1
            DOT(1, 0) SU2 DOT(1, 2) F1(7, 1, 6) DOT(1, 1) SU3 DOT(1, 3) DOT(1, 4) DOT(1, 5) DOT(1, 6)
        }
        asm volatile("s_mov_b64 exec, %1" : "+v"(c.dg) : "s"(act));
        snop(LI ? 5 : 6);
        SB
        const bool b0 = __builtin_amdgcn_inverse_ballot_w64(m1), b1 = __builtin_amdgcn_inverse_ballot_w64(m2);
        const float va = b0 ? c.dg[1] : c.dg[0], vb = b0 ? c.dg[3] : c.dg[2], v = b1 ? vb : va;
        TG(c.tq[4])
        STAMP(4)
        TS(c.ts[14])
        deliver(c, v, big, 2 * nq + 1, s8, fk, sums, outp);
        ALL
        const int ln = __builtin_amdgcn_mbcnt_hi(~0u, __builtin_amdgcn_mbcnt_lo(~0u, 0u));
        const int lane = ln, tid = w * 64 + ln;
        (void)lane; (void)tid;
        FINISH(c)
    }
}

extern "C" void kernel_launch(void* const* d_in, const int* in_sizes, int n_in,
                              void* d_out, int out_size, void* d_ws, size_t ws_size,
                              hipStream_t stream) {
    const int* idx = (const int*)d_in[0];
    const uint16_t* table = (const uint16_t*)d_in[1];
    const uint16_t* se_w1 = (const uint16_t*)d_in[2];
    const uint16_t* se_w2 = (const uint16_t*)d_in[3];
    const uint16_t* w_bi = (const uint16_t*)d_in[4];
    const uint16_t* w_cross = (const uint16_t*)d_in[5];
    const uint16_t* w_deep1 = (const uint16_t*)d_in[6];
    const uint16_t* w_deep2 = (const uint16_t*)d_in[7];
    (void)hipFuncSetAttribute((const void*)k_main, hipFuncAttributeMaxDynamicSharedMemorySize, LDS_T);
    k_main<<<256, 512, LDS_T, stream>>>(idx, table, w_bi, w_deep1, w_cross, (unsigned*)d_ws, se_w1, se_w2, w_deep2, (__hip_bfloat16*)d_out, (unsigned long long*)((char*)d_ws + (128ull << 20)));
}
```

```cpp
#include <hip/hip_runtime.h>
#include <hip/hip_bf16.h>
#include <stdint.h>

#define EXM 1
#define PU 0
#define WSX 0
#define TB 0x2344
#define TBP 4
#define SMP 0
#define PRO 4
#define B1P 0
#define L1M 8
#define TGE 0
#define PT 7
#define NF 26
#define TRC 1.0028f
#define NP 13
#define PROD_CLASS(slot) ((((slot) & 3) == 1) && (((slot) >> 2) & 1) == 0)
#define STAMP(k)
#define TS(x)
#define TSP(P)
#define TX(k)
#define TP(x)
#define TEM 0x3ff
#define TE(x)
#define TI(n)
#define TF(k)
#define TH(k)
#define TG(x) TE(x)
using bf16x8 = __attribute__((ext_vector_type(8))) short;
using f32x4 = __attribute__((ext_vector_type(4))) float;
using f32x8 = __attribute__((ext_vector_type(8))) float;
using f32x2 = __attribute__((ext_vector_type(2))) float;
using s16x4 = __attribute__((ext_vector_type(4))) short;
using u32x4 = __attribute__((ext_vector_type(4))) unsigned;
using u32x8 = __attribute__((ext_vector_type(8))) unsigned;
using u32x2 = __attribute__((ext_vector_type(2))) unsigned;
using bf2 = __attribute__((ext_vector_type(2))) __bf16;
#define ST "\n\tSTUB"
#define ST_ "STUB\n\t"
#define PIN(x) asm volatile("" : "+v"(x))
#define PINS(x) asm volatile("" : "+s"(x))
#define SB __builtin_amdgcn_sched_barrier(0);
#define CAT8(a, b) __builtin_shufflevector((a), (b), 0, 1, 2, 3, 4, 5, 6, 7)

#define SLOT 16384
#define OFF_B 65536
#define OFF_FL 131072
#define OFF_WBI (OFF_FL + 4096)
#define OFF_SW1 (OFF_WBI + 2048)
#define OFF_SW2 (OFF_SW1 + 1024)
#define OFF_WD2 (PRO & 2 ? OFF_SW2 + 1024 : OFF_SW1 + 768)
#define OFF_TAG (OFF_SW2 + 1280)
#define OFF_NB (OFF_TAG + 768)
#define OFF_JNK (OFF_NB + 2048)
#define OFF_IDX (OFF_JNK + 2048)
#define OFF_RAW (OFF_IDX + 8192)
#define LDS_T (OFF_IDX + 16384)
constexpr int FOFF[4] = {SLOT, OFF_B + SLOT, 2 * SLOT, OFF_B + 2 * SLOT};
#define P_FLAG 3
#define P_POLL 6
#define P_ST 3
#define P_CHK 9
#define P_STAT 8

#define WS_FLAG 1024
#define WS_SUM 4096
#define WS_SIDE 65536
#define WS_FRAG (1 << 20)

#define LDS(T, a) (*(__attribute__((address_space(3))) T*)(size_t)(a))
#define LDSV(a) (*(volatile const __attribute__((address_space(3))) unsigned*)(size_t)(a))
__device__ __forceinline__ unsigned pk_bf16(float lo, float hi) {
    const f32x2 v = {lo, hi};
    return __builtin_bit_cast(unsigned, __builtin_convertvector(v, bf2));
}
__device__ __forceinline__ unsigned cvt_o(float lo, float hi) { unsigned r; asm volatile("v_cvt_pk_bf16_f32 %0, %1, %2" ST : "=v"(r) : "v"(lo), "v"(hi)); return r; }
__device__ __forceinline__ float exp_o(float a) { float r; asm volatile("v_exp_f32 %0, %1" : "=v"(r) : "v"(a)); return r; }
template <typename A, typename B> __device__ __forceinline__ f32x4 mf0(const A& a, const B& b) { f32x4 d; asm volatile("v_mfma_f32_16x16x32_bf16 %0, %1, %2, 0" ST : "=v"(d) : "v"(a), "v"(b)); return d; }
template <typename A, typename B> __device__ __forceinline__ void mfa(f32x4& d, const A& a, const B& b) { asm volatile("v_mfma_f32_16x16x32_bf16 %0, %1, %2, %0" ST : "+v"(d) : "v"(a), "v"(b)); }
__device__ __forceinline__ float lo_f(unsigned x) { return __uint_as_float(x << 16); }
__device__ __forceinline__ float hi_f(unsigned x) { return __uint_as_float(x & 0xffff0000u); }
__device__ __forceinline__ s16x4 tr_read(unsigned a) {
    typedef short v4s __attribute__((ext_vector_type(4)));
    return __builtin_amdgcn_ds_read_tr16_b64_v4i16((__attribute__((address_space(3))) v4s*)(size_t)a);
}
#define DMA(name, ins) __device__ __forceinline__ void name(unsigned voff, const void* sbase, unsigned ldsaddr) { \
    asm volatile("s_mov_b32 m0, %2\n\ts_nop 0\n\t" ins :: "v"(voff), "s"(sbase), "s"(ldsaddr) : "memory"); }
DMA(dma16, "global_load_lds_dwordx4 %0, %1") DMA(dma4, "global_load_lds_dword %0, %1")
DMA(dma16sc, "global_load_lds_dwordx4 %0, %1 sc1") DMA(dma4sc, "global_load_lds_dword %0, %1 sc1")
__device__ __forceinline__ void st16sc(unsigned voff, void* sbase, u32x4 v) {
    asm volatile("global_store_dwordx4 %0, %1, %2 sc1\n\ts_nop 1" :: "v"(voff), "v"(v), "s"(sbase) : "memory");
}
template <int O> __device__ __forceinline__ void set_m0(unsigned base) { asm volatile("s_add_u32 m0, %0, %1" :: "s"(base), "n"(O) : "memory", "scc"); }
template <int O> __device__ __forceinline__ void bdma16o(unsigned voff, u32x4 rs, unsigned soff) {
    asm volatile("buffer_load_dwordx4 %0, %1, %2 offen offset:%3 lds" :: "v"(voff), "s"(rs), "s"(soff), "n"(O) : "memory");
}
template <int O> __device__ __forceinline__ void dma16o(unsigned voff, const void* sbase) {
    asm volatile("global_load_lds_dwordx4 %0, %1 offset:%2" :: "v"(voff), "s"(sbase), "n"(O) : "memory");
}
template <int O> __device__ __forceinline__ void dma16sco(unsigned voff, const void* sbase) {
    asm volatile("global_load_lds_dwordx4 %0, %1 offset:%2 sc1" ST :: "v"(voff), "s"(sbase), "n"(O) : "memory");
}
#define WAITCNT(vm, lgkm) __builtin_amdgcn_s_waitcnt(((vm) & 15) | (((vm) >> 4) << 14) | 0x70 | ((lgkm) << 8))
#define WAITX(vm, x, m) { WAITCNT((vm) + (x), 0); asm volatile("s_cmp_eq_u32 %0, 0\n\ts_cbranch_scc1 1f\n\ts_waitcnt vmcnt(%1)\n1:" :: "s"(m), "n"(vm) : "memory", "scc"); }
#define BARRIER asm volatile("s_barrier" ST ::: "memory")
extern "C" __device__ void snop(short) __asm("llvm.amdgcn.s.nop");
#define USE(x) asm volatile("" :: "v"(x))
__device__ __forceinline__ float sum32(float v) { const u32x2 p = __builtin_amdgcn_permlane32_swap(__float_as_uint(v), __float_as_uint(v), false, false); return __uint_as_float(p[0]) + __uint_as_float(p[1]); }
__device__ __forceinline__ float sum16(float v) { const u32x2 p = __builtin_amdgcn_permlane16_swap(__float_as_uint(v), __float_as_uint(v), false, false); return __uint_as_float(p[0]) + __uint_as_float(p[1]); }
__device__ __forceinline__ float sel(unsigned m, float a, float b) {
    return __uint_as_float(__builtin_amdgcn_bitop3_b32(m, __float_as_uint(a), __float_as_uint(b), 0xca));
}
#define SLOW(b)
template <int b> __device__ __forceinline__ void wait_set(unsigned f, unsigned a, unsigned& slow) {
    if (__builtin_expect(__builtin_amdgcn_ballot_w64(f == 0u) != 0ull, (WSX >> b) & 1)) {
        SLOW(b)
        while (__builtin_amdgcn_ballot_w64(LDSV(a) == 0u) != 0ull) { }
    }
    asm volatile("" ::: "memory");
}

struct Com {
    char* ws;
    unsigned lane16, ldsb, slow = 0;
    int c, nq, rho, st;
};
typedef __attribute__((address_space(1))) char* gp;
struct Fin { unsigned kr, kl; double c0; unsigned long long last, lim; };
__device__ __forceinline__ void fin(Fin& k) {
    k.kr = 0x7d000000u; k.kl = 0xde000000u; k.c0 = -0x1.1p11; k.last = 0x1c00000000000000ull; k.lim = 0x1c04000000000000ull;
    PINS(k.kr); PINS(k.kl); PINS(k.c0); PINS(k.last); PINS(k.lim);
}
__device__ __forceinline__ void deliver(const Com& c, float v, double big, int part, unsigned s8, Fin k, gp sums, gp outp) {
    double mine = __builtin_fma((double)v, 0x1p-969, big);
    const bool far = (__float_as_uint(v) << 1) + k.kr < k.kl;
    if (__builtin_expect(__builtin_amdgcn_ballot_w64(far) != 0ull, 0)) {
        if (far) {
            mine = __builtin_bit_cast(double, u32x2{0u, 0x04004000u + (0x40000u << part)});
            __hip_atomic_store((__attribute__((address_space(1))) float*)((gp)c.ws + (WS_SIDE + c.st * 2048 + c.c * 64 + part * 256) + (s8 >> 1)), v, __ATOMIC_RELAXED, __HIP_MEMORY_SCOPE_AGENT);
        }
        asm volatile("s_waitcnt vmcnt(0)" ::: "memory");
        k.lim = 0ull;
    }
    SB
    const unsigned long long ret = __hip_atomic_fetch_add((__attribute__((address_space(1))) unsigned long long*)(sums + s8), __builtin_bit_cast(unsigned long long, mine), __ATOMIC_RELAXED, __HIP_MEMORY_SCOPE_AGENT);
    SB
    double own = __builtin_fma(mine, 0x1p969, k.c0);
    unsigned s2 = s8 >> 2;
    PIN(own); PIN(s2);
    SB
    own = __builtin_fma(__builtin_bit_cast(double, ret), 0x1p585, own);
    SB
    asm volatile("v_cmpx_le_u64 vcc, %0, %1" :: "s"(k.last), "v"(ret), "v"(own) : "vcc");
    SB
    float r = (float)own;
    asm volatile("" :: "v"(r));
    SB
    if (__builtin_expect(__builtin_amdgcn_ballot_w64(ret >= k.lim) != 0ull, 0)) {
        const unsigned long long tot = ret + __builtin_bit_cast(unsigned long long, mine);
        const unsigned th = (unsigned)(tot >> 32);
        r = (float)__builtin_fma(__builtin_bit_cast(double, tot & 0xfc03ffffffffffffull), 0x1p521, -0x1.2p10);
#pragma unroll 1
        for (int q = 0; q < 8; ++q)
            if ((th >> (18 + q)) & 1u) r += __hip_atomic_load((__attribute__((address_space(1))) float*)((gp)c.ws + (WS_SIDE + c.st * 2048 + c.c * 64 + q * 256) + (s8 >> 1)), __ATOMIC_RELAXED, __HIP_MEMORY_SCOPE_AGENT);
    }
    *(__attribute__((address_space(1))) uint16_t*)(outp + s2) = (uint16_t)pk_bf16(r, r);
}
#define BIG 0x1.04p-959

struct Deep : Com {
    u32x4 rw1;
    const char* wsf[2]; const char* wsh[2];
    unsigned ldsh;
    unsigned sG[4], sB, tb;
    unsigned m1, m3, m4;
    int unit;
    unsigned long long pmask;
    unsigned xa;
    unsigned one;
    unsigned tad, tag, pl;
    unsigned boff[4], ra[4], rb[4];
    u32x4 w2[2];
    f32x4 ac[4][4];
    bf16x8 fa[2][4];
    s16x4 lo[2][4], hi[2][4];
};
template <int Q> __device__ __forceinline__ void prepB(Deep& c) {
    set_m0<OFF_B + (Q & 3) * SLOT>(c.ldsb);
    SB
    if constexpr (Q == 0) c.sB = 24 * 32768; else c.sB = c.sG[(Q - 1) & 3] + ((Q - 1) >> 2) * (8 * 32768);
    asm volatile("" :: "s"(c.sB));
    SB
}
template <int I> __device__ __forceinline__ void issueB(Deep& c) { bdma16o<I * 1024>(c.boff[I], c.rw1, c.sB); }
__device__ __forceinline__ void mkboff(Deep& d, int lane, int cw, int nq) {
    const unsigned b0 = (unsigned)(((cw & 1) * 64 + (cw >> 1) * 32 + (lane >> 3)) * 512 + nq * 128 + ((lane & 7) ^ ((lane >> 3) & 2)) * 16);
    d.boff[0] = b0; d.boff[1] = (b0 ^ 64u) + 3072; d.boff[2] = b0 + 6144; d.boff[3] = (b0 ^ 64u) + 9216;
#pragma unroll
    for (int k = 0; k < 4; ++k) PIN(d.boff[k]);
}
template <int P, int N> __device__ __forceinline__ void rd1(Deep& c) {
    constexpr int tab[12] = {0, 4, 5, 6, 7, 1, 2, 3, 8, 9, 10, 11}, k = tab[N < 12 ? N : 0];
    if constexpr (N >= 12) { }
    else if constexpr (k < 4) c.fa[P & 1][k] = LDS(const bf16x8, c.ra[k] + (P & 3) * SLOT);
    else if constexpr (k & 1) c.hi[P & 1][(k - 4) >> 1] = tr_read(c.rb[(k - 4) >> 1] + (P & 3) * SLOT + 512);
    else c.lo[P & 1][(k - 4) >> 1] = tr_read(c.rb[(k - 4) >> 1] + (P & 3) * SLOT);
    SB
}
template <int P, int M, int K> __device__ __forceinline__ void mk(Deep& c) {
    if constexpr (P >= 0) mfa(c.ac[M][K], CAT8(c.lo[P & 1][K], c.hi[P & 1][K]), c.fa[P & 1][M]);
    SB
}
#define DEF11 0
template <int P, int N> __device__ __forceinline__ void m1(Deep& c) { if constexpr (!(DEF11 && P == 11 && ((N >> 1) & 3) == 0)) mk<P, (N >> 1) & 3, (N >> 3) * 2 + (N & 1)>(c); }
template <int P, int M> __device__ __forceinline__ void m4(Deep& c) { mk<P, M, 0>(c); mk<P, M, 1>(c); mk<P, M, 2>(c); mk<P, M, 3>(c); }
#define E9 2
#define T9 0
#define TOP10 4
constexpr int dyoung(int S) { return S < 7 ? 8 : S < 9 ? 4 : E9; }
template <int U0, int I> __device__ __forceinline__ void dq(Deep& c) {
    constexpr int U = U0 + (I >> 2);
    if constexpr (U < NP) {
        if constexpr ((I & 3) == 0 && U >= 10) prepB<U>(c);
        issueB<I & 3>(c);
        if constexpr (U == 12 && (I & 3) == 3) set_m0<FOFF[1]>(c.ldsb);
    } else if constexpr (U == 15) {
        dma16sco<(I & 1) * 1024>(c.lane16, c.wsh[(I >> 1) & 1]);
        if constexpr ((I & 3) == 3) set_m0<FOFF[3]>(c.ldsb);
    } else {
        dma16sco<(I & 3) * 1024>(c.lane16, c.wsf[U == 16]);
        if constexpr (U == 14 && (I & 3) == 3) set_m0<FOFF[2]>(c.ldsh);
    }
    SB
}
template <int S, int I0, int I1> __device__ __forceinline__ void dissue(Deep& c) {
    if constexpr (I0 < I1 && I0 < 4 && S < 7) { dq<S + 3, I0>(c); dissue<S, I0 + 1, I1>(c); }
}
constexpr int cum(unsigned long long t, int n) { int k = 0; for (int i = 0; i < n; ++i) k += (t >> (4 * i)) & 15; return k; }
template <int U0, int I0, int I1> __device__ __forceinline__ void dqs(Deep& c) { if constexpr (I0 < I1) { dq<U0, I0>(c); dqs<U0, I0 + 1, I1>(c); } }
template <int P, int N0, int N1> __device__ __forceinline__ void rdn(Deep& c) { if constexpr (N0 < N1) { rd1<P, N0>(c); rdn<P, N0 + 1, N1>(c); } }
__device__ __forceinline__ unsigned tag_addr(int lane, int unit, int nq, int st, int xcd) {
    const unsigned l4 = (unsigned)lane * 4u;
    const unsigned a0 = (unsigned)(WS_FLAG + unit * 4) + l4 * 32u;
    const unsigned a1 = (((unsigned)(nq * 256) + l4 * 64u) & 768u) + (unsigned)(st * 4);
    const unsigned a2 = (unsigned)(WS_FLAG + xcd * 128 - 64) + l4;
    const unsigned lo = l4 < 32u ? a0 : a1, hi = l4 < 168u ? a2 : (unsigned)(st * 4);
    return l4 < 64u ? lo : hi;
}
__device__ __forceinline__ void tags(unsigned tad, const void* ws, unsigned lds, unsigned long long m) {
    asm volatile("s_mov_b32 m0, %2\n\ts_mov_b64 exec, %3\n\tglobal_load_lds_dword %0, %1 sc1\n\ts_mov_b64 exec, -1" :: "v"(tad), "s"(ws), "s"(lds), "s"(m) : "memory");
}
__device__ __forceinline__ void publish(Deep& c, unsigned long long m) {
    asm volatile("s_cmp_eq_u64 %3, 0\n\ts_cbranch_scc1 1f\n\ts_mov_b64 exec, %3\n\tglobal_atomic_add %0, %1, %2\n\ts_mov_b64 exec, -1\n1:"
                 :: "v"(c.tad), "v"(c.one), "s"(c.ws), "s"(m) : "memory", "scc");
}
__device__ __forceinline__ void rawo(const Deep& d, unsigned lane16, int cw, unsigned (&ro)[3]) {
    const unsigned r0 = (lane16 >> 6) & 15u, u = (unsigned)((cw >> 1) * (NF * NF * 128) + d.unit * 128 + (cw & 1) * 64) + ((lane16 ^ (lane16 >> 3 & 0x20u)) & 0x30u);
    ro[0] = r0 * (NF * 128) + u; ro[1] = min(r0 + 16u, NF - 1u) * (NF * 128) + u - 1024u; ro[2] = (lane16 >> 2) + (unsigned)(cw * 256);
}
__device__ __forceinline__ void raw(const Deep& d, unsigned m1, int cw, const void* w_cross, const void* ws, unsigned (&ro)[3]) {
    if (__builtin_expect(m1 == 0u, 1)) {
        dma16(ro[0], w_cross, OFF_RAW + cw * 2048);
        dma16o<1024>(ro[1], w_cross);
        dma4sc(ro[2], ws, OFF_NB + cw * 256);
        set_m0<OFF_B>(d.ldsb); snop(0);
    }
    SB
}
template <int W> __device__ __forceinline__ void produce(Deep& c) {
    if (c.m1 == 0u) {
        const int unit = c.unit;
        const unsigned p8 = (c.lane16 << 4) & 0x300u, e2 = (c.lane16 >> 5) & 0x1eu;
        const unsigned rb = (unsigned)(OFF_RAW + c.c * 2048) + e2, rs = rb + (p8 >> 1) + (c.lane16 & 0x20u);
        const unsigned dm = p8 == (c.lane16 & 0x300u) ? ~0u : 0u;
        const int m = unit < 24 ? 2 * (unit & 6) + (unit & 1) + ((unit >> 2) & 2) : 4 * (unit & 1) + 2;
        unsigned fo = (unsigned)(WS_FRAG + (c.c & 1) * 131072 + (c.c >> 1) * 2048 + (unit >> 4) * 1024 + m * 64) + (((e2 & 12u) << 1) | ((e2 >> 2) & 4u) | (e2 & 2u)) * 2048u + ((c.lane16 & 0x30u) ^ (unsigned)((m & 4) << 3));
        if (unit >= 24) fo += (c.lane16 << 1) & 0x200u;
        WAITCNT(W, 15);
        asm volatile("" ::: "memory");
        u32x4 fr[2];
#pragma unroll
        for (int z = 0; z < 2; ++z) {
#pragma unroll
            for (int d = 0; d < 4; ++d) {
                const unsigned rp = (d < 3 ? rs ^ (unsigned)(z * 32) : rb + z * 32) + (unsigned)(512 * d);
                fr[z][d] = LDS(const uint16_t, rp) | ((unsigned)LDS(const uint16_t, rp + 64) << 16);
            }
            fr[z][3] &= dm;
        }
        st16sc(fo, c.ws, fr[0]); st16sc(fo + 65536u, c.ws, fr[1]);
        if (unit >= 24) { st16sc(fo ^ 512u, c.ws, u32x4{0u, 0u, 0u, 0u}); st16sc((fo ^ 512u) + 65536u, c.ws, u32x4{0u, 0u, 0u, 0u}); }
    }
    SB
}
template <bool L, int I> __device__ __forceinline__ void part(Deep& c) {
#define PART(cmp) asm volatile(cmp " %0, %1\n\ts_cbranch_scc1 1f\n\tbuffer_load_dwordx4 %2, %3, %4 offen offset:%5 lds\n1:" \
                               :: "s"(c.tb), "n"(I), "v"(c.boff[I]), "s"(c.rw1), "s"(c.sB), "n"(I * 1024) : "memory")
    SB
    if constexpr (L) PART("s_cmp_gt_u32"); else PART("s_cmp_le_u32");
    SB
}
__device__ __forceinline__ void check_flags(Deep& c) {
    if (c.m3 == 0u) {
        const unsigned l4 = c.lane16 >> 2;
        const unsigned long long open = __builtin_amdgcn_ballot_w64(c.tag == c.pl) & (c.nq ? 0x7ffffff0000ull : 0x3ffffff0000ull);
        if (__builtin_expect(open != 0ull, 0)) {
            unsigned& slow = c.slow; (void)slow; SLOW(4)
#pragma unroll 1
            for (int it = 0; it < (1 << 22); ++it) {
                tags(c.tad, c.ws, OFF_TAG + 256, 0x7ffffff0000ull);
                asm volatile("s_waitcnt vmcnt(0)" ::: "memory");
                if ((__builtin_amdgcn_ballot_w64(LDSV(OFF_TAG + 256 + l4) == c.tag) & open) == 0ull) break;
                __builtin_amdgcn_s_sleep(2);
            }
        }
    }
    SB
}
__device__ __forceinline__ void fetch_status(Deep& c) {
    if (c.m4 == 0u) {
        if (c.nq) dma16sc(c.lane16, c.ws, OFF_NB + 1024);
        else tags(c.tad, c.ws, OFF_TAG + 512, 0xe00ull);
    }
    SB
}
constexpr bool two(int P, int S) { return P == S || P == S + 1; }
constexpr int xk(int P, int k) {
    int x = 0;
    if (k == 1 || k == 2 || k == 4) x += two(P, P_ST) + (two(P, P_POLL) && P + 1 < P_CHK);
    if (k == 4 && P_FLAG != P_ST) x += two(P, P_FLAG);
    if (k == 2 || k == 4) x += P == 9 ? P_STAT == 9 : two(P, P_STAT) && !(P == 8 && P_POLL >= P_STAT);
    return x;
}
constexpr int below(int P, int v) { int b = 0; for (int k = 0; k < 5; ++k) if (xk(P, k) < v && xk(P, k) > b) b = xk(P, k); return b; }
constexpr unsigned xbits(int k) {
    unsigned m = 0;
    for (int P = 0; P < 10; ++P) { int v = below(P, 99); for (int j = 0; v > 0; ++j, v = below(P, v)) if (xk(P, k) == v) m |= 1u << (3 * P + j); }
    return m;
}
template <int P> __device__ __forceinline__ void xwait(Deep& c) {
    constexpr int d = dyoung(P), a = below(P, 99), b = below(P, a), e = below(P, b), f = below(P, e);
    static_assert(f == 0, "");
    WAITCNT(d + a, 0);
#define XW(n) "s_bitcmp1_b32 %0, %" #n "\n\ts_cbranch_scc1 1f\n\ts_waitcnt vmcnt(%"
    if constexpr (e > 0) asm volatile(XW(1) "4)\n\t" XW(2) "5)\n\t" XW(3) "6)\n1:" :: "s"(c.xa), "n"(3 * P), "n"(3 * P + 1), "n"(3 * P + 2), "n"(d + b), "n"(d + e), "n"(d) : "memory", "scc");
    else if constexpr (b > 0) asm volatile(XW(1) "3)\n\t" XW(2) "4)\n1:" :: "s"(c.xa), "n"(3 * P), "n"(3 * P + 1), "n"(d + b), "n"(d) : "memory", "scc");
    else if constexpr (a > 0) asm volatile(XW(1) "2)\n1:" :: "s"(c.xa), "n"(3 * P), "n"(d) : "memory", "scc");
}
#define DTOP 2
#define DTOPB 4
#define A8 12
#define A9 12
#define A10 12
#define T8 0x842100
#define T10 0xaa
constexpr int adv(int P) { return P == 8 ? A8 : P == 9 ? A9 : P == 10 ? A10 : 0; }
constexpr int bits(unsigned t, int n) { return __builtin_popcount(n < 32 ? t & ((1u << n) - 1u) : t); }
template <int P, int N> __device__ __forceinline__ void drun(Deep& c) {
    constexpr int old = 16 - adv(P - 1), tot = old + adv(P), U0 = P == 8 ? 10 : 11, E = P == 8 ? 4 : P == 10 ? 8 : E9, I0 = P == 10 ? TOP10 : 0, per = old >= 12 ? 1 : old >= 6 ? 2 : old >= 4 ? 3 : 12;
    constexpr unsigned T = P == 8 ? T8 : P == 10 ? T10 : T9;
    if constexpr (P == 10 && N == 0) dqs<U0, E9, TOP10>(c);
    dqs<U0, I0 + bits(T, N), I0 + bits(T, N + 1)>(c);
    if constexpr (N == 0 && per == 12) rdn<P, 0, 12>(c);
    if constexpr (N < old) m1<P - 1, adv(P - 1) + N>(c); else m1<P, N - old>(c);
    if constexpr (per < 12) rdn<P, N * per, (N * per + per < 12 ? N * per + per : 12)>(c);
    if constexpr (N == 5) { TX(0) }
    if constexpr (N == 11) { TX(1) }
    if constexpr (N == 17) { TX(2) }
    if constexpr (N + 1 < tot) drun<P, N + 1>(c); else { dqs<U0, I0 + bits(T, tot), E>(c); TX(3) }
}
template <int P, int N> __device__ __forceinline__ void mrest(Deep& c) { if constexpr (N < 16) { m1<P, N>(c); mrest<P, N + 1>(c); } }
#define T11 0x1249
#define NE 6
#define TOP11 1
#define E11 0
#define R12 6
constexpr int mn(int a, int b) { return a < b ? a : b; }
constexpr bool VT = TB != 0x4444 || TBP != 4;
constexpr int TMIN = mn(mn(mn(TB & 15, (TB >> 4) & 15), mn((TB >> 8) & 15, TB >> 12)), TBP);
#define DT 0x2222222
#define DL 12
constexpr int dtop(int P) { return (P & 3) == 2 ? (VT ? TMIN : DTOPB) : (DT >> (4 * P)) & 15; }
template <int P, int N> __device__ __forceinline__ void dbody(Deep& c) {
    constexpr int top = dtop(P);
    m1<P - 1, N>(c);
    rd1<P, N>(c);
    if constexpr (VT && (P & 3) == 2) { if constexpr (N >= DL && N - DL + TMIN < 4) part<true, (N - DL + TMIN) & 3>(c); }
    else if constexpr (N >= DL) dissue<P, top + N - DL, (N == 15 ? 16 : top + N - DL + 1)>(c);
    if constexpr (N < 15) dbody<P, N + 1>(c);
}
template <int N> __device__ __forceinline__ void dlast(Deep& c) {
    dqs<14, TOP11 + bits(T11, N), TOP11 + bits(T11, N + 1)>(c);
    m1<11, N>(c);
    if constexpr (N == R12) { WAITCNT(TOP11 + bits(T11, N + 1), 15); SB }
    if constexpr (N >= R12 && N < R12 + 8) rd1<12, (N - R12 < 4 ? N - R12 + 1 : N - R12 + 4)>(c);
    if constexpr (N < 15) dlast<N + 1>(c); else dqs<14, TOP11 + bits(T11, 16), NE>(c);
}
template <int P> struct DStep {
    static __device__ __forceinline__ void run(Deep& c) {
        TSP(P)
        SB
        if constexpr (P == P_FLAG || P == P_ST) publish(c, P_FLAG == P_ST ? c.pmask : c.pmask & (P == P_ST ? 0x100ull : 0xffull));
        if constexpr (P == P_CHK) check_flags(c);
        if constexpr (P == P_STAT) fetch_status(c);
        if constexpr (P < 8) {
            dissue<P, 0, dtop(P)>(c);
            if constexpr (VT && (P & 3) == 2) {
                if constexpr (TMIN == 0) part<false, 0>(c);
                if constexpr (TMIN <= 1) part<false, 1>(c);
                if constexpr (TMIN <= 2) part<false, 2>(c);
                if constexpr (TMIN <= 3) part<false, 3>(c);
            }
            dbody<P, 0>(c);
        } else if constexpr (P <= 10) {
            drun<P, 0>(c);
            if constexpr (P == 10 && E11) { WAITCNT(4, 15); SB rdn<11, 1, 5>(c); rdn<11, 8, 12>(c); }
        } else {
            if constexpr (PU == 11) __builtin_amdgcn_s_setprio(2);
            rd1<11, 0>(c); rdn<11, 5, 8>(c);
            dqs<14, 0, TOP11>(c);
            if constexpr (!E11) { WAITCNT(4 + TOP11, 15); SB rdn<11, 1, 5>(c); rdn<11, 8, 12>(c); }
            mrest<10, A10>(c);
            rd1<12, 0>(c); rdn<12, 5, 8>(c);
            TS(c.ts[12])
            if constexpr (PU == 12) __builtin_amdgcn_s_setprio(2);
            dlast<0>(c);
        }
        if constexpr (P <= 10) {
            if constexpr (P == 4) {
                c.w2[0] = LDS(const u32x4, c.w2[0][0]); c.w2[1] = LDS(const u32x4, c.w2[1][0]);
            }
            if constexpr (P == P_POLL) { if (c.m3 == 0u) tags(c.tad, c.ws, OFF_TAG + 256, 0x7ffffff0000ull); }
            SB
            if constexpr (P < 6) prepB<P + 4>(c);
#define NPT 0x10000000000ull
            if constexpr ((NPT >> (4 * P)) & 15) asm volatile(".rept %0\n\ts_nop 0\n\t.endr" :: "n"((int)((NPT >> (4 * P)) & 15)));
            if constexpr (P == 10) WAITCNT(63, 0); else xwait<(P < 10 ? P : 0)>(c);
            TX(4)
            if constexpr (P == P_CHK - 1) { SB c.pl = LDSV(OFF_TAG + 256 + (c.lane16 >> 2)); SB }
            BARRIER;
            DStep<P + 1>::run(c);
        }
    }
};

struct Cross : Com {
    const char* tb[4]; const char* wsf;
    unsigned ga[40];
    unsigned re[2], rx[2], rx0[2], six, wf[2], pl, ph, fl;
    unsigned long long xm[2];
    f32x4 dg;
    s16x4 wa[2];
    bf16x8 wb[2], wr[2];
    u32x4 fe[2][2][2];
    f32x4 ew[NF], zs[4];
    u32x4 X[NP], Y[12], bp[4];
    float k0, k1, k2;
    unsigned am, wad[2];
    u32x4 on[4], s1, zq;
};
template <int I> __device__ __forceinline__ void issueA(Cross& c, unsigned off) { dma16o<I * 1024>(off, c.tb[I]); }
template <int K, int S, int N> __device__ __forceinline__ void sw1(Cross& c) {
    constexpr int a = 1 + 4 * K + (S == 1 ? 2 : (S == 3 ? 1 : 0)), b = 1 + 4 * K + (S == 0 ? 1 : (S == 2 ? 2 : 3)), Q = N >> 2, R = N & 3;
    float x = c.ew[2 * a + Q][R], y = c.ew[2 * b + Q][R];
    if (S >= 2) asm volatile("v_permlane32_swap_b32 %0, %1" ST : "+v"(x), "+v"(y)); else asm volatile("v_permlane16_swap_b32 %0, %1" ST : "+v"(x), "+v"(y));
    c.ew[2 * a + Q][R] = x; c.ew[2 * b + Q][R] = y;
}
template <int K, int S, int N0 = 0, int N1 = 8> __device__ __forceinline__ void swn(Cross& c) { if constexpr (N0 < N1) { sw1<K, S, N0>(c); swn<K, S, N0 + 1, N1>(c); } }
#define S8 24
#define S9 24
template <int N0, int N1> __device__ __forceinline__ void swl(Cross& c) { if constexpr (N0 < N1 && N0 < 32) { sw1<2, (N0 >> 3), (N0 & 7)>(c); swl<N0 + 1, N1>(c); } }
constexpr int sw0(int P) { return P <= 3 ? 0 : P <= 5 ? 8 : P == 6 ? 20 : P == 7 ? 32 : P == 8 ? 40 : P == 9 ? 40 + S8 : P == 10 ? 40 + S9 : 64; }
template <int P, int N0, int N1> __device__ __forceinline__ void tsw(Cross& c) {
    if constexpr (N0 < N1 && sw0(P) + N0 < sw0(P + 1)) { sw1<((sw0(P) + N0) >> 5), ((sw0(P) + N0) >> 3) & 3, (sw0(P) + N0) & 7>(c); tsw<P, N0 + 1, N1>(c); }
}
constexpr int fbuf(int P) { return P & 1; }
template <int P, int N> __device__ __forceinline__ void cmm(Cross& c) {
    if constexpr (P >= 0) {
        constexpr int B = fbuf(P);
        if constexpr (N < 2)
            asm volatile("v_smfmac_f32_16x16x64_bf16 %0, %1, %2, %3" :: "v"(c.zs[P ? (P - 1) >> 2 : 3]),
                         "v"(c.on[P ? (P - 1) & 3 : 0]), "v"(__builtin_shufflevector(c.fe[B][N][0], c.fe[B][N][1], 0, 1, 2, 3, 4, 5, 6, 7)), "v"(c.six));
        else if constexpr (N < 4) c.ew[2 * P + (N & 1)] = mf0(P ? c.wb[0] : c.wr[0], c.fe[B][0][N & 1]);
        else mfa(c.ew[2 * P + (N & 1)], P ? c.wb[1] : c.wr[1], c.fe[B][1][N & 1]);
    }
    SB
}
template <int P, int N> __device__ __forceinline__ void rdE(Cross& c) {
    c.fe[fbuf(P)][N >> 1][N & 1] = LDS(const u32x4, c.re[N >> 1] + (P & 3) * SLOT + (N & 1) * 2048);
    SB
}
template <int K> __device__ __forceinline__ void zpack(Cross& c) {
#define ZP "v_add_f32 %1, %1, %2\n\tv_add_f32 %3, %3, %4\n\tv_cvt_pk_bf16_f32 %0, %1, %3"
    if constexpr (K == 2) asm volatile(ZP :: "v"(c.zq[2]), "v"(c.zs[K][0]), "v"(c.zs[K][1]), "v"(c.zs[K][2]), "v"(c.zs[K][3]));
    else {
        unsigned r;
        asm volatile(ZP : "=v"(r) : "v"(c.zs[K][0]), "v"(c.zs[K][1]), "v"(c.zs[K][2]), "v"(c.zs[K][3]));
        c.zq[K] = r;
    }
    SB
}
template <int K> __device__ __forceinline__ void zclear(Cross& c) { c.zs[K] = f32x4{0.f, 0.f, 0.f, 0.f}; PIN(c.zs[K]); SB }
#define NJP 3
template <int P> __device__ __forceinline__ void rdX(Cross& c) {
    constexpr int k = 1 + ((P - 1) & ~3), a = ((P - 1) & 3) * 32, b = (P & 3) * 32;
    constexpr bool jp = (P - 1) / 4 < NJP;
#define RDX "s_mov_b64 exec, %6\n\tds_read2st64_b64 %0, %4 offset0:%7 offset1:%8\n\tds_read2st64_b64 %1, %4 offset0:%9 offset1:%10\n\t" \
            "ds_read2st64_b64 %2, %5 offset0:%7 offset1:%8\n\tds_read2st64_b64 %3, %5 offset0:%9 offset1:%10\n\ts_mov_b64 exec, -1"
#define RDY "s_mov_b64 exec, %5\n\tds_read_b128 %0, %4 offset:%6\n\tds_read_b128 %1, %4 offset:%7\n\tds_read_b128 %2, %4 offset:%8\n\tds_read_b128 %3, %4 offset:%9\n\ts_mov_b64 exec, -1"
    if constexpr (P == 0) { const u32x2 lo = LDS(const u32x2, c.rx0[0]), hi = LDS(const u32x2, c.rx0[1]); c.X[0] = u32x4{lo[0], lo[1], hi[0], hi[1]}; }
    else if constexpr ((P & 3) == 2 && jp)
        asm volatile(RDY : "=&v"(c.X[k]), "=&v"(c.X[k + 1]), "=&v"(c.X[k + 2]), "=&v"(c.X[k + 3]) : "v"(c.rx[0]), "s"(c.xm[0]), "n"(a * 512), "n"(a * 512 + 2048), "n"(b * 512), "n"(b * 512 + 2048) : "memory");
    else if constexpr ((P & 3) == 0 && jp)
        asm volatile(RDY : "+v"(c.X[k]), "+v"(c.X[k + 1]), "+v"(c.X[k + 2]), "+v"(c.X[k + 3]) : "v"(c.rx[0]), "s"(c.xm[1]), "n"(a * 512), "n"(a * 512 + 2048), "n"(b * 512), "n"(b * 512 + 2048) : "memory");
    else if constexpr ((P & 3) == 2)
        asm volatile(RDX : "=&v"(c.X[k]), "=&v"(c.X[k + 1]), "=&v"(c.X[k + 2]), "=&v"(c.X[k + 3]) : "v"(c.rx[0]), "v"(c.rx[1]), "s"(c.xm[0]), "n"(a), "n"(b), "n"(a + 4), "n"(b + 4) : "memory");
    else if constexpr ((P & 3) == 0)
        asm volatile(RDX : "+v"(c.X[k]), "+v"(c.X[k + 1]), "+v"(c.X[k + 2]), "+v"(c.X[k + 3]) : "v"(c.rx[0]), "v"(c.rx[1]), "s"(c.xm[1]), "n"(a), "n"(b), "n"(a + 4), "n"(b + 4) : "memory");
    SB
}
template <int K, int N0, int N1> __device__ __forceinline__ void xpair(Cross& c) {
    if constexpr (N0 < N1 && K < NJP) {
        constexpr int m = N0 >> 2, d = N0 & 3, s = 1 + 4 * K + (d & 2), e = (m & 2) + (d & 1);
        unsigned r;
        asm volatile("v_perm_b32 %0, %1, %2, %3" ST : "=v"(r) : "v"(c.X[s + 1][e]), "v"(c.X[s][e]), "s"((m & 1) ? c.ph : c.pl));
        c.Y[4 * K + m][d] = r;
        xpair<K, N0 + 1, N1>(c);
    }
}
#define C11 1
#define G8 0x5421
#define G8P(n) { if constexpr ((G8 & 15) == (n)) GQ(10, 0) if constexpr (((G8 >> 4) & 15) == (n)) GQ(10, 1) if constexpr (((G8 >> 8) & 15) == (n)) GQ(10, 2) if constexpr (((G8 >> 12) & 15) == (n)) GQ(10, 3) }
#define FPOS 0x01504241
#define FFL 4
#define BP 1
#define FI1(k, n) if (((FPOS >> (8 * (k))) & 255) == (n)) { dma16sco<(k) * 1024>(c.lane16, c.wsf); SB }
#define FI(n) FI1(0, n) FI1(1, n) FI1(2, n) FI1(3, n) if ((n) == BP) { asm volatile("" ::: "memory"); SB __builtin_amdgcn_s_barrier(); SB asm volatile("" ::: "memory"); SB } if ((n) == FFL) { WAITCNT(0, 15); SB LDS(unsigned, c.fl) = c.fl; SB }
#define GQ(Q, J) { issueA<J>(c, c.ga[4 * (Q) - 12 + (J)]); if constexpr ((J) == 3) set_m0<(((Q) + 1) & 3) * SLOT>(c.ldsb); SB }
static_assert(FOFF[0] == SLOT, "");
__device__ __forceinline__ void csetup(Cross& c) {
            const unsigned r6 = c.lane16 * 4u + (unsigned)c.rho * 256u;
            const unsigned am = (c.lane16 >> 8) + (unsigned)c.rho;
            c.wf[0] = (r6 & 0x3c0u) | (((am << 4) ^ ((r6 >> 3) & 0x20u)) & 0x30u) | SLOT;
            c.wf[1] = c.wf[0] + OFF_B;
            const unsigned wap = OFF_SW2 + (c.lane16 & 0x3c0u) + ((c.lane16 >> 1) & 0x18u);
            c.wa[0] = tr_read(wap); c.wa[1] = tr_read(wap + 32);
            const unsigned r2 = (c.lane16 >> 3) & 30u, s1 = OFF_SW1 + min(r2, 24u), sg = s1 + (am & 3u) * 52u;
#pragma unroll
            for (int k = 0; k < 4; ++k) {
                const unsigned a = (k < 3 ? sg : s1) + k * 208;
                const unsigned v = LDS(const uint16_t, a) | ((unsigned)LDS(const uint16_t, a + 26) << 16);
                c.s1[k] = r2 < 26u ? v : 0u;
            }
            PIN(c.s1);
            PIN(c.wf[0]); PIN(c.wf[1]); PIN(c.wa[0]); PIN(c.wa[1]);
}
template <int P> struct CStep {
    static __device__ __forceinline__ void run(Cross& c) {
        TSP(P)
        if constexpr (P == 7) { STAMP(6) }
        SB
        if constexpr (P == 1) {
#pragma unroll
            for (int k = 0; k < 2; ++k) c.wr[k] = CAT8(tr_read(c.wad[1] + k * 1024), tr_read(c.wad[1] + k * 1024 + 128));
            SB
            cmm<0, 0>(c); rdE<1, 0>(c); set_m0<0>(c.ldsb); cmm<0, 1>(c); rdE<1, 1>(c);
            issueA<0>(c, c.ga[4]); rdE<1, 2>(c); issueA<1>(c, c.ga[5]); rdE<1, 3>(c);
#pragma unroll
            for (int k = 0; k < 2; ++k) c.wb[k] = CAT8(tr_read(c.wad[0] + k * 1024), tr_read(c.wad[0] + k * 1024 + 128));
            SB
            cmm<0, 2>(c); cmm<0, 3>(c); issueA<2>(c, c.ga[6]); cmm<0, 4>(c); cmm<0, 5>(c); issueA<3>(c, c.ga[7]);
            PIN(c.wb[0]); PIN(c.wb[1]);
        } else if constexpr (P <= 8) {
            cmm<P - 1, 2>(c); rdE<P, 0>(c);
            cmm<P - 1, 3>(c); rdE<P, 1>(c);
            cmm<P - 1, 4>(c); rdE<P, 2>(c);
            cmm<P - 1, 5>(c); rdE<P, 3>(c);
            cmm<P - 1, 0>(c);
            if constexpr (P < 8) set_m0<((P + 3) & 3) * SLOT>(c.ldsb);
            cmm<P - 1, 1>(c);
            rdX<P>(c);
        }
        constexpr bool xw = P && !(P & 1);
#define GA(J) if constexpr (P < 7 && P != 1) issueA<J>(c, c.ga[4 * P + (J)]); SB
        if constexpr (P == 4 || P == 7) zclear<(P + 1) / 4>(c);
        if constexpr (P == 7) {
#pragma unroll
            for (int r = 0; r < 4; ++r) { c.bp[r][3] = cvt_o(c.ew[0][r], c.ew[1][r]); c.bp[r][0] = cvt_o(c.ew[2][r], c.ew[3][r]); }
            c.k0 = -2.0f * 0.7978845608028654f * 1.4426950408889634f / 64.0f; c.k1 = c.k0 * (0.044715f / 4096.0f); c.k2 = -64.0f / 1.4426950408889634f;
            c.am = (c.wf[0] & 16u) ? ~0u : 0u;
            c.fl = (c.lane16 >> 2) + (unsigned)(OFF_FL + 2048 + c.c * 256);
            PIN(c.k0); PIN(c.k1); PIN(c.k2); PIN(c.am); PIN(c.fl);
            SB
        }
        if constexpr (P == 2 || P == 6) zpack<(P == 2 ? 3 : 0)>(c);
        if constexpr (P == 8) {
            TX(0) G8P(0)
            cmm<8, 2>(c); cmm<8, 3>(c); cmm<8, 4>(c); cmm<8, 5>(c); cmm<8, 0>(c); cmm<8, 1>(c);
            G8P(1) TX(1)
            WAITCNT(63, 0); SB
            xpair<1, 0, 8>(c); SB G8P(2) xpair<1, 8, 16>(c); SB G8P(3)
            zpack<1>(c); TX(2)
            tsw<8, 0, S8 / 2>(c); SB G8P(4) tsw<8, S8 / 2, S8>(c); SB G8P(5)
        } else if constexpr (P == 9) {
            GQ(11, 0) rdE<9, 0>(c); rdE<9, 1>(c); rdE<9, 2>(c); rdE<9, 3>(c); tsw<9, 0, 2>(c); SB GQ(11, 1) tsw<9, 2, 6>(c); SB GQ(11, 2) TX(0)
            cmm<9, 2>(c); cmm<9, 3>(c); cmm<9, 4>(c); GQ(11, 3) cmm<9, 5>(c); cmm<9, 0>(c); cmm<9, 1>(c); TX(1)
            GQ(12, 0) tsw<9, 6, 10>(c); SB GQ(12, 1) tsw<9, 10, 14>(c); SB GQ(12, 2) TX(2) tsw<9, 14, 24>(c); SB GQ(12, 3)
        } else if constexpr (P == 10) {
            { unsigned z_; asm volatile("" : "=v"(z_)); c.zq[2] = z_; SB }
            rdE<10, 0>(c); rdE<10, 1>(c); PIN(c.zq); SB rdE<10, 2>(c); rdE<10, 3>(c);
            tsw<10, 0, 24>(c); SB
#pragma unroll
            for (int r = 0; r < 4; ++r) c.bp[r][1] = cvt_o(c.ew[10][r], c.ew[11][r]);
            SB
            cmm<10, 2>(c); cmm<10, 3>(c); cmm<10, 4>(c); cmm<10, 5>(c); rdX<10>(c); cmm<10, 0>(c); cmm<10, 1>(c); TX(0)
            WAITCNT(4, 15); asm volatile("" ::: "memory"); SB TX(1)
            rdE<11, 0>(c); rdE<11, 1>(c); rdE<11, 2>(c); rdE<11, 3>(c);
            swn<2, 0, 0, 8>(c); SB
            WAITCNT(63, 0); SB TX(2)
            cmm<11, 0>(c); cmm<11, 1>(c);
            asm volatile("v_mfma_f32_16x16x32_bf16 %0, %2, %4, 0\n\tv_mfma_f32_16x16x32_bf16 %1, %2, %5, 0\n\tv_mfma_f32_16x16x32_bf16 %0, %3, %6, %0\n\tv_mfma_f32_16x16x32_bf16 %1, %3, %7, %1"
                         : "=&v"(c.ew[22]), "=&v"(c.ew[23]) : "v"(c.wb[0]), "v"(c.wb[1]), "v"(c.fe[1][0][0]), "v"(c.fe[1][0][1]), "v"(c.fe[1][1][0]), "v"(c.fe[1][1][1]));
            SB
        }
        else if constexpr (xw) { tsw<P, 0, 4>(c); WAITCNT(63, 0); GA(0) tsw<P, 4, 6>(c); GA(1) tsw<P, 6, 8>(c); GA(2) tsw<P, 8, 10>(c); GA(3) tsw<P, 10, 12>(c); }
        else { GA(0) GA(1) tsw<P, 0, 4>(c); GA(2) tsw<P, 4, 8>(c); GA(3) tsw<P, 8, 12>(c); }
        if constexpr (P == 4) { xpair<0, 0, 16>(c); SB }
        if constexpr (P == 3) csetup(c);
        SB
        TX(3)
        WAITCNT(P == 7 || P == 8 ? 4 : P < 10 ? 8 + (P == 0 && SMP == 2 ? 2 : 0) : 0, 0);
        TX(4)
        if constexpr (P == 10) { asm volatile("" ::: "memory"); SB rdE<12, 0>(c); rdE<12, 1>(c); rdE<12, 2>(c); rdE<12, 3>(c); FI(0x41) }
        else { BARRIER; CStep<P + 1>::run(c); }
    }
};

#define PFC 147
#define NP0 1
#define NPL "\n\tglobal_load_lds_dword %2, %0 offset:%6"
#define OB 0
#define PRI 1
#define OBC
#define OBD
#define PFO 100
#define WPRE 5
#define ALN 4096
extern "C" __global__ __launch_bounds__(512) __attribute__((aligned(ALN))) void k_main(const int* __restrict__ idx, const uint16_t* __restrict__ table,
        const char* __restrict__ w1a, unsigned long long w1b, const uint16_t* __restrict__ w_cross, unsigned* __restrict__ ws32, const uint16_t* __restrict__ se_w1, const uint16_t* __restrict__ w_bi,
        const uint16_t* __restrict__ se_w2_, const uint16_t* __restrict__ w_deep2_, __hip_bfloat16* __restrict__ out_) {
    SB
    asm volatile(".set .Lnext, 1\n\t.macro STUB1 n\n\t.if .Lnext == \\n\n.Lst\\n: s_cbranch_execz .Lback\n\t.endif\n\t.endm\n\t"
                 ".macro STUB\n\t.if . - k_main >= .Lnext * 4096\n\t"
                 "STUB1 1\n\tSTUB1 2\n\tSTUB1 3\n\tSTUB1 4\n\t.set .Lnext, .Lnext + 1\n\t.endif\n\t.endm\n\t"
                 ".macro PLACE m, k\n\t.rept ((\\k - (. - k_main)) & (\\m - 1)) / 4\n\ts_nop 0\n\t.endr\n\t.endm");
#define stamps ((unsigned long long*)((char*)ws32 + (128ull << 20)))
    int tid = threadIdx.x, bid = blockIdx.x;
    unsigned w64;
    unsigned long long pc;
    {
        unsigned t8, tf;
        asm volatile("s_and_b32 %1, %5, 24\n\tv_lshlrev_b32 %2, 3, %4\n\ts_getpc_b64 %0\n\ts_bfm_b32 m0, 2, 14\n\tv_lshl_add_u32 %3, %1, 9, %2\n\t"
                     "global_load_lds_dword %3, %0 offset:%7\n\tv_readfirstlane_b32 %1, %4" NPL
                     : "=&s"(pc), "=&s"(w64), "=&v"(t8), "=&v"(tf) : "v"(tid), "s"(bid), "n"(PFO), "n"((PFC - 128) * 128 + PFO) : "memory", "scc");
        static_assert(PFC >= 128 && PFC < 158, "");
        static_assert(3 * SLOT == 0xc000, "");
    }
    if (w64 < 256u) {
        unsigned va, t0, t1;
        asm volatile("s_and_b32 %1, %3, 7\n\ts_lshr_b32 %2, %3, 5\n\ts_lshl3_add_u32 %1, %1, %2\n\ts_mulk_i32 %1, %6\n\ts_mul_i32 %2, %4, 10\n\ts_add_u32 %1, %1, %2\n\t"
                     "s_lshl_b32 %2, %4, 5\n\tv_lshl_add_u32 %0, %5, 4, %1\n\ts_add_u32 m0, %2, %7"
                     : "=&v"(va), "=&s"(t0), "=&s"(t1) : "s"(bid), "s"(w64), "v"(tid), "n"(64 * NF * 4), "n"(OFF_IDX) : "memory", "scc");
        static_assert(16 * NF * 4 - 1024 == 640, "");
        if (PRI == 3) __builtin_amdgcn_s_setprio(1);
        SB
        asm volatile("global_load_lds_dwordx4 %0, %1\n\ts_mov_b32 exec_hi, 0xff\n\tglobal_load_lds_dwordx4 %0, %1 offset:1024\n\ts_mov_b32 exec_hi, -1" OBC
                     :: "v"(va), "s"(idx), "s"(table), "s"(w1a), "s"(w1b), "s"(w_cross), "s"(ws32), "s"(se_w1), "s"(w_bi) : "memory");
        SB
    }
    TP(tz3)
    asm volatile("" : "+s"(w64), "+s"(bid), "+v"(tid));
    __builtin_assume(tid >= 0 && tid < 512); __builtin_assume(bid >= 0 && bid < 256); __builtin_assume(w64 < 512u);
    const int lane = tid & 63;
    const int w = (int)(w64 >> 6);
    const int xcd = bid & 7, slot = bid >> 3;
    const int st = xcd * 8 + (slot >> 2), nq = slot & 3;
    const int s0 = st * 64;
    const int rho = (st + nq) & 3, cw = w & 3;
    const int l15 = lane & 15, g = lane >> 4;
#define PFN 1
    float gc0 = -2.0f * 0.7978845608028654f * 1.4426950408889634f, gc1 = gc0 * 0.044715f;
#define FIN_ST(c)
#define FIN_SA(c)
#define ALL
#define DA 0x7a0
#define PADE if (sizeof(c) == sizeof(Cross)) asm volatile("s_endpgm\n\tPLACE 4096, %0" :: "n"(DA) : "memory");
#define FINISH(c) STAMP(5) FIN_ST(c) FIN_SA(c) PADE __builtin_amdgcn_endpgm(); __builtin_unreachable();
    unsigned lane16 = (unsigned)lane * 16u;
    int deep = w >> 2;
    Deep d;
    d.ldsb = (unsigned)(cw * 4096);
#define PTS 2101376
#define PTW 5
#define TA 1
#define XS 0
#define XJ 0
#define DV 0
#define CV 0
#define PG 0
#define SCOUT asm volatile("s_mov_b64 exec, 0\n\ts_cmp_eq_u32 %0, 0\n\ts_cbranch_scc1 .Lst1\n\ts_cmp_eq_u32 %0, 1\n\ts_cbranch_scc1 .Lst2\n\t" \
                     "s_cmp_eq_u32 %0, 2\n\ts_cbranch_scc1 .Lst3\n\ts_branch .Lst4\n.Lback:\n\ts_mov_b64 exec, -1" :: "s"(cw) : "scc");
#define DB 24
#define PLB SB asm volatile("PLACE 32, %0" :: "n"(DB)); SB
#define DJ 1
#define KW 1
#define RB 0
    const int r0 = s0 + 16 * cw;
    unsigned soon;
    d.unit = xcd + 8 * ((st & 7) >> 1);
    unsigned srcA, srcB, ib0, ib[4], sm[3];
#define XSET { srcA = (unsigned)(((lane & 7) ^ g) * 16); srcB = srcA ^ 64u; \
        ib0 = (unsigned)(OFF_IDX + cw * 2048 + (lane >> 3) * 104); \
        _Pragma("unroll") for (int k = 0; k < 4; ++k) { ib[k] = ib0 + (unsigned)(((k + rho) & 3) * 8); PIN(ib[k]); } \
        PIN(srcA); PIN(srcB); PIN(ib0); \
        const int k = (l15 & 1) | ((l15 >> 2) & 2), r = 4 * cw + g; \
        unsigned u0 = (unsigned)(nq * 128) + ((lane16 >> 2) & 124u), u1 = (unsigned)min(cw * 64 + lane, 168) * 4u; \
        if (XS) { PIN(u0); PIN(u1); } \
        sm[0] = (unsigned)(cw * 2048 + (lane >> 1) * 128 + nq * 32 + (lane & 1) * 16); \
        sm[1] = cw == 3 ? u0 : u1; \
        sm[2] = (unsigned)((r < 13 ? r : 12) * (NF * 2) + k * 16 + (k == 3 ? 0 : ((l15 + 2 * rho) & 6) * 2)); \
        PIN(sm[0]); PIN(sm[1]); PIN(sm[2]); }
    typedef unsigned long long u64x2 __attribute__((ext_vector_type(2)));
    const u32x4 rw1 = __builtin_bit_cast(u32x4, u64x2{(unsigned long long)w1a, (unsigned long long)w1b});
    d.rw1 = rw1;
    typedef const __attribute__((address_space(4))) unsigned long long* kap;
    kap ka = (kap)__builtin_amdgcn_kernarg_segment_ptr();
    gp out;
    Cross x;
    x.c = cw; x.nq = nq; x.rho = rho; x.st = st; x.lane16 = lane16; x.ws = (char*)ws32; x.ldsb = d.ldsb;
    unsigned pm;
    const char* sp;
    u32x2 ia[NP], ic[NP];
    if (deep) {
        mkboff(d, lane, cw, nq);
        d.m1 = (unsigned)((nq ^ 1) | (st & 1) | ((d.unit + 32 - NF) >> 5)); PINS(d.m1);
        unsigned va = min(lane16, 80u) * 8u + (unsigned)((bid * 4 + cw) * 768), vb = __umul24(lane16, PTS / 16);
        unsigned long long ptm = w == PTW ? (1ull << PT) - 1 : 0ull;
        d.sB = 24 * 32768;
        PIN(va); PIN(vb); PINS(ptm); PINS(d.sB); PINS(d.ldsb);
        SB
#define TOUCH(t) asm volatile(OBD "buffer_load_dword %0, %2, 0 offen lds\n\ts_mov_b64 exec, %4\n\tglobal_load_lds_dword %1, %3\n\t" t "s_mov_b64 exec, -1" \
                 :: "v"(va), "v"(vb), "s"(rw1), "s"(table), "s"(ptm), "s"(d.ldsb), "n"(OFF_B), "s"(w_cross), "s"(ws32), "s"(idx) : "memory", "scc"); SB
#define B0(t) asm volatile("buffer_load_dwordx4 %0, %4, %5 offen lds\n\tbuffer_load_dwordx4 %1, %4, %5 offen offset:1024 lds\n\t" \
                 "buffer_load_dwordx4 %2, %4, %5 offen offset:2048 lds\n\tbuffer_load_dwordx4 %3, %4, %5 offen offset:3072 lds" t \
                 :: "v"(d.boff[0]), "v"(d.boff[1]), "v"(d.boff[2]), "v"(d.boff[3]), "s"(rw1), "s"(d.sB) : "memory"); SB
        if (d.m1 != 0u) { TOUCH("s_add_u32 m0, %5, %6\n\t") B0("") }
        PINS(d.m1);
        if (d.m1 == 0u) {
            unsigned ro[3];
            rawo(d, lane16, cw, ro); PIN(ro[0]); PIN(ro[1]); PIN(ro[2]);
            SB
            TOUCH("") raw(d, 0u, cw, w_cross, ws32, ro); B0(" ")
        }
        TP(tz1)
        PINS(d.m1);
#pragma unroll
        for (int k = 0; k < 4; ++k) { d.sG[k] = (unsigned)(((k + rho) & 3) * (2 * 32768)); PINS(d.sG[k]); }
        soon = min(d.m1, (unsigned)(nq ^ 3)); PINS(soon);
        SB
        if (TGE == 0) if (cw == 3) tags(tag_addr(lane, d.unit, nq, st, xcd), ws32, OFF_TAG, nq ? 0x7ffffff0000ull : 0x3ffffff0e00ull);
        if ((cw ^ 2) + nq == 0 && lane < 32) st16sc(lane16, (char*)ws32 + WS_SUM + st * 512, u32x4{0u, 0u, 0u, 0u});
        SB
        if (soon != 0u) { prepB<1>(d); snop(0); SB issueB<0>(d); issueB<1>(d); issueB<2>(d); issueB<3>(d); }
        SB
        if (DJ) asm volatile("s_branch .Ldeep" :: "s"(nq), "s"(cw), "s"(rho), "s"(st), "s"(xcd), "s"(r0), "s"(d.unit) : "memory");
        SB
    } else {
        Cross& c = x;
        WAITCNT(63, 0);
        SB
        if (PRI == 1) __builtin_amdgcn_s_setprio(1);
        PINS(ka);
        const char* se_w2 = (const char*)ka[8]; const char* wd2 = (const char*)ka[9]; out = (gp)ka[10];
        SB
        if (!XS) XSET
#pragma unroll
        for (int k = 0; k < 4; ++k) { c.tb[k] = (const char*)table - k * 1024; PINS(c.tb[k]); }
#define CSET { \
            const int G = 2 * g + rho; \
            const unsigned ea = (unsigned)(cw * 4096 + l15 * 128); \
            c.re[0] = ea + (unsigned)((g ^ (l15 >> 1)) * 16); c.re[1] = c.re[0] ^ 64u; \
            c.rx[0] = ea + (unsigned)(((2 * nq + (g >> 1)) ^ (l15 >> 1)) * 16); c.rx[1] = c.rx[0] + 8u; \
            c.rx0[0] = c.rx[0] + (unsigned)((rho & 1) * 2048 + (G & 2) * 4); c.rx0[1] = c.rx[0] + (unsigned)((~rho & 1) * 2048 + ((G + 1) & 2) * 4); \
            c.six = (l15 & 1) ? 0xddddeeeeu : 0xdddd4444u; \
            c.pl = 0x05040100u; c.ph = 0x07060302u; c.xm[0] = 0x0000ffff0000ffffull; c.xm[1] = 0xffff0000ffff0000ull; \
            PINS(c.pl); PINS(c.ph); PINS(c.xm[0]); PINS(c.xm[1]); \
_Pragma("unroll")             for (int k = 0; k < 4; ++k) { const unsigned o = (l15 >> 1) == 2 * k + (g >> 1) ? 0x3f803f80u : 0u; c.on[k] = u32x4{o, o, o, o}; PIN(c.on[k]); } \
            PIN(c.re[0]); PIN(c.re[1]); PIN(c.rx[0]); PIN(c.rx[1]); PIN(c.rx0[0]); PIN(c.rx0[1]); PIN(c.six); \
            zclear<3>(c); zclear<0>(c); \
        }
        if (CV & 1) CSET
        if (CV & 4) { SB SCOUT }
        sp = cw == 3 ? wd2 : (const char*)se_w1;
        if (KW) { SB PINS(sp); PINS(se_w2); PINS(out); }
        if (PRI == 2) __builtin_amdgcn_s_setprio(1);
        set_m0<0>(c.ldsb);
        SB
        TP(tz1)
        WAITCNT(0, 15);
        asm volatile("" ::: "memory");
        STAMP(1)
        TP(c.tq[0])
        ia[0] = LDS(const u32x2, ib0 + 96); ic[0] = LDS(const u32x2, ib0 + 96 + 832);
#pragma unroll
        for (int q = 1; q < 3; ++q) { ia[q] = LDS(const u32x2, ib[q - 1]); ic[q] = LDS(const u32x2, ib[q - 1] + 832); }
        SB
#define GATHER(q) issueA<0>(c, ia[q][0] * 128u + srcA); issueA<1>(c, ic[q][0] * 128u + srcB); issueA<2>(c, ia[q][1] * 128u + srcA); issueA<3>(c, ic[q][1] * 128u + srcB);
        GATHER(0)
        set_m0<SLOT>(c.ldsb);
        SB
        GATHER(1)
#define SM0 asm volatile("s_mov_b32 m0, %2\n\ts_mov_b32 exec_hi, 0\n\tglobal_load_lds_dwordx4 %0, %1\n\ts_mov_b32 exec_hi, -1" :: "v"(sm[0]), "s"(w_bi), "s"(OFF_WBI + cw * 512) : "memory");
#define SM1 dma4(sm[1], sp, OFF_SW1 + cw * 256); dma4(sm[2], se_w2, OFF_SW2 + cw * 256);
        SM0 SM1
        set_m0<2 * SLOT>(c.ldsb);
        TP(c.tq[1])
    }
    if (PRO & 4) PINS(d.m1);
    int crs = deep ^ 1;
    PINS(crs); PINS(deep);
    if (crs == 0) {
        Deep& c = d;
        int nq_ = nq, cw_ = cw, rho_ = rho, st_ = st, xcd_ = xcd, r0_ = r0;
        if (DJ) asm volatile(".Ldeep:" : "+s"(nq_), "+s"(cw_), "+s"(rho_), "+s"(st_), "+s"(xcd_), "+s"(r0_), "+s"(c.unit), "+s"(c.m1) :: "memory");
        SB
        WAITCNT(63, 0);
        TP(tz2)
        SB
        const int nq = nq_, cw = cw_, rho = rho_, st = st_, xcd = xcd_, r0 = r0_;
        __builtin_assume(nq >= 0 && nq < 4); __builtin_assume(cw >= 0 && cw < 4); __builtin_assume(rho >= 0 && rho < 4);
        int lane;
        asm volatile("v_mbcnt_lo_u32_b32 %0, -1, 0\n\tv_mbcnt_hi_u32_b32 %0, -1, %0" : "=v"(lane));
        const int l15 = lane & 15, g = lane >> 4, tid = w * 64 + lane;
        PINS(ka);
        const gp out = (gp)ka[10];
        c.c = cw; c.nq = nq; c.rho = rho; c.st = st; c.lane16 = (unsigned)lane * 16u; c.ws = (char*)ws32;
#define MV 0
#define B4 issueB<0>(c); issueB<1>(c); issueB<2>(c); issueB<3>(c);
#define B1L(v) if (v == 0u) { prepB<1>(c); snop(0); SB B4 }
#define EARLY if (MV != 3) { PINS(c.m1); produce<5>(c); } PINS(soon); \
        if (MV == 2) { PINS(c.m1); B1L(c.m1) } else if (MV == 3) { B1L(n3) } else { B1L(soon) } \
        if (MV != 2) prepB<2>(c); \
        TP(c.tq[0]) \
        BARRIER;            \
        TP(c.tq[1]) \
        if (MV == 2) { B1L(n3) prepB<2>(c); snop(0); SB } \
        B4
        unsigned n3 = (unsigned)(nq ^ 3);
        if (MV) { PINS(n3); mkboff(d, lane, cw, nq); SB }
        if (MV == 1 || MV == 2) { EARLY PINS(c.m1); SB }
        if (MV == 3) { PINS(c.m1); produce<5>(c); PINS(c.m1); B1L(c.m1) PINS(c.m1); SB }
        c.m3 = (unsigned)(cw ^ 3); c.m4 = c.m3 | min(c.m1, (unsigned)nq);
        c.tad = tag_addr(lane, c.unit, nq, st, xcd);
        PIN(c.tad);
        SB
        if (!MV) mkboff(d, lane, cw, nq);
        {
            const unsigned a0 = (unsigned)(cw * 4096 + (cw & 1) * 2048 + l15 * 128 + ((4 * (cw >> 1) + g) ^ (l15 >> 1)) * 16);
            const int q = l15 >> 2, p = l15 & 3;
            const unsigned b0 = (unsigned)(OFF_B + cw * 4096 + (8 * g + q) * 128 + ((p >> 1) ^ (q & 2) ^ ((g & 1) << 2)) * 16 + (p & 1) * 8);
#pragma unroll
            for (int k = 0; k < 4; ++k) { c.ra[k] = a0 ^ (unsigned)(k * 4096); c.rb[k] = b0 ^ (unsigned)(k * 32); PIN(c.ra[k]); PIN(c.rb[k]); }
#pragma unroll
            for (int k = 0; k < 16; ++k) { c.ac[k >> 2][k & 3] = f32x4{0.f, 0.f, 0.f, 0.f}; PIN(c.ac[k >> 2][k & 3]); }
        }
        unsigned exw[4], fl = (c.lane16 >> 2) + (unsigned)(OFF_FL + cw * 256), fr = c.lane16 + OFF_FL, s8 = c.lane16 >> 1;
        Fin fk; fin(fk);
#pragma unroll
        for (int k = 1; k < 4; ++k) { exw[k] = (c.lane16 >> 2) + (unsigned)(OFF_B + (cw ^ k) * 4096); PIN(exw[k]); }
        double big = BIG;
        f32x4 va = {0.f, 0.f, 0.f, 0.f};
        float gc2 = 1.0f / TRC;
        f32x2 gcp = {gc0, gc1}, gcq = {gc2, gc2};
        c.w2[0][0] = OFF_WD2 + g * 16; c.w2[1][0] = OFF_WD2 + 64 + g * 16; PIN(c.w2[0]); PIN(c.w2[1]);
        const unsigned dl = (unsigned)((l15 >> 2) ^ (2 * (g & 1))), on = (l15 & 1) ? 0x3f800000u : 0x3f80u;
        u32x4 unit = {dl == 0u ? on : 0u, dl == 0u ? on : 0u, dl == 1u ? on : 0u, dl == 1u ? on : 0u};
        unsigned uix = (l15 & 2) ? 0xddddeeeeu : 0xdddd4444u;
        u32x2 pr[4][4];
#pragma unroll
        for (int k = 0; k < 4; ++k) { pr[0][k] = u32x2{0u, 0u}; PIN(pr[0][k]); }
        PIN(uix);
        if (PFN) LDS(f32x4, fr + (unsigned)(cw * 1024)) = va;
        PIN(fl); PIN(fr); PIN(s8); PIN(big); PIN(gcp); PIN(gcq); gc0 = gcp[0]; gc1 = gcp[1]; gc2 = gcq[0]; PIN(va); PIN(unit);
        c.wsf[0] = c.ws + WS_FRAG + 16384 + nq * 65536 + cw * 4096; c.wsf[1] = c.wsf[0] + 32768;
        c.wsh[0] = c.ws + WS_FRAG + 32768 + nq * 65536 + cw * 2048; c.wsh[1] = c.wsh[0] + 8192; c.ldsh = (unsigned)(cw * 2048);
        PINS(c.wsh[0]); PINS(c.wsh[1]); PINS(c.wsf[0]); PINS(c.wsf[1]); PINS(c.ldsh);
        c.pmask = cw != 3 ? 0ull : (c.m1 == 0u ? 0x1ffull : 0x100ull);
        unsigned long long wmask = (cw ^ 3) + nq ? 0ull : 0xe00ull;
        unsigned m5 = c.m3 | c.m1;
        PINS(wmask); PINS(m5);
        constexpr unsigned xb1 = xbits(1), xb2 = xbits(2), xb3 = xbits(3), xb4 = xbits(4);
        c.xa = c.m1 == 0u ? (cw == 3 ? xb4 : xb3) : cw != 3 ? 0u : nq ? xb1 : xb2;
        c.one = 1u; PIN(c.one);
        PINS(c.pmask); PINS(c.m3); PINS(c.m4); PINS(c.xa);
        if (VT) { c.tb = c.m1 == 0u ? TBP : (TB >> (4 * nq)) & 15; PINS(c.tb); }
        gp sums, outp;
        unsigned tga;
#define DTL sums = (gp)c.ws + (unsigned)(WS_SUM + r0 * 8); outp = out + (unsigned)(r0 * 2); tga = OFF_TAG + (c.lane16 >> 2); PINS(sums); PINS(outp); PIN(tga);
        if (DV & 1) { DTL PLB }
        if (CV & 2) { SB SCOUT SB }
        if (MV == 0 || MV == 3) { EARLY }
        prepB<3>(c);
        TP(c.tq[2])
        if (!(DV & 1)) { DTL PLB }
        WAITX(8, 2, c.m1)
        c.tag = LDSV(tga);
        if (!(DV & 2)) PIN(c.tag);
        TP(c.tq[3])
        BARRIER;
        TP(c.tq[4])
        SB
        DStep<0>::run(c);
        TS(c.ts[13])
        u32x2 px[4][4];
#define BX 0
#define EX2(e, k) if (BX & 1) { px[e][k][0] = cvt_o(c.ac[e][k][0], c.ac[e][k][1]); px[e][k][1] = cvt_o(c.ac[e][k][2], c.ac[e][k][3]); SB } else { px[e][k][0] = pk_bf16(c.ac[e][k][0], c.ac[e][k][1]); px[e][k][1] = pk_bf16(c.ac[e][k][2], c.ac[e][k][3]); USE(px[e][k][0]); USE(px[e][k][1]); SB }
#define EXO(e, k) (((e) - 1) * 1024 + ((k) & 1) * 512)
#define WT1(e, k, d) { asm volatile("ds_write_addtid_b32 %0 offset:%1" :: "v"(px[e][k][d]), "n"(((k) < 2 ? 62464 : 32768) + EXO(e, k) + (d) * 256) : "memory"); SB }
#define WT(e, k) { asm volatile("ds_write_addtid_b32 %0 offset:%2\n\tds_write_addtid_b32 %1 offset:%3" ST :: "v"(px[e][k][0]), "v"(px[e][k][1]), \
            "n"(((k) < 2 ? 62464 : 32768) + EXO(e, k)), "n"(((k) < 2 ? 62464 : 32768) + EXO(e, k) + 256) : "memory"); SB }
#define W6(k) WT(3, k) WT(2, k) WT(1, k)
#define RD(h_) { constexpr int h = (h_) & 1; if (!(h_)) f = LDSV(fr + 1024); asm volatile("" ::: "memory"); SB \
            _Pragma("unroll") for (int k = 2 * (h); k < 2 * (h) + 2; ++k) _Pragma("unroll") for (int e = 3; e > 0; --e) _Pragma("unroll") for (int d = 0; d < 2; ++d) \
                pr[e][k][d] = LDS(const unsigned, exw[e] + ((h) ? 0 : 3 * SLOT) + EXO(e, k) + d * 256); SB }
#define CK(h) { if (__builtin_expect(__builtin_amdgcn_ballot_w64(f == 0u) != 0ull, 0)) { unsigned& slow = c.slow; (void)slow; SLOW(3) \
            _Pragma("unroll 1") for (int it = 0; it < (1 << 20); ++it) { RD(0) RD(1) if (__builtin_amdgcn_ballot_w64(f == 0u) == 0ull) break; } } SB }
#define AD(k) asm volatile("v_smfmac_f32_16x16x64_bf16 %0, %1, %2, %3" ST : "+v"(c.ac[0][k]) : "v"(unit), \
            "v"(u32x8{pr[0][k][0], pr[0][k][1], pr[1][k][0], pr[1][k][1], pr[2][k][0], pr[2][k][1], pr[3][k][0], pr[3][k][1]}), "v"(uix));
#define G4(m, k, ins) asm volatile(ins : m(y[4 * k]), m(y[4 * k + 1]), m(y[4 * k + 2]), m(y[4 * k + 3]) \
            : "v"(c.ac[0][k][0]), "v"(c.ac[0][k][1]), "v"(c.ac[0][k][2]), "v"(c.ac[0][k][3]), "v"(gc0), "v"(gc1), "v"(gc2));
#define GA_ "v_mul_f32 %0, %4, %4\n\tv_mul_f32 %1, %5, %5\n\tv_mul_f32 %2, %6, %6\n\tv_mul_f32 %3, %7, %7\n\t" \
            "v_fma_f32 %0, %0, %9, %8\n\tv_fma_f32 %1, %1, %9, %8\n\tv_fma_f32 %2, %2, %9, %8\n\tv_fma_f32 %3, %3, %9, %8\n\t" ST_
#define GM_ "v_mul_f32 %0, %0, %4\n\tv_mul_f32 %1, %1, %5\n\tv_mul_f32 %2, %2, %6\n\tv_mul_f32 %3, %3, %7\n\t" ST_
#define GB_ GM_ "v_exp_f32 %0, %0\n\tv_exp_f32 %1, %1\n\tv_exp_f32 %2, %2\n\tv_exp_f32 %3, %3\n\t"
#define GC_ ST_ "v_fma_f32 %0, %0, %10, %10\n\tv_fma_f32 %1, %1, %10, %10\n\tv_fma_f32 %2, %2, %10, %10\n\tv_fma_f32 %3, %3, %10, %10\n\t" ST_ \
            "v_rcp_f32 %0, %0\n\tv_rcp_f32 %1, %1\n\tv_rcp_f32 %2, %2\n\tv_rcp_f32 %3, %3\n\t" GM_
#define GELU(k) { G4("=&v", k, GA_ GB_ GC_) }
#define GEL1(k, I0) { G4("=&v", k, GA_) I0 G4("+v", k, GB_ GC_) }
#define GEL2(k, I1, I2) { if (k < 2 || PU2 == 2 * k + 6) { G4("=&v", k, GA_ GB_) I1 G4("+v", k, GC_) } else GELU(k) }
#define WDOT(q) asm volatile("v_smfmac_f32_16x16x64_bf16 %0, %1, %2, %3 abid:1" :: "v"(va), "v"(c.w2[q]), \
            "v"(f32x8{y[8 * q], y[8 * q + 1], y[8 * q + 2], y[8 * q + 3], y[8 * q + 4], y[8 * q + 5], y[8 * q + 6], y[8 * q + 7]}), "v"(uix));
#define PROTO { watch = __builtin_amdgcn_ballot_w64(tw == c.tag) & wmask; \
            if (__builtin_expect(m5 == 0u, 0)) { \
                const u32x4 e4 = LDS(const u32x4, fr + (OFF_NB - OFF_FL)), l4 = LDS(const u32x4, fr + (OFF_NB + 1024 - OFF_FL)); \
                watch |= __builtin_amdgcn_ballot_w64(e4[0] == l4[0]) | __builtin_amdgcn_ballot_w64(e4[1] == l4[1]) | __builtin_amdgcn_ballot_w64(e4[2] == l4[2]) | __builtin_amdgcn_ballot_w64(e4[3] == l4[3]); \
            } \
            PINS(watch); } SB
#define FLA { LDS(unsigned, fl + 1024) = fl; SB }
#define FLB { LDS(unsigned, fl + 3072) = fl; SB }
        unsigned f = 0;
        float y[16];
        unsigned long long watch = 0ull;
#define FW 4
#define FLP 1
#define WSP 1
#define PD 3
#define PR(n) { if ((n) == PU) __builtin_amdgcn_s_setprio(2); if ((n) == PD || (n) == PD2) __builtin_amdgcn_s_setprio(0); if ((n) == PU2) __builtin_amdgcn_s_setprio(2); SB }
#define PU2 13
#define PD2 -1
#define FH(n) if ((n) == FW) { WAITCNT(0, 15); SB set_m0<OFF_B + 3 * SLOT - 62464>(c.ldsb); LDS(unsigned, fl + 3072) = fl; SB }
#define FS(n) if ((n) == FLP) { WAITCNT(0, 15); SB LDS(unsigned, fl) = fl; SB publish(c, c.pmask); SB TE(c.tq[3]) }
#define PP 4
#define PQ(n) if ((n) == PP) { PROTO }
        PR(0) FH(0)
        TE(c.tq[0])
        mk<12, 3, 0>(c); mk<12, 2, 0>(c); mk<12, 1, 0>(c); FH(1) mk<12, 3, 1>(c); mk<12, 2, 1>(c); mk<12, 1, 1>(c); FH(2)
        mk<12, 3, 2>(c); mk<12, 2, 2>(c); mk<12, 1, 2>(c); FH(3) EX2(3, 0) mk<12, 3, 3>(c); EX2(2, 0) mk<12, 2, 3>(c); EX2(1, 0) mk<12, 1, 3>(c); FH(4) PR(1)
#define OWN 4
#define OW(k) if ((k) < OWN) mk<12, 0, k>(c);
#define OX(k) if ((k) >= OWN) mk<12, 0, k>(c);
        WT(3, 0) EX2(3, 1) OW(0) WT(2, 0) EX2(2, 1) WT(1, 0) EX2(1, 1) OW(1) WT(3, 1) EX2(3, 2) WT(2, 1) EX2(2, 2) OW(2) WT(1, 1) set_m0<OFF_B - 32768>(c.ldsb); EX2(1, 2) WT(3, 2) EX2(3, 3) OW(3) WT(2, 2) EX2(2, 3) WT(1, 2) EX2(1, 3)
        W6(3) set_m0<FOFF[2] + 8192>(c.ldsh); SB FLA
        TE(c.tq[1])
        static_assert(NE == 6 && WSP == 1, "");
        PR(2) dqs<14, NE, 8>(c);
        const unsigned tw = LDSV(fl + (OFF_TAG + 512 - OFF_FL - 768));
        SB
#define CKL 2
#define FRP 12
        dqs<14, 8, FRP>(c);
        if (CKL == 3) asm volatile("ds_read_b32 %0, %1 offset:1024" : "=v"(f) : "v"(fr) : "memory");
        dqs<14, FRP, 12>(c); PR(3)
        if (DEF11) m4<11, 0>(c);
        OX(0) OX(1) OX(2) OX(3)
        PQ(2) PR(9)
#define R2(n, a, o) "ds_read2st64_b32 %" #n ", %" #a " offset0:" #o " offset1:" #o "+1\n\t"
#define R3(n0, n1, n2, o) R2(n0, 16, o+8) R2(n1, 15, o+4) R2(n2, 14, o) ST_
        if (CKL == 1) {
            asm volatile("1:\n\tds_read_b32 %0, %13 offset:1024\n\t" R3(1, 2, 3, 192) R3(4, 5, 6, 194) R3(7, 8, 9, 0) R3(10, 11, 12, 2)
                         "s_waitcnt lgkmcnt(12)\n\tv_cmp_eq_u32 vcc, 0, %0\n\ts_cbranch_vccnz 1b"
                         : "=&v"(f), "=&v"(pr[3][0]), "=&v"(pr[2][0]), "=&v"(pr[1][0]), "=&v"(pr[3][1]), "=&v"(pr[2][1]), "=&v"(pr[1][1]),
                           "=&v"(pr[3][2]), "=&v"(pr[2][2]), "=&v"(pr[1][2]), "=&v"(pr[3][3]), "=&v"(pr[2][3]), "=&v"(pr[1][3])
                         : "v"(fr), "v"(exw[1]), "v"(exw[2]), "v"(exw[3]) : "memory", "vcc");
            PR(4)
        }
        else if (CKL == 3) {
            asm volatile("1:\n\ts_waitcnt lgkmcnt(0)\n\tv_cmp_eq_u32 vcc, 0, %0\n\tds_read_b32 %0, %13 offset:1024\n\ts_cbranch_vccnz 1b\n\t"
                         R3(1, 2, 3, 192) R3(4, 5, 6, 194) R3(7, 8, 9, 0) R3(10, 11, 12, 2)
                         : "+&v"(f), "=&v"(pr[3][0]), "=&v"(pr[2][0]), "=&v"(pr[1][0]), "=&v"(pr[3][1]), "=&v"(pr[2][1]), "=&v"(pr[1][1]),
                           "=&v"(pr[3][2]), "=&v"(pr[2][2]), "=&v"(pr[1][2]), "=&v"(pr[3][3]), "=&v"(pr[2][3]), "=&v"(pr[1][3])
                         : "v"(fr), "v"(exw[1]), "v"(exw[2]), "v"(exw[3]) : "memory", "vcc");
            PR(4) PQ(3)
        }
        else if (CKL == 2) { while (__builtin_amdgcn_ballot_w64(LDSV(fr + 1024) == 0u) != 0ull) { } RD(2) RD(1) PR(4) }
        else { RD(0) RD(1) PR(4) CK(0) }
        TE(c.tq[2])
#define AW(n) if (CKL & 1) { SB WAITCNT(63, n); SB }
#define ADN 0
        AW(9) AD(0) USE(f); AW(6) AD(1) AW(3) AD(2) AW(0) AD(3) snop(ADN); SB
        PR(5) PQ(4) FS(0)
        if (FLP == 1) GEL2(0, FS(1), ) else GELU(0)
        FS(2) PR(6) PQ(5)
        if (FLP == 3) GEL2(1, FS(3) PR(7), ) else GELU(1)
        FS(4) PR(8)
#define WDP 1
#define GP 0
#define PK2(op, a, b, m) op " %0, %0, " a m "\n\t" op " %1, %1, " b m "\n\t"
#define T4(op) op " %2, %2\n\t" op " %3, %3\n\t" op " %4, %4\n\t" op " %5, %5\n\t"
#define GPK(k, I) { f32x2 p0, p1; asm volatile("" : "=v"(p0), "=v"(p1)); I \
            asm volatile("v_pk_mul_f32 %0, %6, %6\n\tv_pk_mul_f32 %1, %7, %7\n\t" PK2("v_pk_fma_f32", "%8, %8", "%8, %8", " op_sel:[0,1,0] op_sel_hi:[1,1,0]") PK2("v_pk_mul_f32", "%6", "%7", "") \
                T4("v_exp_f32") ST_ PK2("v_pk_fma_f32", "%9, %9", "%9, %9", " op_sel_hi:[1,0,0]") T4("v_rcp_f32") ST_ PK2("v_pk_mul_f32", "%6", "%7", "") \
                :: "v"(p0), "v"(p1), "v"(p0[0]), "v"(p0[1]), "v"(p1[0]), "v"(p1[1]), "v"(f32x2{c.ac[0][k][0], c.ac[0][k][1]}), "v"(f32x2{c.ac[0][k][2], c.ac[0][k][3]}), "v"(gcp), "v"(gcq)); \
            y[4 * k] = p0[0]; y[4 * k + 1] = p0[1]; y[4 * k + 2] = p1[0]; y[4 * k + 3] = p1[1]; }
        if (GP & 4) GPK(2, ) else GEL2(2, PR(10), )
        PR(11)
        if (GP & 8) GPK(3, ) else GEL2(3, PR(12), )
        PR(13)
        if (GP & 8) { snop(0); SB }
        WDOT(0)
        WDOT(1)
        asm volatile("s_mov_b64 exec, 0xffff" : "+v"(va));
        snop(6);
        SB
        TE(c.tq[4])
        const float v = va[0];
        TS(c.ts[14])
        deliver(c, v, big, 2 * nq, s8, fk, sums, outp);
        ALL
        if (__builtin_expect(watch != 0ull, 0)) {
            asm volatile("s_mov_b64 exec, -1" ::: "memory");
            unsigned& slow = c.slow; (void)slow; SLOW(5)
            const u32x4 e4 = LDS(const u32x4, OFF_NB + c.lane16), l4 = LDS(const u32x4, OFF_NB + 1024 + c.lane16);
            bool opn[4];
#pragma unroll
            for (int k = 0; k < 4; ++k) opn[k] = c.m1 == 0u && e4[k] == l4[k];
            bool own = nq == 0 && lane >= 9 && lane < 12 && LDSV(OFF_TAG + 512 + lane * 4) == c.tag;
#pragma unroll 1
            for (int it = 0; it < (1 << 22) && __builtin_amdgcn_ballot_w64(opn[0] || opn[1] || opn[2] || opn[3] || own) != 0ull; ++it) {
                bool chg = false;
#pragma unroll
                for (int k = 0; k < 4; ++k)
                    if (opn[k] && __hip_atomic_load(ws32 + lane * 4 + k, __ATOMIC_RELAXED, __HIP_MEMORY_SCOPE_AGENT) != e4[k]) { opn[k] = false; chg = true; }
                if (own && __hip_atomic_load(ws32 + (c.tad >> 2), __ATOMIC_RELAXED, __HIP_MEMORY_SCOPE_AGENT) != c.tag) { own = false; chg = true; }
                if (__builtin_amdgcn_ballot_w64(chg) != 0ull) publish(c, c.pmask);
                else __builtin_amdgcn_s_sleep(2);
            }
        }
        FINISH(c)
    } else {
        Cross& c = x;
#pragma unroll
        for (int q = 3; q < NP; ++q) { ia[q] = LDS(const u32x2, ib[(q - 1) & 3] + ((q - 1) >> 2) * 32); ic[q] = LDS(const u32x2, ib[(q - 1) & 3] + ((q - 1) >> 2) * 32 + 832); }
#pragma unroll
        for (int q = 3; q < NP; ++q) {
            c.ga[4 * q - 12] = ia[q][0] * 128u + srcA; c.ga[4 * q - 11] = ic[q][0] * 128u + srcB;
            c.ga[4 * q - 10] = ia[q][1] * 128u + srcA; c.ga[4 * q - 9] = ic[q][1] * 128u + srcB;
        }
#pragma unroll
        for (int k = 0; k < 40; ++k) PIN(c.ga[k]);
        if (!(CV & 1)) CSET
        SB
        if (!(CV & 6)) SCOUT
        unsigned g2[4];
#define G2(cmp) asm volatile(cmp " %8, 0\n\ts_cbranch_scc1 1f\n\tglobal_load_lds_dwordx4 %0, %4\n\tglobal_load_lds_dwordx4 %1, %5 offset:1024\n\t" \
            "global_load_lds_dwordx4 %2, %6 offset:2048\n\tglobal_load_lds_dwordx4 %3, %7 offset:3072\n1:" :: "v"(g2[0]), "v"(g2[1]), "v"(g2[2]), "v"(g2[3]), \
            "s"(c.tb[0]), "s"(c.tb[1]), "s"(c.tb[2]), "s"(c.tb[3]), "s"(pm) : "memory", "scc");
        if (PG) {
            pm = (unsigned)((nq ^ 1) | (st & 1) | ((xcd + 8 * ((st & 7) >> 1) + 32 - NF) >> 5));
            if (PG == 2) pm = 1u;
            if (PG == 3) pm = min(pm, (unsigned)(nq ^ 3));
            if (PG == 4) pm = 0u;
            g2[0] = ia[2][0] * 128u + srcA; g2[1] = ic[2][0] * 128u + srcB; g2[2] = ia[2][1] * 128u + srcA; g2[3] = ic[2][1] * 128u + srcB;
            G2("s_cmp_lg_u32") SB
        }
        static_assert(SMP == 0, "");
        BARRIER;
        TP(tz2)
        SB
        if (PG) { G2("s_cmp_eq_u32") } else { GATHER(2) }
        SB
        static_assert(SMP != 1 && SMP != 2, "");
        TP(c.tq[2])
        {
            const int q = (lane >> 2) & 3, p = lane & 3;
            c.wad[0] = (unsigned)(OFF_WBI + (8 * g + q) * 32 + p * 8); c.wad[1] = (unsigned)(OFF_WBI + (8 * g + q) * 32 + ((p + rho) & 3) * 8);
            PIN(c.wad[0]); PIN(c.wad[1]);
        }
        gp sums = (gp)c.ws + (unsigned)(WS_SUM + (s0 + 16 * cw) * 8), outp = out + (unsigned)((s0 + 16 * cw) * 2);
        PINS(sums); PINS(outp);
        c.wsf = c.ws + WS_FRAG + nq * 65536 + cw * 4096; PINS(c.wsf);
        unsigned long long act = 0xf0000f0000f0000full, m1 = 0xaaaaaaaaaaaaaaaaull, m2 = 0xccccccccccccccccull;
        double bigs = BIG;
        PINS(act); PINS(m1); PINS(m2); PINS(bigs);
        Fin fk; fin(fk);
        TP(c.tq[3])
        WAITCNT(11, 0); BARRIER;
        TP(c.tq[4])
        if (!PRI) __builtin_amdgcn_s_setprio(1);
        CStep<0>::run(c);
        TS(c.ts[11])
        u32x4 bq[4];
        u32x4 (&bp)[4] = c.bp;
        f32x2 T[6][8];
#define SPLIT(s0, s1, i0, i1) { _Pragma("unroll") for (int s = s0; s < s1; ++s) _Pragma("unroll") for (int i_ = i0; i_ < i1; ++i_) { \
            const f32x4 x = c.ew[2 * (1 + 4 * (s >> 1) + (i_ >> 1)) + (s & 1)]; T[s][i_] = f32x2{x[2 * (i_ & 1)], x[2 * (i_ & 1) + 1]}; } }
#define CB(B, i, h, k) { B[(2 * (i) + (h)) & 3][k] = cvt_o(T[2 * (k)][i][h], T[2 * (k) + 1][i][h]); }
#define SC(i, k) { asm volatile("v_pk_mul_f32 %0, %1, %0 op_sel_hi:[0,1]" : "+v"(T[2 * (k)][i]) : "v"(ao[k])); asm volatile("v_pk_mul_f32 %0, %1, %0 op_sel:[1,0]" : "+v"(T[2 * (k) + 1][i]) : "v"(ao[k])); }
        SPLIT(0, 4, 0, 8)
        SB
#define FLZ 8
#define SW3 6
#define SWA 8
#define FL1 3072
#define FLR 25
#define FLW 27
        STAMP(2)
        FI(0)
#define HO 1
#define M45 asm volatile("v_mfma_f32_16x16x32_bf16 %0, %2, %3, %0\n\tv_mfma_f32_16x16x32_bf16 %1, %2, %4, %1" :: "v"(c.ew[24]), "v"(c.ew[25]), "v"(c.wb[1]), "v"(c.fe[0][1][0]), "v"(c.fe[0][1][1]));
        cmm<12, 0>(c); cmm<12, 2>(c); cmm<12, 3>(c); cmm<12, 1>(c); M45
        rdX<12>(c);
        zpack<2>(c);
        FI(0x42)
        swl<8, 9>(c);
        const f32x4 hp = mf0(c.s1, c.zq);
        TS(c.ts[12])
        FI(0x50) TH(0)
        f32x2 hb;
        float hq[2];
        asm volatile("" : "=v"(hb), "=v"(hq[0]), "=v"(hq[1]));
        if (HO == 0) snop(5);
        SB
        swl<8 + HO, 8 + SWA>(c);
        FI(1) TH(1)
        asm volatile("v_mul_f32 %0, %4, %4\n\tv_mul_f32 %1, %5, %5\n\tv_mul_f32 %2, %6, %6\n\tv_mul_f32 %3, %7, %7\n\t"
                     "v_fma_f32 %0, %0, %9, %8\n\tv_fma_f32 %1, %1, %9, %8\n\tv_fma_f32 %2, %2, %9, %8\n\tv_fma_f32 %3, %3, %9, %8\n\t"
                     "v_mul_f32 %0, %0, %4\n\tv_mul_f32 %1, %1, %5\n\tv_mul_f32 %2, %2, %6\n\tv_mul_f32 %3, %3, %7\n\t"
                     "v_exp_f32 %0, %0\n\tv_exp_f32 %1, %1\n\tv_exp_f32 %2, %2\n\tv_exp_f32 %3, %3\n\t"
                     "v_fma_f32 %0, %0, %10, %10\n\tv_fma_f32 %1, %1, %10, %10\n\tv_fma_f32 %2, %2, %10, %10\n\tv_fma_f32 %3, %3, %10, %10\n\t"
                     "v_rcp_f32 %0, %0\n\tv_rcp_f32 %1, %1\n\tv_rcp_f32 %2, %2\n\tv_rcp_f32 %3, %3\n\t"
                     "v_mul_f32 %0, %0, %4\n\tv_mul_f32 %1, %1, %5\n\tv_mul_f32 %2, %2, %6\n\tv_mul_f32 %3, %3, %7\n\t"
                     "v_cvt_pk_bf16_f32 %0, %0, %1\n\tv_cvt_pk_bf16_f32 %1, %2, %3"
                     :: "v"(hb[0]), "v"(hb[1]), "v"(hq[0]), "v"(hq[1]), "v"(hp[0]), "v"(hp[1]), "v"(hp[2]), "v"(hp[3]), "v"(c.k0), "v"(c.k1), "v"(c.k2));
        FI(2) TH(2)
        swl<8 + SWA, 10 + SWA>(c);
        f32x4 av[2];
        asm volatile("v_mfma_f32_16x16x16_bf16 %0, %1, %2, 0" : "=&v"(av[0]) : "v"(c.wa[0]), "v"(hb));
        asm volatile("v_mfma_f32_16x16x16_bf16 %0, %1, %2, 0" : "=&v"(av[1]) : "v"(c.wa[1]), "v"(hb));
        FI(3)
        swl<10 + SWA, 18 + SWA>(c);
        TH(3) FI(4)
        TS(c.ts[13])
        SB
        float as[8];
        asm volatile("v_exp_f32 %0, %8\n\tv_exp_f32 %1, %9\n\tv_exp_f32 %2, %10\n\tv_exp_f32 %3, %11\n\tv_exp_f32 %4, %12\n\tv_exp_f32 %5, %13\n\tv_exp_f32 %6, %14\n\tv_exp_f32 %7, %15\n\t"
                     "v_add_f32 %0, 1.0, %0\n\tv_add_f32 %1, 1.0, %1\n\tv_add_f32 %2, 1.0, %2\n\tv_add_f32 %3, 1.0, %3\n\tv_add_f32 %4, 1.0, %4\n\tv_add_f32 %5, 1.0, %5\n\tv_add_f32 %6, 1.0, %6\n\tv_add_f32 %7, 1.0, %7\n\t"
                     "v_rcp_f32 %0, %0\n\tv_rcp_f32 %1, %1\n\tv_rcp_f32 %2, %2\n\tv_rcp_f32 %3, %3\n\tv_rcp_f32 %4, %4\n\tv_rcp_f32 %5, %5\n\tv_rcp_f32 %6, %6\n\tv_rcp_f32 %7, %7"
                     : "=&v"(as[0]), "=&v"(as[1]), "=&v"(as[2]), "=&v"(as[3]), "=&v"(as[4]), "=&v"(as[5]), "=&v"(as[6]), "=&v"(as[7])
                     : "v"(av[0][0]), "v"(av[0][1]), "v"(av[0][2]), "v"(av[0][3]), "v"(av[1][0]), "v"(av[1][1]), "v"(av[1][2]), "v"(av[1][3]));
        FI(5) TH(4)
        unsigned flg = LDSV(c.wf[1] + (OFF_FL + 2048 - OFF_B - SLOT));
        SB
        swl<18 + SWA, 18 + SWA + SW3>(c);
        FI(6)
        TG(c.tq[0]) TF(0)
        SB
        WAITCNT(63, 0);
        SB
        wait_set<0>(flg, c.wf[1] + (OFF_FL + 2048 - OFF_B - SLOT), c.slow);
        SB TF(1)
#define WSEQ(n) LDS(const u32x4, c.wf[((n) >> 4) & 1] + ((n) >> 5) * SLOT + (((n) & 15) ^ ((~(n) >> 2) & 1)) * 1024)
        u32x4 wq[64];
#pragma unroll
        for (int r = 0; r < WPRE; ++r) wq[r] = WSEQ(r);
        SB TF(2)
        swl<18 + SWA + SW3, 32>(c);
        SPLIT(4, 6, 0, 8)
        CB(bp, 0, 0, 2) CB(bp, 0, 1, 2)
        f32x2 ao[4] = {{as[0], as[1]}, {as[2], as[3]}, {as[4], as[5]}, {as[6], as[7]}};
        {
            f32x2 m[4] = {{c.ew[0][0], c.ew[0][1]}, {c.ew[1][0], c.ew[1][1]}, {c.ew[0][2], c.ew[0][3]}, {c.ew[1][2], c.ew[1][3]}};
#pragma unroll
            for (int r = 0; r < 4; r += 2) {
                asm volatile("v_pk_mul_f32 %0, %1, %0 op_sel_hi:[0,1]" : "+v"(m[r]) : "v"(ao[3]));
                asm volatile("v_pk_mul_f32 %0, %1, %0 op_sel:[1,0]" : "+v"(m[r + 1]) : "v"(ao[3]));
            }
            SC(0, 0) SC(0, 1) SC(0, 2) TF(3)
#pragma unroll
            for (int r = 0; r < 4; ++r) bq[r][3] = cvt_o(m[r & 2][r & 1], m[(r & 2) + 1][r & 1]);
        }
        ao[3][0] = sel(c.am, as[7], as[6]);
        PIN(ao[3]);
        SB
        FI(8)
        TG(c.tq[1]) TF(4)
        f32x4 u[2][4];
        float S1[7][16];
#define SQ(n) (((n) >> 1) + 4 * ((n) & 1))
#define FM(n, h, k) if ((k) < NJP) { asm volatile("v_pk_fma_f32 %0, %1, %2, %3" ST : "+v"(T[2 * (k) + (h)][SQ(n)]) : "v"(f32x2{u[h][2 + ((k) >> 1)][2 * ((k) & 1)], u[h][2 + ((k) >> 1)][2 * ((k) & 1) + 1]}), "v"(ao[k]), "v"(f32x2{u[h][(k) >> 1][2 * ((k) & 1)], u[h][(k) >> 1][2 * ((k) & 1) + 1]})); }
#define F1(n, h, s) if ((s) >= 2 * NJP) { float r_; \
            if ((s) < 6) { r_ = T[(s) % 6][SQ(n)][h]; asm volatile("v_fma_f32 %0, %1, %2, %3" : "+v"(r_) : "v"(u[h][2 + ((s) >> 2)][(s) & 3]), "v"(ao[(s) >> 1][(s) & 1]), "v"(u[h][(s) >> 2][(s) & 3])); } \
            else { asm volatile("v_fma_f32 %0, %1, %2, %3" : "=v"(r_) : "v"(u[h][3][2]), "v"(ao[3][0]), "v"(u[h][1][2]), "v"(u[h][3][3]), "v"(u[h][1][3])); } S1[s][2 * SQ(n) + (h)] = r_; }
#define FS1(h, k, d) { asm volatile("v_fma_f32 %0, %1, %2, %3" :: "v"(T[2 * (k) + (h)][7][d]), "v"(u[h][2 + ((k) >> 1)][2 * ((k) & 1) + (d)]), "v"(ao[k][d]), "v"(u[h][(k) >> 1][2 * ((k) & 1) + (d)])); }
#define LI 1
#define TT8(s, q) f32x8{T[s][2 * (q)][0], T[s][2 * (q)][1], T[s][2 * (q) + 1][0], T[s][2 * (q) + 1][1], T[s][2 * (q) + 4][0], T[s][2 * (q) + 4][1], T[s][2 * (q) + 5][0], T[s][2 * (q) + 5][1]}
#define SS8(s, q) f32x8{S1[s][4 * (q)], S1[s][4 * (q) + 1], S1[s][4 * (q) + 2], S1[s][4 * (q) + 3], S1[s][4 * (q) + 8], S1[s][4 * (q) + 9], S1[s][4 * (q) + 10], S1[s][4 * (q) + 11]}
#define DOT1(a_, b_) asm volatile("v_smfmac_f32_16x16x64_bf16 %0, %1, %2, %3 abid:1" : "+v"(c.dg) : "v"(a_), "v"(b_), "v"(c.six));
#define DOT(q, s) { if ((s) == 6) { DOT1(c.X[0], (SS8(6, q))) } else if ((s) < 2 * NJP) { DOT1(c.Y[(4 * ((s) >> 1) + ((s) & 1) + 2 * (q)) % 12], (TT8((s) % 6, q))) } \
            else { DOT1(c.X[1 + 4 * (((s) % 6) >> 1) + ((s) & 1) + 2 * (q)], (SS8(s, q))) } }
#define DPOS 0x0600000504070301ull
#define DPOS2 0x20
#define DP(n, j) if (n >= 4 && n < 6) { constexpr int s_ = (((DPOS | DPOS2) >> (4 * ((8 * n + j) & 15))) & 15); if (s_) DOT(0, (s_ + 6) % 7) }
#define XQ(n, j) (((n) & 3) * 4 + ((j) > 5 ? 4 : (j) > 3 ? 2 : (j) - 2))
#define PJ(n, j) { constexpr int i = SQ(n), i1 = SQ((n + 1) & 7), np = (n + 7) & 7, z = 8 * n + j, h = j >> 2; \
                constexpr bool nx = n < 7, pv = n >= 1, al = nx && n != 1; \
                SB \
                if (z == FLW + 4 * NE - 16) { wait_set<2>(flg, c.wf[1] + (OFF_FL - OFF_B - SLOT), c.slow); SB } \
                if (z == FLW - 16) { wait_set<1>(flg, c.wf[1] + (OFF_FL + FL1 - OFF_B - SLOT), c.slow); SB } \
                if (!(j & 1)) { if (z + WPRE <= 64) WAITCNT(63, WPRE - 2); SB } \
                if (j & 2) u[h][(j & 3) ^ h ^ 1] = mf0(wq[z], bq[(2 * i + h) & 3]); else u[h][(j & 3) ^ h ^ 1] = mf0(wq[z], bp[(2 * i + h) & 3]); \
                SB \
                if (z == FLR + 4 * NE - 16) flg = LDSV(c.wf[1] + (OFF_FL - OFF_B - SLOT)); \
                if (z == FLR - 16) flg = LDSV(c.wf[1] + (OFF_FL + FL1 - OFF_B - SLOT)); \
                if (z + WPRE < 64) wq[(z + WPRE) & 63] = WSEQ(z + WPRE); \
                SB \
                if (j == 0) { CB(bq, i, 0, 0) DP(n, 0) CB(bq, i, 0, 1) } \
                if (j == 1) { CB(bq, i, 0, 2) DP(n, 1) CB(bq, i, 1, 0) CB(bq, i, 1, 1) } \
                if (n >= 4 && (j == 2 || j == 3 || j == 5)) xpair<2, XQ(n, j), XQ(n, j + 1)>(c); \
                if (j == 2) { if (pv) F1(np, 1, 4) CB(bq, i, 1, 2) DP(n, 2) if (pv) { F1(np, 1, 0) F1(np, 1, 1) F1(np, 1, 2) F1(np, 1, 3) } } \
                if (j == 3) { if (pv) F1(np, 1, 5) DP(n, 3) if (pv) F1(np, 1, 6) } \
                if (j == 4) { if (al) CB(bp, i1, 0, 0) DP(n, 4) if (al) CB(bp, i1, 0, 1) } \
                if (j == 5) { F1(n, 0, 4) DP(n, 5) F1(n, 0, 5) F1(n, 0, 6) } \
                if (j == 6) { if (nx) CB(bp, i1, 0, 2) DP(n, 6) if (al) CB(bp, i1, 1, 0) F1(n, 0, 0) F1(n, 0, 1) } \
                if (LI && n == 7) { if (j == 4 || j > 5) { FS1(0, j == 4 ? 2 : j & 1, 0) FS1(0, j == 4 ? 2 : j & 1, 1) } } \
                else if (j == 7) { \
                    F1(n, 0, 2) F1(n, 0, 3) \
                    if (al) CB(bp, i1, 1, 1) \
                    if (nx) CB(bp, i1, 1, 2) \
                    FM(n, 0, 0) FM(n, 0, 1) FM(n, 0, 2) \
                    if (nx) { SC(i1, 0) SC(i1, 1) SC(i1, 2) } else asm volatile("s_nop 3"); \
                    FM(n, 1, 0) FM(n, 1, 1) FM(n, 1, 2) \
                    if (n == 3) { f32x2 a_, b_; asm volatile("v_mov_b64 %0, 0\n\tv_mov_b64 %1, 0" : "=v"(a_), "=v"(b_)); c.dg = f32x4{a_[0], a_[1], b_[0], b_[1]}; } \
                } }
        static_assert(FLW + WPRE <= 32 && FLZ <= FLR && FLR < FLW, "");
#define CP 9
#define PN(n) TI(n) if (n == CP) { __builtin_amdgcn_s_setprio(0); SB } PJ(n, 0) PJ(n, 1) PJ(n, 2) PJ(n, 3) PJ(n, 4) PJ(n, 5) PJ(n, 6) PJ(n, 7)
        PN(0)
        PN(1)
        TG(c.tq[2])
        PN(2)
        PN(3)
        TG(c.tq[3])
        PN(4) PN(5) PN(6) PN(7) TI(8)
        unsigned s8;
        double big;
#define SU0 { SB s8 = __builtin_amdgcn_mbcnt_lo((unsigned)act, 0u); asm volatile("" :: "v"(s8)); SB }
#define SU1 { SB s8 = __builtin_amdgcn_mbcnt_hi((unsigned)(act >> 32), s8); asm volatile("" :: "v"(s8)); SB }
#define SU2 { SB s8 <<= 3; asm volatile("" :: "v"(s8)); SB }
#define SU3 { SB big = bigs; PIN(big); SB }
#undef DOT1
#define DOT1(a_, b_) asm volatile("v_smfmac_f32_16x16x64_bf16 %0, %1, %2, %3 abid:1" :: "v"(c.dg), "v"(a_), "v"(b_), "v"(c.six));
        if (LI) {
            DOT(1, 4) SU0 FS1(1, 0, 0) DOT(1, 0) FS1(1, 0, 1) FS1(1, 1, 0) DOT(1, 2) FS1(1, 1, 1) FS1(1, 2, 0) DOT(1, 1) FS1(1, 2, 1) F1(7, 1, 6)
            DOT(1, 3) SU1 DOT(1, 5) SU2 DOT(1, 6) SU3
        } else {
            SU0 SU1
            DOT(1, 0) SU2 DOT(1, 2) F1(7, 1, 6) DOT(1, 1) SU3 DOT(1, 3) DOT(1, 4) DOT(1, 5) DOT(1, 6)
        }
        asm volatile("s_mov_b64 exec, %1" : "+v"(c.dg) : "s"(act));
        snop(LI ? 5 : 6);
        SB
        const bool b0 = __builtin_amdgcn_inverse_ballot_w64(m1), b1 = __builtin_amdgcn_inverse_ballot_w64(m2);
        const float va = b0 ? c.dg[1] : c.dg[0], vb = b0 ? c.dg[3] : c.dg[2], v = b1 ? vb : va;
        TG(c.tq[4])
        STAMP(4)
        TS(c.ts[14])
        deliver(c, v, big, 2 * nq + 1, s8, fk, sums, outp);
        ALL
        const int ln = __builtin_amdgcn_mbcnt_hi(~0u, __builtin_amdgcn_mbcnt_lo(~0u, 0u));
        const int lane = ln, tid = w * 64 + ln;
        (void)lane; (void)tid;
        FINISH(c)
    }
}

extern "C" void kernel_launch(void* const* d_in, const int* in_sizes, int n_in,
                              void* d_out, int out_size, void* d_ws, size_t ws_size,
                              hipStream_t stream) {
    const int* idx = (const int*)d_in[0];
    const uint16_t* table = (const uint16_t*)d_in[1];
    const uint16_t* se_w1 = (const uint16_t*)d_in[2];
    const uint16_t* se_w2 = (const uint16_t*)d_in[3];
    const uint16_t* w_bi = (const uint16_t*)d_in[4];
    const uint16_t* w_cross = (const uint16_t*)d_in[5];
    const uint16_t* w_deep1 = (const uint16_t*)d_in[6];
    const uint16_t* w_deep2 = (const uint16_t*)d_in[7];
    const char* w1a = (const char*)((unsigned long long)w_deep1 & 0xffffffffffffull);
    const unsigned long long w1b = 0x0002000000000000ull | (NF * 32768);
    (void)hipFuncSetAttribute((const void*)k_main, hipFuncAttributeMaxDynamicSharedMemorySize, LDS_T);
    k_main<<<256, 512, LDS_T, stream>>>(idx, table, w1a, w1b, w_cross, (unsigned*)d_ws, se_w1, w_bi, se_w2, w_deep2, (__hip_bfloat16*)d_out);
}
```

```cpp
#include <hip/hip_runtime.h>
#include <hip/hip_bf16.h>
#include <stdint.h>

#define EXM 1
#define PU 0
#define WSX 0
#define TB 0x2344
#define TBP 4
#define SMP 0
#define PRO 4
#define B1P 0
#define L1M 8
#define TGE 0
#define PT 7
#define NF 26
#define TRC 1.0028f
#define NP 13
#define PROD_CLASS(slot) ((((slot) & 3) == 1) && (((slot) >> 2) & 1) == 0)
#define STAMP(k)
#define TS(x)
#define TSP(P)
#define TX(k)
#define TP(x)
#define TEM 0x3ff
#define TE(x)
#define TI(n)
#define TF(k)
#define TH(k)
#define TG(x) TE(x)
using bf16x8 = __attribute__((ext_vector_type(8))) short;
using f32x4 = __attribute__((ext_vector_type(4))) float;
using f32x8 = __attribute__((ext_vector_type(8))) float;
using f32x2 = __attribute__((ext_vector_type(2))) float;
using s16x4 = __attribute__((ext_vector_type(4))) short;
using u32x4 = __attribute__((ext_vector_type(4))) unsigned;
using u32x8 = __attribute__((ext_vector_type(8))) unsigned;
using u32x2 = __attribute__((ext_vector_type(2))) unsigned;
using bf2 = __attribute__((ext_vector_type(2))) __bf16;
#define ST "\n\tSTUB"
#define ST_ "STUB\n\t"
#define PIN(x) asm volatile("" : "+v"(x))
#define PINS(x) asm volatile("" : "+s"(x))
#define SB __builtin_amdgcn_sched_barrier(0);
#define CAT8(a, b) __builtin_shufflevector((a), (b), 0, 1, 2, 3, 4, 5, 6, 7)

#define SLOT 16384
#define OFF_B 65536
#define OFF_FL 131072
#define OFF_WBI (OFF_FL + 4096)
#define OFF_SW1 (OFF_WBI + 2048)
#define OFF_SW2 (OFF_SW1 + 1024)
#define OFF_WD2 (PRO & 2 ? OFF_SW2 + 1024 : OFF_SW1 + 768)
#define OFF_TAG (OFF_SW2 + 1280)
#define OFF_NB (OFF_TAG + 768)
#define OFF_JNK (OFF_NB + 2048)
#define OFF_IDX (OFF_JNK + 2048)
#define OFF_RAW (OFF_IDX + 8192)
#define LDS_T (OFF_IDX + 16384)
constexpr int FOFF[4] = {SLOT, OFF_B + SLOT, 2 * SLOT, OFF_B + 2 * SLOT};
#define P_FLAG 3
#define P_POLL 6
#define P_ST 3
#define P_CHK 9
#define P_STAT 8

#define WS_FLAG 1024
#define WS_SUM 4096
#define WS_SIDE 65536
#define WS_FRAG (1 << 20)

#define LDS(T, a) (*(__attribute__((address_space(3))) T*)(size_t)(a))
#define LDSV(a) (*(volatile const __attribute__((address_space(3))) unsigned*)(size_t)(a))
__device__ __forceinline__ unsigned pk_bf16(float lo, float hi) {
    const f32x2 v = {lo, hi};
    return __builtin_bit_cast(unsigned, __builtin_convertvector(v, bf2));
}
__device__ __forceinline__ unsigned cvt_o(float lo, float hi) { unsigned r; asm volatile("v_cvt_pk_bf16_f32 %0, %1, %2" ST : "=v"(r) : "v"(lo), "v"(hi)); return r; }
__device__ __forceinline__ float exp_o(float a) { float r; asm volatile("v_exp_f32 %0, %1" : "=v"(r) : "v"(a)); return r; }
template <typename A, typename B> __device__ __forceinline__ f32x4 mf0(const A& a, const B& b) { f32x4 d; asm volatile("v_mfma_f32_16x16x32_bf16 %0, %1, %2, 0" ST : "=v"(d) : "v"(a), "v"(b)); return d; }
template <typename A, typename B> __device__ __forceinline__ void mfa(f32x4& d, const A& a, const B& b) { asm volatile("v_mfma_f32_16x16x32_bf16 %0, %1, %2, %0" ST : "+v"(d) : "v"(a), "v"(b)); }
__device__ __forceinline__ float lo_f(unsigned x) { return __uint_as_float(x << 16); }
__device__ __forceinline__ float hi_f(unsigned x) { return __uint_as_float(x & 0xffff0000u); }
__device__ __forceinline__ s16x4 tr_read(unsigned a) {
    typedef short v4s __attribute__((ext_vector_type(4)));
    return __builtin_amdgcn_ds_read_tr16_b64_v4i16((__attribute__((address_space(3))) v4s*)(size_t)a);
}
#define DMA(name, ins) __device__ __forceinline__ void name(unsigned voff, const void* sbase, unsigned ldsaddr) { \
    asm volatile("s_mov_b32 m0, %2\n\ts_nop 0\n\t" ins :: "v"(voff), "s"(sbase), "s"(ldsaddr) : "memory"); }
DMA(dma16, "global_load_lds_dwordx4 %0, %1") DMA(dma4, "global_load_lds_dword %0, %1")
DMA(dma16sc, "global_load_lds_dwordx4 %0, %1 sc1") DMA(dma4sc, "global_load_lds_dword %0, %1 sc1")
__device__ __forceinline__ void st16sc(unsigned voff, void* sbase, u32x4 v) {
    asm volatile("global_store_dwordx4 %0, %1, %2 sc1\n\ts_nop 1" :: "v"(voff), "v"(v), "s"(sbase) : "memory");
}
template <int O> __device__ __forceinline__ void set_m0(unsigned base) { asm volatile("s_add_u32 m0, %0, %1" :: "s"(base), "n"(O) : "memory", "scc"); }
template <int O> __device__ __forceinline__ void bdma16o(unsigned voff, u32x4 rs, unsigned soff) {
    asm volatile("buffer_load_dwordx4 %0, %1, %2 offen offset:%3 lds" :: "v"(voff), "s"(rs), "s"(soff), "n"(O) : "memory");
}
template <int O> __device__ __forceinline__ void dma16o(unsigned voff, const void* sbase) {
    asm volatile("global_load_lds_dwordx4 %0, %1 offset:%2" :: "v"(voff), "s"(sbase), "n"(O) : "memory");
}
template <int O> __device__ __forceinline__ void dma16sco(unsigned voff, const void* sbase) {
    asm volatile("global_load_lds_dwordx4 %0, %1 offset:%2 sc1" ST :: "v"(voff), "s"(sbase), "n"(O) : "memory");
}
#define WAITCNT(vm, lgkm) __builtin_amdgcn_s_waitcnt(((vm) & 15) | (((vm) >> 4) << 14) | 0x70 | ((lgkm) << 8))
#define WAITX(vm, x, m) { WAITCNT((vm) + (x), 0); asm volatile("s_cmp_eq_u32 %0, 0\n\ts_cbranch_scc1 1f\n\ts_waitcnt vmcnt(%1)\n1:" :: "s"(m), "n"(vm) : "memory", "scc"); }
#define BARRIER asm volatile("s_barrier" ST ::: "memory")
extern "C" __device__ void snop(short) __asm("llvm.amdgcn.s.nop");
#define USE(x) asm volatile("" :: "v"(x))
__device__ __forceinline__ float sum32(float v) { const u32x2 p = __builtin_amdgcn_permlane32_swap(__float_as_uint(v), __float_as_uint(v), false, false); return __uint_as_float(p[0]) + __uint_as_float(p[1]); }
__device__ __forceinline__ float sum16(float v) { const u32x2 p = __builtin_amdgcn_permlane16_swap(__float_as_uint(v), __float_as_uint(v), false, false); return __uint_as_float(p[0]) + __uint_as_float(p[1]); }
__device__ __forceinline__ float sel(unsigned m, float a, float b) {
    return __uint_as_float(__builtin_amdgcn_bitop3_b32(m, __float_as_uint(a), __float_as_uint(b), 0xca));
}
#define SLOW(b)
template <int b> __device__ __forceinline__ void wait_set(unsigned f, unsigned a, unsigned& slow) {
    if (__builtin_expect(__builtin_amdgcn_ballot_w64(f == 0u) != 0ull, (WSX >> b) & 1)) {
        SLOW(b)
        while (__builtin_amdgcn_ballot_w64(LDSV(a) == 0u) != 0ull) { }
    }
    asm volatile("" ::: "memory");
}

struct Com {
    char* ws;
    unsigned lane16, ldsb, slow = 0;
    int c, nq, rho, st;
};
typedef __attribute__((address_space(1))) char* gp;
struct Fin { unsigned kr, kl; double c0; unsigned long long last, lim; };
__device__ __forceinline__ void fin(Fin& k) {
    k.kr = 0x7d000000u; k.kl = 0xde000000u; k.c0 = -0x1.1p11; k.last = 0x1c00000000000000ull; k.lim = 0x1c04000000000000ull;
    PINS(k.kr); PINS(k.kl); PINS(k.c0); PINS(k.last); PINS(k.lim);
}
template <class F> __device__ __forceinline__ void deliver(const Com& c, float v, double big, int part, unsigned s8, Fin k, gp sums, gp outp, F meanwhile) {
    double mine = __builtin_fma((double)v, 0x1p-969, big);
    const bool far = (__float_as_uint(v) << 1) + k.kr < k.kl;
    if (__builtin_expect(__builtin_amdgcn_ballot_w64(far) != 0ull, 0)) {
        if (far) {
            mine = __builtin_bit_cast(double, u32x2{0u, 0x04004000u + (0x40000u << part)});
            __hip_atomic_store((__attribute__((address_space(1))) float*)((gp)c.ws + (WS_SIDE + c.st * 2048 + c.c * 64 + part * 256) + (s8 >> 1)), v, __ATOMIC_RELAXED, __HIP_MEMORY_SCOPE_AGENT);
        }
        asm volatile("s_waitcnt vmcnt(0)" ::: "memory");
        k.lim = 0ull;
    }
    SB
    const unsigned long long ret = __hip_atomic_fetch_add((__attribute__((address_space(1))) unsigned long long*)(sums + s8), __builtin_bit_cast(unsigned long long, mine), __ATOMIC_RELAXED, __HIP_MEMORY_SCOPE_AGENT);
    SB
    double own = __builtin_fma(mine, 0x1p969, k.c0);
    unsigned s2 = s8 >> 2;
    PIN(own); PIN(s2);
    SB
    meanwhile();
    SB
    own = __builtin_fma(__builtin_bit_cast(double, ret), 0x1p585, own);
    SB
    asm volatile("v_cmpx_le_u64 vcc, %0, %1" :: "s"(k.last), "v"(ret), "v"(own) : "vcc");
    SB
    float r = (float)own;
    asm volatile("" :: "v"(r));
    SB
    if (__builtin_expect(__builtin_amdgcn_ballot_w64(ret >= k.lim) != 0ull, 0)) {
        const unsigned long long tot = ret + __builtin_bit_cast(unsigned long long, mine);
        const unsigned th = (unsigned)(tot >> 32);
        r = (float)__builtin_fma(__builtin_bit_cast(double, tot & 0xfc03ffffffffffffull), 0x1p521, -0x1.2p10);
#pragma unroll 1
        for (int q = 0; q < 8; ++q)
            if ((th >> (18 + q)) & 1u) r += __hip_atomic_load((__attribute__((address_space(1))) float*)((gp)c.ws + (WS_SIDE + c.st * 2048 + c.c * 64 + q * 256) + (s8 >> 1)), __ATOMIC_RELAXED, __HIP_MEMORY_SCOPE_AGENT);
    }
    *(__attribute__((address_space(1))) uint16_t*)(outp + s2) = (uint16_t)pk_bf16(r, r);
}
#define BIG 0x1.04p-959

struct Deep : Com {
    u32x4 rw1;
    const char* wsf[2]; const char* wsh[2];
    unsigned ldsh;
    unsigned sG[4], sB, tb;
    unsigned m1, m3, m4;
    int unit;
    unsigned long long pmask;
    unsigned xa;
    unsigned one;
    unsigned tad, tag, pl;
    unsigned boff[4], ra[4], rb[4];
    u32x4 w2[2];
    f32x4 ac[4][4];
    bf16x8 fa[2][4];
    s16x4 lo[2][4], hi[2][4];
};
template <int Q> __device__ __forceinline__ void prepB(Deep& c) {
    set_m0<OFF_B + (Q & 3) * SLOT>(c.ldsb);
    SB
    if constexpr (Q == 0) c.sB = 24 * 32768; else c.sB = c.sG[(Q - 1) & 3] + ((Q - 1) >> 2) * (8 * 32768);
    asm volatile("" :: "s"(c.sB));
    SB
}
template <int I> __device__ __forceinline__ void issueB(Deep& c) { bdma16o<I * 1024>(c.boff[I], c.rw1, c.sB); }
__device__ __forceinline__ void mkboff(Deep& d, int lane, int cw, int nq) {
    const unsigned b0 = (unsigned)(((cw & 1) * 64 + (cw >> 1) * 32 + (lane >> 3)) * 512 + nq * 128 + ((lane & 7) ^ ((lane >> 3) & 2)) * 16);
    d.boff[0] = b0; d.boff[1] = (b0 ^ 64u) + 3072; d.boff[2] = b0 + 6144; d.boff[3] = (b0 ^ 64u) + 9216;
#pragma unroll
    for (int k = 0; k < 4; ++k) PIN(d.boff[k]);
}
template <int P, int N> __device__ __forceinline__ void rd1(Deep& c) {
    constexpr int tab[12] = {0, 4, 5, 6, 7, 1, 2, 3, 8, 9, 10, 11}, k = tab[N < 12 ? N : 0];
    if constexpr (N >= 12) { }
    else if constexpr (k < 4) c.fa[P & 1][k] = LDS(const bf16x8, c.ra[k] + (P & 3) * SLOT);
    else if constexpr (k & 1) c.hi[P & 1][(k - 4) >> 1] = tr_read(c.rb[(k - 4) >> 1] + (P & 3) * SLOT + 512);
    else c.lo[P & 1][(k - 4) >> 1] = tr_read(c.rb[(k - 4) >> 1] + (P & 3) * SLOT);
    SB
}
template <int P, int M, int K> __device__ __forceinline__ void mk(Deep& c) {
    if constexpr (P >= 0) mfa(c.ac[M][K], CAT8(c.lo[P & 1][K], c.hi[P & 1][K]), c.fa[P & 1][M]);
    SB
}
#define DEF11 0
template <int P, int N> __device__ __forceinline__ void m1(Deep& c) { if constexpr (!(DEF11 && P == 11 && ((N >> 1) & 3) == 0)) mk<P, (N >> 1) & 3, (N >> 3) * 2 + (N & 1)>(c); }
template <int P, int M> __device__ __forceinline__ void m4(Deep& c) { mk<P, M, 0>(c); mk<P, M, 1>(c); mk<P, M, 2>(c); mk<P, M, 3>(c); }
#define E9 2
#define T9 0
#define TOP10 4
constexpr int dyoung(int S) { return S < 7 ? 8 : S < 9 ? 4 : E9; }
template <int U0, int I> __device__ __forceinline__ void dq(Deep& c) {
    constexpr int U = U0 + (I >> 2);
    if constexpr (U < NP) {
        if constexpr ((I & 3) == 0 && U >= 10) prepB<U>(c);
        issueB<I & 3>(c);
        if constexpr (U == 12 && (I & 3) == 3) set_m0<FOFF[1]>(c.ldsb);
    } else if constexpr (U == 15) {
        dma16sco<(I & 1) * 1024>(c.lane16, c.wsh[(I >> 1) & 1]);
        if constexpr ((I & 3) == 3) set_m0<FOFF[3]>(c.ldsb);
    } else {
        dma16sco<(I & 3) * 1024>(c.lane16, c.wsf[U == 16]);
        if constexpr (U == 14 && (I & 3) == 3) set_m0<FOFF[2]>(c.ldsh);
    }
    SB
}
template <int S, int I0, int I1> __device__ __forceinline__ void dissue(Deep& c) {
    if constexpr (I0 < I1 && I0 < 4 && S < 7) { dq<S + 3, I0>(c); dissue<S, I0 + 1, I1>(c); }
}
constexpr int cum(unsigned long long t, int n) { int k = 0; for (int i = 0; i < n; ++i) k += (t >> (4 * i)) & 15; return k; }
template <int U0, int I0, int I1> __device__ __forceinline__ void dqs(Deep& c) { if constexpr (I0 < I1) { dq<U0, I0>(c); dqs<U0, I0 + 1, I1>(c); } }
template <int P, int N0, int N1> __device__ __forceinline__ void rdn(Deep& c) { if constexpr (N0 < N1) { rd1<P, N0>(c); rdn<P, N0 + 1, N1>(c); } }
__device__ __forceinline__ unsigned tag_addr(int lane, int unit, int nq, int st, int xcd) {
    const unsigned l4 = (unsigned)lane * 4u;
    const unsigned a0 = (unsigned)(WS_FLAG + unit * 4) + l4 * 32u;
    const unsigned a1 = (((unsigned)(nq * 256) + l4 * 64u) & 768u) + (unsigned)(st * 4);
    const unsigned a2 = (unsigned)(WS_FLAG + xcd * 128 - 64) + l4;
    const unsigned lo = l4 < 32u ? a0 : a1, hi = l4 < 168u ? a2 : (unsigned)(st * 4);
    return l4 < 64u ? lo : hi;
}
__device__ __forceinline__ void tags(unsigned tad, const void* ws, unsigned lds, unsigned long long m) {
    asm volatile("s_mov_b32 m0, %2\n\ts_mov_b64 exec, %3\n\tglobal_load_lds_dword %0, %1 sc1\n\ts_mov_b64 exec, -1" :: "v"(tad), "s"(ws), "s"(lds), "s"(m) : "memory");
}
__device__ __forceinline__ void publish(Deep& c, unsigned long long m) {
    asm volatile("s_cmp_eq_u64 %3, 0\n\ts_cbranch_scc1 1f\n\ts_mov_b64 exec, %3\n\tglobal_atomic_add %0, %1, %2\n\ts_mov_b64 exec, -1\n1:"
                 :: "v"(c.tad), "v"(c.one), "s"(c.ws), "s"(m) : "memory", "scc");
}
__device__ __forceinline__ void rawo(const Deep& d, unsigned lane16, int cw, unsigned (&ro)[3]) {
    const unsigned r0 = (lane16 >> 6) & 15u, u = (unsigned)((cw >> 1) * (NF * NF * 128) + d.unit * 128 + (cw & 1) * 64) + ((lane16 ^ (lane16 >> 3 & 0x20u)) & 0x30u);
    ro[0] = r0 * (NF * 128) + u; ro[1] = min(r0 + 16u, NF - 1u) * (NF * 128) + u - 1024u; ro[2] = (lane16 >> 2) + (unsigned)(cw * 256);
}
__device__ __forceinline__ void raw(const Deep& d, unsigned m1, int cw, const void* w_cross, const void* ws, unsigned (&ro)[3]) {
    if (__builtin_expect(m1 == 0u, 1)) {
        dma16(ro[0], w_cross, OFF_RAW + cw * 2048);
        dma16o<1024>(ro[1], w_cross);
        dma4sc(ro[2], ws, OFF_NB + cw * 256);
        set_m0<OFF_B>(d.ldsb); snop(0);
    }
    SB
}
template <int W> __device__ __forceinline__ void produce(Deep& c) {
    if (c.m1 == 0u) {
        const int unit = c.unit;
        const unsigned p8 = (c.lane16 << 4) & 0x300u, e2 = (c.lane16 >> 5) & 0x1eu;
        const unsigned rb = (unsigned)(OFF_RAW + c.c * 2048) + e2, rs = rb + (p8 >> 1) + (c.lane16 & 0x20u);
        const unsigned dm = p8 == (c.lane16 & 0x300u) ? ~0u : 0u;
        const int m = unit < 24 ? 2 * (unit & 6) + (unit & 1) + ((unit >> 2) & 2) : 4 * (unit & 1) + 2;
        unsigned fo = (unsigned)(WS_FRAG + (c.c & 1) * 131072 + (c.c >> 1) * 2048 + (unit >> 4) * 1024 + m * 64) + (((e2 & 12u) << 1) | ((e2 >> 2) & 4u) | (e2 & 2u)) * 2048u + ((c.lane16 & 0x30u) ^ (unsigned)((m & 4) << 3));
        if (unit >= 24) fo += (c.lane16 << 1) & 0x200u;
        WAITCNT(W, 15);
        asm volatile("" ::: "memory");
        u32x4 fr[2];
#pragma unroll
        for (int z = 0; z < 2; ++z) {
#pragma unroll
            for (int d = 0; d < 4; ++d) {
                const unsigned rp = (d < 3 ? rs ^ (unsigned)(z * 32) : rb + z * 32) + (unsigned)(512 * d);
                fr[z][d] = LDS(const uint16_t, rp) | ((unsigned)LDS(const uint16_t, rp + 64) << 16);
            }
            fr[z][3] &= dm;
        }
        st16sc(fo, c.ws, fr[0]); st16sc(fo + 65536u, c.ws, fr[1]);
        if (unit >= 24) { st16sc(fo ^ 512u, c.ws, u32x4{0u, 0u, 0u, 0u}); st16sc((fo ^ 512u) + 65536u, c.ws, u32x4{0u, 0u, 0u, 0u}); }
    }
    SB
}
template <bool L, int I> __device__ __forceinline__ void part(Deep& c) {
#define PART(cmp) asm volatile(cmp " %0, %1\n\ts_cbranch_scc1 1f\n\tbuffer_load_dwordx4 %2, %3, %4 offen offset:%5 lds\n1:" \
                               :: "s"(c.tb), "n"(I), "v"(c.boff[I]), "s"(c.rw1), "s"(c.sB), "n"(I * 1024) : "memory")
    SB
    if constexpr (L) PART("s_cmp_gt_u32"); else PART("s_cmp_le_u32");
    SB
}
__device__ __forceinline__ void check_flags(Deep& c) {
    if (c.m3 == 0u) {
        const unsigned l4 = c.lane16 >> 2;
        const unsigned long long open = __builtin_amdgcn_ballot_w64(c.tag == c.pl) & (c.nq ? 0x7ffffff0000ull : 0x3ffffff0000ull);
        if (__builtin_expect(open != 0ull, 0)) {
            unsigned& slow = c.slow; (void)slow; SLOW(4)
#pragma unroll 1
            for (int it = 0; it < (1 << 22); ++it) {
                tags(c.tad, c.ws, OFF_TAG + 256, 0x7ffffff0000ull);
                asm volatile("s_waitcnt vmcnt(0)" ::: "memory");
                if ((__builtin_amdgcn_ballot_w64(LDSV(OFF_TAG + 256 + l4) == c.tag) & open) == 0ull) break;
                __builtin_amdgcn_s_sleep(2);
            }
        }
    }
    SB
}
__device__ __forceinline__ void fetch_status(Deep& c) {
    if (c.m4 == 0u) {
        if (c.nq) dma16sc(c.lane16, c.ws, OFF_NB + 1024);
        else tags(c.tad, c.ws, OFF_TAG + 512, 0xe00ull);
    }
    SB
}
constexpr bool two(int P, int S) { return P == S || P == S + 1; }
constexpr int xk(int P, int k) {
    int x = 0;
    if (k == 1 || k == 2 || k == 4) x += two(P, P_ST) + (two(P, P_POLL) && P + 1 < P_CHK);
    if (k == 4 && P_FLAG != P_ST) x += two(P, P_FLAG);
    if (k == 2 || k == 4) x += P == 9 ? P_STAT == 9 : two(P, P_STAT) && !(P == 8 && P_POLL >= P_STAT);
    return x;
}
constexpr int below(int P, int v) { int b = 0; for (int k = 0; k < 5; ++k) if (xk(P, k) < v && xk(P, k) > b) b = xk(P, k); return b; }
constexpr unsigned xbits(int k) {
    unsigned m = 0;
    for (int P = 0; P < 10; ++P) { int v = below(P, 99); for (int j = 0; v > 0; ++j, v = below(P, v)) if (xk(P, k) == v) m |= 1u << (3 * P + j); }
    return m;
}
template <int P> __device__ __forceinline__ void xwait(Deep& c) {
    constexpr int d = dyoung(P), a = below(P, 99), b = below(P, a), e = below(P, b), f = below(P, e);
    static_assert(f == 0, "");
    WAITCNT(d + a, 0);
#define XW(n) "s_bitcmp1_b32 %0, %" #n "\n\ts_cbranch_scc1 1f\n\ts_waitcnt vmcnt(%"
    if constexpr (e > 0) asm volatile(XW(1) "4)\n\t" XW(2) "5)\n\t" XW(3) "6)\n1:" :: "s"(c.xa), "n"(3 * P), "n"(3 * P + 1), "n"(3 * P + 2), "n"(d + b), "n"(d + e), "n"(d) : "memory", "scc");
    else if constexpr (b > 0) asm volatile(XW(1) "3)\n\t" XW(2) "4)\n1:" :: "s"(c.xa), "n"(3 * P), "n"(3 * P + 1), "n"(d + b), "n"(d) : "memory", "scc");
    else if constexpr (a > 0) asm volatile(XW(1) "2)\n1:" :: "s"(c.xa), "n"(3 * P), "n"(d) : "memory", "scc");
}
#define DTOP 2
#define DTOPB 4
#define A8 12
#define A9 12
#define A10 12
#define T8 0x842100
#define T10 0xaa
constexpr int adv(int P) { return P == 8 ? A8 : P == 9 ? A9 : P == 10 ? A10 : 0; }
constexpr int bits(unsigned t, int n) { return __builtin_popcount(n < 32 ? t & ((1u << n) - 1u) : t); }
template <int P, int N> __device__ __forceinline__ void drun(Deep& c) {
    constexpr int old = 16 - adv(P - 1), tot = old + adv(P), U0 = P == 8 ? 10 : 11, E = P == 8 ? 4 : P == 10 ? 8 : E9, I0 = P == 10 ? TOP10 : 0, per = old >= 12 ? 1 : old >= 6 ? 2 : old >= 4 ? 3 : 12;
    constexpr unsigned T = P == 8 ? T8 : P == 10 ? T10 : T9;
    if constexpr (P == 10 && N == 0) dqs<U0, E9, TOP10>(c);
    dqs<U0, I0 + bits(T, N), I0 + bits(T, N + 1)>(c);
    if constexpr (N == 0 && per == 12) rdn<P, 0, 12>(c);
    if constexpr (N < old) m1<P - 1, adv(P - 1) + N>(c); else m1<P, N - old>(c);
    if constexpr (per < 12) rdn<P, N * per, (N * per + per < 12 ? N * per + per : 12)>(c);
    if constexpr (N == 5) { TX(0) }
    if constexpr (N == 11) { TX(1) }
    if constexpr (N == 17) { TX(2) }
    if constexpr (N + 1 < tot) drun<P, N + 1>(c); else { dqs<U0, I0 + bits(T, tot), E>(c); TX(3) }
}
template <int P, int N> __device__ __forceinline__ void mrest(Deep& c) { if constexpr (N < 16) { m1<P, N>(c); mrest<P, N + 1>(c); } }
#define T11 0x1249
#define NE 6
#define TOP11 1
#define E11 0
#define R12 6
constexpr int mn(int a, int b) { return a < b ? a : b; }
constexpr bool VT = TB != 0x4444 || TBP != 4;
constexpr int TMIN = mn(mn(mn(TB & 15, (TB >> 4) & 15), mn((TB >> 8) & 15, TB >> 12)), TBP);
#define DT 0x2222222
#define DL 12
constexpr int dtop(int P) { return (P & 3) == 2 ? (VT ? TMIN : DTOPB) : (DT >> (4 * P)) & 15; }
template <int P, int N> __device__ __forceinline__ void dbody(Deep& c) {
    constexpr int top = dtop(P);
    m1<P - 1, N>(c);
    rd1<P, N>(c);
    if constexpr (VT && (P & 3) == 2) { if constexpr (N >= DL && N - DL + TMIN < 4) part<true, (N - DL + TMIN) & 3>(c); }
    else if constexpr (N >= DL) dissue<P, top + N - DL, (N == 15 ? 16 : top + N - DL + 1)>(c);
    if constexpr (N < 15) dbody<P, N + 1>(c);
}
template <int N> __device__ __forceinline__ void dlast(Deep& c) {
    dqs<14, TOP11 + bits(T11, N), TOP11 + bits(T11, N + 1)>(c);
    m1<11, N>(c);
    if constexpr (N == R12) { WAITCNT(TOP11 + bits(T11, N + 1), 15); SB }
    if constexpr (N >= R12 && N < R12 + 8) rd1<12, (N - R12 < 4 ? N - R12 + 1 : N - R12 + 4)>(c);
    if constexpr (N < 15) dlast<N + 1>(c); else dqs<14, TOP11 + bits(T11, 16), NE>(c);
}
template <int P> struct DStep {
    static __device__ __forceinline__ void run(Deep& c) {
        TSP(P)
        SB
        if constexpr (P == P_FLAG || P == P_ST) publish(c, P_FLAG == P_ST ? c.pmask : c.pmask & (P == P_ST ? 0x100ull : 0xffull));
        if constexpr (P == P_CHK) check_flags(c);
        if constexpr (P == P_STAT) fetch_status(c);
        if constexpr (P < 8) {
            dissue<P, 0, dtop(P)>(c);
            if constexpr (VT && (P & 3) == 2) {
                if constexpr (TMIN == 0) part<false, 0>(c);
                if constexpr (TMIN <= 1) part<false, 1>(c);
                if constexpr (TMIN <= 2) part<false, 2>(c);
                if constexpr (TMIN <= 3) part<false, 3>(c);
            }
            dbody<P, 0>(c);
        } else if constexpr (P <= 10) {
            drun<P, 0>(c);
            if constexpr (P == 10 && E11) { WAITCNT(4, 15); SB rdn<11, 1, 5>(c); rdn<11, 8, 12>(c); }
        } else {
            if constexpr (PU == 11) __builtin_amdgcn_s_setprio(2);
            rd1<11, 0>(c); rdn<11, 5, 8>(c);
            dqs<14, 0, TOP11>(c);
            if constexpr (!E11) { WAITCNT(4 + TOP11, 15); SB rdn<11, 1, 5>(c); rdn<11, 8, 12>(c); }
            mrest<10, A10>(c);
            rd1<12, 0>(c); rdn<12, 5, 8>(c);
            TS(c.ts[12])
            if constexpr (PU == 12) __builtin_amdgcn_s_setprio(2);
            dlast<0>(c);
        }
        if constexpr (P <= 10) {
            if constexpr (P == 4) {
                c.w2[0] = LDS(const u32x4, c.w2[0][0]); c.w2[1] = LDS(const u32x4, c.w2[1][0]);
            }
            if constexpr (P == P_POLL) { if (c.m3 == 0u) tags(c.tad, c.ws, OFF_TAG + 256, 0x7ffffff0000ull); }
            SB
            if constexpr (P < 6) prepB<P + 4>(c);
#define NPT 0x10000000000ull
            if constexpr ((NPT >> (4 * P)) & 15) asm volatile(".rept %0\n\ts_nop 0\n\t.endr" :: "n"((int)((NPT >> (4 * P)) & 15)));
            if constexpr (P == 10) WAITCNT(63, 0); else xwait<(P < 10 ? P : 0)>(c);
            TX(4)
            if constexpr (P == P_CHK - 1) { SB c.pl = LDSV(OFF_TAG + 256 + (c.lane16 >> 2)); SB }
            BARRIER;
            DStep<P + 1>::run(c);
        }
    }
};

struct Cross : Com {
    const char* tb[4]; const char* wsf;
    unsigned ga[40];
    unsigned re[2], rx[2], rx0[2], six, wf[2], pl, ph, fl;
    unsigned long long xm[2];
    f32x4 dg;
    s16x4 wa[2];
    bf16x8 wb[2], wr[2];
    u32x4 fe[2][2][2];
    f32x4 ew[NF], zs[4];
    u32x4 X[NP], Y[12], bp[4];
    float k0, k1, k2;
    unsigned am, wad[2];
    u32x4 on[4], s1, zq;
};
template <int I> __device__ __forceinline__ void issueA(Cross& c, unsigned off) { dma16o<I * 1024>(off, c.tb[I]); }
template <int K, int S, int N> __device__ __forceinline__ void sw1(Cross& c) {
    constexpr int a = 1 + 4 * K + (S == 1 ? 2 : (S == 3 ? 1 : 0)), b = 1 + 4 * K + (S == 0 ? 1 : (S == 2 ? 2 : 3)), Q = N >> 2, R = N & 3;
    float x = c.ew[2 * a + Q][R], y = c.ew[2 * b + Q][R];
    if (S >= 2) asm volatile("v_permlane32_swap_b32 %0, %1" ST : "+v"(x), "+v"(y)); else asm volatile("v_permlane16_swap_b32 %0, %1" ST : "+v"(x), "+v"(y));
    c.ew[2 * a + Q][R] = x; c.ew[2 * b + Q][R] = y;
}
template <int K, int S, int N0 = 0, int N1 = 8> __device__ __forceinline__ void swn(Cross& c) { if constexpr (N0 < N1) { sw1<K, S, N0>(c); swn<K, S, N0 + 1, N1>(c); } }
#define S8 24
#define S9 24
template <int N0, int N1> __device__ __forceinline__ void swl(Cross& c) { if constexpr (N0 < N1 && N0 < 32) { sw1<2, (N0 >> 3), (N0 & 7)>(c); swl<N0 + 1, N1>(c); } }
constexpr int sw0(int P) { return P <= 3 ? 0 : P <= 5 ? 8 : P == 6 ? 20 : P == 7 ? 32 : P == 8 ? 40 : P == 9 ? 40 + S8 : P == 10 ? 40 + S9 : 64; }
template <int P, int N0, int N1> __device__ __forceinline__ void tsw(Cross& c) {
    if constexpr (N0 < N1 && sw0(P) + N0 < sw0(P + 1)) { sw1<((sw0(P) + N0) >> 5), ((sw0(P) + N0) >> 3) & 3, (sw0(P) + N0) & 7>(c); tsw<P, N0 + 1, N1>(c); }
}
constexpr int fbuf(int P) { return P & 1; }
template <int P, int N> __device__ __forceinline__ void cmm(Cross& c) {
    if constexpr (P >= 0) {
        constexpr int B = fbuf(P);
        if constexpr (N < 2)
            asm volatile("v_smfmac_f32_16x16x64_bf16 %0, %1, %2, %3" :: "v"(c.zs[P ? (P - 1) >> 2 : 3]),
                         "v"(c.on[P ? (P - 1) & 3 : 0]), "v"(__builtin_shufflevector(c.fe[B][N][0], c.fe[B][N][1], 0, 1, 2, 3, 4, 5, 6, 7)), "v"(c.six));
        else if constexpr (N < 4) c.ew[2 * P + (N & 1)] = mf0(P ? c.wb[0] : c.wr[0], c.fe[B][0][N & 1]);
        else mfa(c.ew[2 * P + (N & 1)], P ? c.wb[1] : c.wr[1], c.fe[B][1][N & 1]);
    }
    SB
}
template <int P, int N> __device__ __forceinline__ void rdE(Cross& c) {
    c.fe[fbuf(P)][N >> 1][N & 1] = LDS(const u32x4, c.re[N >> 1] + (P & 3) * SLOT + (N & 1) * 2048);
    SB
}
template <int K> __device__ __forceinline__ void zpack(Cross& c) {
#define ZP "v_add_f32 %1, %1, %2\n\tv_add_f32 %3, %3, %4\n\tv_cvt_pk_bf16_f32 %0, %1, %3"
    if constexpr (K == 2) asm volatile(ZP :: "v"(c.zq[2]), "v"(c.zs[K][0]), "v"(c.zs[K][1]), "v"(c.zs[K][2]), "v"(c.zs[K][3]));
    else {
        unsigned r;
        asm volatile(ZP : "=v"(r) : "v"(c.zs[K][0]), "v"(c.zs[K][1]), "v"(c.zs[K][2]), "v"(c.zs[K][3]));
        c.zq[K] = r;
    }
    SB
}
template <int K> __device__ __forceinline__ void zclear(Cross& c) { c.zs[K] = f32x4{0.f, 0.f, 0.f, 0.f}; PIN(c.zs[K]); SB }
#define NJP 3
template <int P> __device__ __forceinline__ void rdX(Cross& c) {
    constexpr int k = 1 + ((P - 1) & ~3), a = ((P - 1) & 3) * 32, b = (P & 3) * 32;
    constexpr bool jp = (P - 1) / 4 < NJP;
#define RDX "s_mov_b64 exec, %6\n\tds_read2st64_b64 %0, %4 offset0:%7 offset1:%8\n\tds_read2st64_b64 %1, %4 offset0:%9 offset1:%10\n\t" \
            "ds_read2st64_b64 %2, %5 offset0:%7 offset1:%8\n\tds_read2st64_b64 %3, %5 offset0:%9 offset1:%10\n\ts_mov_b64 exec, -1"
#define RDY "s_mov_b64 exec, %5\n\tds_read_b128 %0, %4 offset:%6\n\tds_read_b128 %1, %4 offset:%7\n\tds_read_b128 %2, %4 offset:%8\n\tds_read_b128 %3, %4 offset:%9\n\ts_mov_b64 exec, -1"
    if constexpr (P == 0) { const u32x2 lo = LDS(const u32x2, c.rx0[0]), hi = LDS(const u32x2, c.rx0[1]); c.X[0] = u32x4{lo[0], lo[1], hi[0], hi[1]}; }
    else if constexpr ((P & 3) == 2 && jp)
        asm volatile(RDY : "=&v"(c.X[k]), "=&v"(c.X[k + 1]), "=&v"(c.X[k + 2]), "=&v"(c.X[k + 3]) : "v"(c.rx[0]), "s"(c.xm[0]), "n"(a * 512), "n"(a * 512 + 2048), "n"(b * 512), "n"(b * 512 + 2048) : "memory");
    else if constexpr ((P & 3) == 0 && jp)
        asm volatile(RDY : "+v"(c.X[k]), "+v"(c.X[k + 1]), "+v"(c.X[k + 2]), "+v"(c.X[k + 3]) : "v"(c.rx[0]), "s"(c.xm[1]), "n"(a * 512), "n"(a * 512 + 2048), "n"(b * 512), "n"(b * 512 + 2048) : "memory");
    else if constexpr ((P & 3) == 2)
        asm volatile(RDX : "=&v"(c.X[k]), "=&v"(c.X[k + 1]), "=&v"(c.X[k + 2]), "=&v"(c.X[k + 3]) : "v"(c.rx[0]), "v"(c.rx[1]), "s"(c.xm[0]), "n"(a), "n"(b), "n"(a + 4), "n"(b + 4) : "memory");
    else if constexpr ((P & 3) == 0)
        asm volatile(RDX : "+v"(c.X[k]), "+v"(c.X[k + 1]), "+v"(c.X[k + 2]), "+v"(c.X[k + 3]) : "v"(c.rx[0]), "v"(c.rx[1]), "s"(c.xm[1]), "n"(a), "n"(b), "n"(a + 4), "n"(b + 4) : "memory");
    SB
}
template <int K, int N0, int N1> __device__ __forceinline__ void xpair(Cross& c) {
    if constexpr (N0 < N1 && K < NJP) {
        constexpr int m = N0 >> 2, d = N0 & 3, s = 1 + 4 * K + (d & 2), e = (m & 2) + (d & 1);
        unsigned r;
        asm volatile("v_perm_b32 %0, %1, %2, %3" ST : "=v"(r) : "v"(c.X[s + 1][e]), "v"(c.X[s][e]), "s"((m & 1) ? c.ph : c.pl));
        c.Y[4 * K + m][d] = r;
        xpair<K, N0 + 1, N1>(c);
    }
}
#define C11 1
#define G8 0x5421
#define G8P(n) { if constexpr ((G8 & 15) == (n)) GQ(10, 0) if constexpr (((G8 >> 4) & 15) == (n)) GQ(10, 1) if constexpr (((G8 >> 8) & 15) == (n)) GQ(10, 2) if constexpr (((G8 >> 12) & 15) == (n)) GQ(10, 3) }
#define FPOS 0x01504241
#define FFL 4
#define BP 1
#define FI1(k, n) if (((FPOS >> (8 * (k))) & 255) == (n)) { dma16sco<(k) * 1024>(c.lane16, c.wsf); SB }
#define FI(n) FI1(0, n) FI1(1, n) FI1(2, n) FI1(3, n) if ((n) == BP) { asm volatile("" ::: "memory"); SB __builtin_amdgcn_s_barrier(); SB asm volatile("" ::: "memory"); SB } if ((n) == FFL) { WAITCNT(0, 15); SB LDS(unsigned, c.fl) = c.fl; SB }
#define GQ(Q, J) { issueA<J>(c, c.ga[4 * (Q) - 12 + (J)]); if constexpr ((J) == 3) set_m0<(((Q) + 1) & 3) * SLOT>(c.ldsb); SB }
static_assert(FOFF[0] == SLOT, "");
__device__ __forceinline__ void csetup(Cross& c) {
            const unsigned r6 = c.lane16 * 4u + (unsigned)c.rho * 256u;
            const unsigned am = (c.lane16 >> 8) + (unsigned)c.rho;
            c.wf[0] = (r6 & 0x3c0u) | (((am << 4) ^ ((r6 >> 3) & 0x20u)) & 0x30u) | SLOT;
            c.wf[1] = c.wf[0] + OFF_B;
            const unsigned wap = OFF_SW2 + (c.lane16 & 0x3c0u) + ((c.lane16 >> 1) & 0x18u);
            c.wa[0] = tr_read(wap); c.wa[1] = tr_read(wap + 32);
            const unsigned r2 = (c.lane16 >> 3) & 30u, s1 = OFF_SW1 + min(r2, 24u), sg = s1 + (am & 3u) * 52u;
#pragma unroll
            for (int k = 0; k < 4; ++k) {
                const unsigned a = (k < 3 ? sg : s1) + k * 208;
                const unsigned v = LDS(const uint16_t, a) | ((unsigned)LDS(const uint16_t, a + 26) << 16);
                c.s1[k] = r2 < 26u ? v : 0u;
            }
            PIN(c.s1);
            PIN(c.wf[0]); PIN(c.wf[1]); PIN(c.wa[0]); PIN(c.wa[1]);
}
template <int P> struct CStep {
    static __device__ __forceinline__ void run(Cross& c) {
        TSP(P)
        if constexpr (P == 7) { STAMP(6) }
        SB
        if constexpr (P == 1) {
#pragma unroll
            for (int k = 0; k < 2; ++k) c.wr[k] = CAT8(tr_read(c.wad[1] + k * 1024), tr_read(c.wad[1] + k * 1024 + 128));
            SB
            cmm<0, 0>(c); rdE<1, 0>(c); set_m0<0>(c.ldsb); cmm<0, 1>(c); rdE<1, 1>(c);
            issueA<0>(c, c.ga[4]); rdE<1, 2>(c); issueA<1>(c, c.ga[5]); rdE<1, 3>(c);
#pragma unroll
            for (int k = 0; k < 2; ++k) c.wb[k] = CAT8(tr_read(c.wad[0] + k * 1024), tr_read(c.wad[0] + k * 1024 + 128));
            SB
            cmm<0, 2>(c); cmm<0, 3>(c); issueA<2>(c, c.ga[6]); cmm<0, 4>(c); cmm<0, 5>(c); issueA<3>(c, c.ga[7]);
            PIN(c.wb[0]); PIN(c.wb[1]);
        } else if constexpr (P <= 8) {
            cmm<P - 1, 2>(c); rdE<P, 0>(c);
            cmm<P - 1, 3>(c); rdE<P, 1>(c);
            cmm<P - 1, 4>(c); rdE<P, 2>(c);
            cmm<P - 1, 5>(c); rdE<P, 3>(c);
            cmm<P - 1, 0>(c);
            if constexpr (P < 8) set_m0<((P + 3) & 3) * SLOT>(c.ldsb);
            cmm<P - 1, 1>(c);
            rdX<P>(c);
        }
        constexpr bool xw = P && !(P & 1);
#define GA(J) if constexpr (P < 7 && P != 1) issueA<J>(c, c.ga[4 * P + (J)]); SB
        if constexpr (P == 4 || P == 7) zclear<(P + 1) / 4>(c);
        if constexpr (P == 7) {
#pragma unroll
            for (int r = 0; r < 4; ++r) { c.bp[r][3] = cvt_o(c.ew[0][r], c.ew[1][r]); c.bp[r][0] = cvt_o(c.ew[2][r], c.ew[3][r]); }
            c.k0 = -2.0f * 0.7978845608028654f * 1.4426950408889634f / 64.0f; c.k1 = c.k0 * (0.044715f / 4096.0f); c.k2 = -64.0f / 1.4426950408889634f;
            c.am = (c.wf[0] & 16u) ? ~0u : 0u;
            c.fl = (c.lane16 >> 2) + (unsigned)(OFF_FL + 2048 + c.c * 256);
            PIN(c.k0); PIN(c.k1); PIN(c.k2); PIN(c.am); PIN(c.fl);
            SB
        }
        if constexpr (P == 2 || P == 6) zpack<(P == 2 ? 3 : 0)>(c);
        if constexpr (P == 8) {
            TX(0) G8P(0)
            cmm<8, 2>(c); cmm<8, 3>(c); cmm<8, 4>(c); cmm<8, 5>(c); cmm<8, 0>(c); cmm<8, 1>(c);
            G8P(1) TX(1)
            WAITCNT(63, 0); SB
            xpair<1, 0, 8>(c); SB G8P(2) xpair<1, 8, 16>(c); SB G8P(3)
            zpack<1>(c); TX(2)
            tsw<8, 0, S8 / 2>(c); SB G8P(4) tsw<8, S8 / 2, S8>(c); SB G8P(5)
        } else if constexpr (P == 9) {
            GQ(11, 0) rdE<9, 0>(c); rdE<9, 1>(c); rdE<9, 2>(c); rdE<9, 3>(c); tsw<9, 0, 2>(c); SB GQ(11, 1) tsw<9, 2, 6>(c); SB GQ(11, 2) TX(0)
            cmm<9, 2>(c); cmm<9, 3>(c); cmm<9, 4>(c); GQ(11, 3) cmm<9, 5>(c); cmm<9, 0>(c); cmm<9, 1>(c); TX(1)
            GQ(12, 0) tsw<9, 6, 10>(c); SB GQ(12, 1) tsw<9, 10, 14>(c); SB GQ(12, 2) TX(2) tsw<9, 14, 24>(c); SB GQ(12, 3)
        } else if constexpr (P == 10) {
            { unsigned z_; asm volatile("" : "=v"(z_)); c.zq[2] = z_; SB }
            rdE<10, 0>(c); rdE<10, 1>(c); PIN(c.zq); SB rdE<10, 2>(c); rdE<10, 3>(c);
            tsw<10, 0, 24>(c); SB
#pragma unroll
            for (int r = 0; r < 4; ++r) c.bp[r][1] = cvt_o(c.ew[10][r], c.ew[11][r]);
            SB
            cmm<10, 2>(c); cmm<10, 3>(c); cmm<10, 4>(c); cmm<10, 5>(c); rdX<10>(c); cmm<10, 0>(c); cmm<10, 1>(c); TX(0)
            WAITCNT(4, 15); asm volatile("" ::: "memory"); SB TX(1)
            rdE<11, 0>(c); rdE<11, 1>(c); rdE<11, 2>(c); rdE<11, 3>(c);
            swn<2, 0, 0, 8>(c); SB
            WAITCNT(63, 0); SB TX(2)
            cmm<11, 0>(c); cmm<11, 1>(c);
            asm volatile("v_mfma_f32_16x16x32_bf16 %0, %2, %4, 0\n\tv_mfma_f32_16x16x32_bf16 %1, %2, %5, 0\n\tv_mfma_f32_16x16x32_bf16 %0, %3, %6, %0\n\tv_mfma_f32_16x16x32_bf16 %1, %3, %7, %1"
                         : "=&v"(c.ew[22]), "=&v"(c.ew[23]) : "v"(c.wb[0]), "v"(c.wb[1]), "v"(c.fe[1][0][0]), "v"(c.fe[1][0][1]), "v"(c.fe[1][1][0]), "v"(c.fe[1][1][1]));
            SB
        }
        else if constexpr (xw) { tsw<P, 0, 4>(c); WAITCNT(63, 0); GA(0) tsw<P, 4, 6>(c); GA(1) tsw<P, 6, 8>(c); GA(2) tsw<P, 8, 10>(c); GA(3) tsw<P, 10, 12>(c); }
        else { GA(0) GA(1) tsw<P, 0, 4>(c); GA(2) tsw<P, 4, 8>(c); GA(3) tsw<P, 8, 12>(c); }
        if constexpr (P == 4) { xpair<0, 0, 16>(c); SB }
        if constexpr (P == 3) csetup(c);
        SB
        TX(3)
        WAITCNT(P == 7 || P == 8 ? 4 : P < 10 ? 8 + (P == 0 && SMP == 2 ? 2 : 0) : 0, 0);
        TX(4)
        if constexpr (P == 10) { asm volatile("" ::: "memory"); SB rdE<12, 0>(c); rdE<12, 1>(c); rdE<12, 2>(c); rdE<12, 3>(c); FI(0x41) }
        else { BARRIER; CStep<P + 1>::run(c); }
    }
};

#define PFC 147
#define NP0 1
#define NPL "\n\tglobal_load_lds_dword %2, %0 offset:%6"
#define OB 0
#define PRI 1
#define OBC
#define OBD
#define PFO 100
#define WPRE 5
#define ALN 4096
extern "C" __global__ __launch_bounds__(512) __attribute__((aligned(ALN))) void k_main(const int* __restrict__ idx, const uint16_t* __restrict__ table,
        const char* __restrict__ w1a, unsigned long long w1b, const uint16_t* __restrict__ w_cross, unsigned* __restrict__ ws32, const uint16_t* __restrict__ se_w1, const uint16_t* __restrict__ w_bi,
        const uint16_t* __restrict__ se_w2_, const uint16_t* __restrict__ w_deep2_, __hip_bfloat16* __restrict__ out_) {
    SB
    asm volatile(".set .Lnext, 1\n\t.macro STUB1 n\n\t.if .Lnext == \\n\n.Lst\\n: s_cbranch_execz .Lback\n\t.endif\n\t.endm\n\t"
                 ".macro STUB\n\t.if . - k_main >= .Lnext * 4096\n\t"
                 "STUB1 1\n\tSTUB1 2\n\tSTUB1 3\n\tSTUB1 4\n\t.set .Lnext, .Lnext + 1\n\t.endif\n\t.endm\n\t"
                 ".macro PLACE m, k\n\t.rept ((\\k - (. - k_main)) & (\\m - 1)) / 4\n\ts_nop 0\n\t.endr\n\t.endm");
#define stamps ((unsigned long long*)((char*)ws32 + (128ull << 20)))
    int tid = threadIdx.x, bid = blockIdx.x;
    unsigned w64;
    unsigned long long pc;
    {
        unsigned t8, tf;
        asm volatile("s_and_b32 %1, %5, 24\n\tv_lshlrev_b32 %2, 3, %4\n\ts_getpc_b64 %0\n\ts_bfm_b32 m0, 2, 14\n\tv_lshl_add_u32 %3, %1, 9, %2\n\t"
                     "global_load_lds_dword %3, %0 offset:%7\n\tv_readfirstlane_b32 %1, %4" NPL
                     : "=&s"(pc), "=&s"(w64), "=&v"(t8), "=&v"(tf) : "v"(tid), "s"(bid), "n"(PFO), "n"((PFC - 128) * 128 + PFO) : "memory", "scc");
        static_assert(PFC >= 128 && PFC < 158, "");
        static_assert(3 * SLOT == 0xc000, "");
    }
    if (w64 < 256u) {
        unsigned va, t0, t1;
        asm volatile("s_and_b32 %1, %3, 7\n\ts_lshr_b32 %2, %3, 5\n\ts_lshl3_add_u32 %1, %1, %2\n\ts_mulk_i32 %1, %6\n\ts_mul_i32 %2, %4, 10\n\ts_add_u32 %1, %1, %2\n\t"
                     "s_lshl_b32 %2, %4, 5\n\tv_lshl_add_u32 %0, %5, 4, %1\n\ts_add_u32 m0, %2, %7"
                     : "=&v"(va), "=&s"(t0), "=&s"(t1) : "s"(bid), "s"(w64), "v"(tid), "n"(64 * NF * 4), "n"(OFF_IDX) : "memory", "scc");
        static_assert(16 * NF * 4 - 1024 == 640, "");
        if (PRI == 3) __builtin_amdgcn_s_setprio(1);
        SB
        asm volatile("global_load_lds_dwordx4 %0, %1\n\ts_mov_b32 exec_hi, 0xff\n\tglobal_load_lds_dwordx4 %0, %1 offset:1024\n\ts_mov_b32 exec_hi, -1" OBC
                     :: "v"(va), "s"(idx), "s"(table), "s"(w1a), "s"(w1b), "s"(w_cross), "s"(ws32), "s"(se_w1), "s"(w_bi) : "memory");
        SB
    }
    TP(tz3)
    asm volatile("" : "+s"(w64), "+s"(bid), "+v"(tid));
    __builtin_assume(tid >= 0 && tid < 512); __builtin_assume(bid >= 0 && bid < 256); __builtin_assume(w64 < 512u);
    const int lane = tid & 63;
    const int w = (int)(w64 >> 6);
    const int xcd = bid & 7, slot = bid >> 3;
    const int st = xcd * 8 + (slot >> 2), nq = slot & 3;
    const int s0 = st * 64;
    const int rho = (st + nq) & 3, cw = w & 3;
    const int l15 = lane & 15, g = lane >> 4;
#define PFN 1
    float gc0 = -2.0f * 0.7978845608028654f * 1.4426950408889634f, gc1 = gc0 * 0.044715f;
#define FIN_ST(c)
#define FIN_SA(c)
#define ALL
#define DA 0x7a0
#define PADE if (sizeof(c) == sizeof(Cross)) asm volatile("s_endpgm\n\tPLACE 4096, %0" :: "n"(DA) : "memory");
#define FINISH(c) STAMP(5) FIN_ST(c) FIN_SA(c) PADE __builtin_amdgcn_endpgm(); __builtin_unreachable();
    unsigned lane16 = (unsigned)lane * 16u;
    int deep = w >> 2;
    Deep d;
    d.ldsb = (unsigned)(cw * 4096);
#define PTS 2101376
#define PTW 5
#define TA 1
#define XS 0
#define XJ 0
#define DV 0
#define CV 0
#define PG 0
#define SCOUT asm volatile("s_mov_b64 exec, 0\n\ts_cmp_eq_u32 %0, 0\n\ts_cbranch_scc1 .Lst1\n\ts_cmp_eq_u32 %0, 1\n\ts_cbranch_scc1 .Lst2\n\t" \
                     "s_cmp_eq_u32 %0, 2\n\ts_cbranch_scc1 .Lst3\n\ts_branch .Lst4\n.Lback:\n\ts_mov_b64 exec, -1" :: "s"(cw) : "scc");
#define DB 24
#define PLB SB asm volatile("PLACE 32, %0" :: "n"(DB)); SB
#define DJ 1
#define KW 1
#define RB 0
    const int r0 = s0 + 16 * cw;
    unsigned soon;
    d.unit = xcd + 8 * ((st & 7) >> 1);
    unsigned srcA, srcB, ib0, ib[4], sm[3];
#define XSET { srcA = (unsigned)(((lane & 7) ^ g) * 16); srcB = srcA ^ 64u; \
        ib0 = (unsigned)(OFF_IDX + cw * 2048 + (lane >> 3) * 104); \
        _Pragma("unroll") for (int k = 0; k < 4; ++k) { ib[k] = ib0 + (unsigned)(((k + rho) & 3) * 8); PIN(ib[k]); } \
        PIN(srcA); PIN(srcB); PIN(ib0); \
        const int k = (l15 & 1) | ((l15 >> 2) & 2), r = 4 * cw + g; \
        unsigned u0 = (unsigned)(nq * 128) + ((lane16 >> 2) & 124u), u1 = (unsigned)min(cw * 64 + lane, 168) * 4u; \
        if (XS) { PIN(u0); PIN(u1); } \
        sm[0] = (unsigned)(cw * 2048 + (lane >> 1) * 128 + nq * 32 + (lane & 1) * 16); \
        sm[1] = cw == 3 ? u0 : u1; \
        sm[2] = (unsigned)((r < 13 ? r : 12) * (NF * 2) + k * 16 + (k == 3 ? 0 : ((l15 + 2 * rho) & 6) * 2)); \
        PIN(sm[0]); PIN(sm[1]); PIN(sm[2]); }
    typedef unsigned long long u64x2 __attribute__((ext_vector_type(2)));
    const u32x4 rw1 = __builtin_bit_cast(u32x4, u64x2{(unsigned long long)w1a, (unsigned long long)w1b});
    d.rw1 = rw1;
    typedef const __attribute__((address_space(4))) unsigned long long* kap;
    kap ka = (kap)__builtin_amdgcn_kernarg_segment_ptr();
    gp out;
    Cross x;
    x.c = cw; x.nq = nq; x.rho = rho; x.st = st; x.lane16 = lane16; x.ws = (char*)ws32; x.ldsb = d.ldsb;
    unsigned pm;
    const char* sp;
    u32x2 ia[NP], ic[NP];
    if (deep) {
        mkboff(d, lane, cw, nq);
        d.m1 = (unsigned)((nq ^ 1) | (st & 1) | ((d.unit + 32 - NF) >> 5)); PINS(d.m1);
        unsigned va = min(lane16, 80u) * 8u + (unsigned)((bid * 4 + cw) * 768), vb = __umul24(lane16, PTS / 16);
        unsigned long long ptm = w == PTW ? (1ull << PT) - 1 : 0ull;
        d.sB = 24 * 32768;
        PIN(va); PIN(vb); PINS(ptm); PINS(d.sB); PINS(d.ldsb);
        SB
#define TOUCH(t) asm volatile(OBD "buffer_load_dword %0, %2, 0 offen lds\n\ts_mov_b64 exec, %4\n\tglobal_load_lds_dword %1, %3\n\t" t "s_mov_b64 exec, -1" \
                 :: "v"(va), "v"(vb), "s"(rw1), "s"(table), "s"(ptm), "s"(d.ldsb), "n"(OFF_B), "s"(w_cross), "s"(ws32), "s"(idx) : "memory", "scc"); SB
#define B0(t) asm volatile("buffer_load_dwordx4 %0, %4, %5 offen lds\n\tbuffer_load_dwordx4 %1, %4, %5 offen offset:1024 lds\n\t" \
                 "buffer_load_dwordx4 %2, %4, %5 offen offset:2048 lds\n\tbuffer_load_dwordx4 %3, %4, %5 offen offset:3072 lds" t \
                 :: "v"(d.boff[0]), "v"(d.boff[1]), "v"(d.boff[2]), "v"(d.boff[3]), "s"(rw1), "s"(d.sB) : "memory"); SB
        if (d.m1 != 0u) { TOUCH("s_add_u32 m0, %5, %6\n\t") B0("") }
        PINS(d.m1);
        if (d.m1 == 0u) {
            unsigned ro[3];
            rawo(d, lane16, cw, ro); PIN(ro[0]); PIN(ro[1]); PIN(ro[2]);
            SB
            TOUCH("") raw(d, 0u, cw, w_cross, ws32, ro); B0(" ")
        }
        TP(tz1)
        PINS(d.m1);
#pragma unroll
        for (int k = 0; k < 4; ++k) { d.sG[k] = (unsigned)(((k + rho) & 3) * (2 * 32768)); PINS(d.sG[k]); }
        soon = min(d.m1, (unsigned)(nq ^ 3)); PINS(soon);
        SB
        if (TGE == 0) if (cw == 3) tags(tag_addr(lane, d.unit, nq, st, xcd), ws32, OFF_TAG, nq ? 0x7ffffff0000ull : 0x3ffffff0e00ull);
        if ((cw ^ 2) + nq == 0 && lane < 32) st16sc(lane16, (char*)ws32 + WS_SUM + st * 512, u32x4{0u, 0u, 0u, 0u});
        SB
        if (soon != 0u) { prepB<1>(d); snop(0); SB issueB<0>(d); issueB<1>(d); issueB<2>(d); issueB<3>(d); }
        SB
        if (DJ) asm volatile("s_branch .Ldeep" :: "s"(nq), "s"(cw), "s"(rho), "s"(st), "s"(xcd), "s"(r0), "s"(d.unit) : "memory");
        SB
    } else {
        Cross& c = x;
        WAITCNT(63, 0);
        SB
        if (PRI == 1) __builtin_amdgcn_s_setprio(1);
        PINS(ka);
        const char* se_w2 = (const char*)ka[8]; const char* wd2 = (const char*)ka[9]; out = (gp)ka[10];
        SB
        if (!XS) XSET
#pragma unroll
        for (int k = 0; k < 4; ++k) { c.tb[k] = (const char*)table - k * 1024; PINS(c.tb[k]); }
#define CSET { \
            const int G = 2 * g + rho; \
            const unsigned ea = (unsigned)(cw * 4096 + l15 * 128); \
            c.re[0] = ea + (unsigned)((g ^ (l15 >> 1)) * 16); c.re[1] = c.re[0] ^ 64u; \
            c.rx[0] = ea + (unsigned)(((2 * nq + (g >> 1)) ^ (l15 >> 1)) * 16); c.rx[1] = c.rx[0] + 8u; \
            c.rx0[0] = c.rx[0] + (unsigned)((rho & 1) * 2048 + (G & 2) * 4); c.rx0[1] = c.rx[0] + (unsigned)((~rho & 1) * 2048 + ((G + 1) & 2) * 4); \
            c.six = (l15 & 1) ? 0xddddeeeeu : 0xdddd4444u; \
            c.pl = 0x05040100u; c.ph = 0x07060302u; c.xm[0] = 0x0000ffff0000ffffull; c.xm[1] = 0xffff0000ffff0000ull; \
            PINS(c.pl); PINS(c.ph); PINS(c.xm[0]); PINS(c.xm[1]); \
_Pragma("unroll")             for (int k = 0; k < 4; ++k) { const unsigned o = (l15 >> 1) == 2 * k + (g >> 1) ? 0x3f803f80u : 0u; c.on[k] = u32x4{o, o, o, o}; PIN(c.on[k]); } \
            PIN(c.re[0]); PIN(c.re[1]); PIN(c.rx[0]); PIN(c.rx[1]); PIN(c.rx0[0]); PIN(c.rx0[1]); PIN(c.six); \
            zclear<3>(c); zclear<0>(c); \
        }
        if (CV & 1) CSET
        if (CV & 4) { SB SCOUT }
        sp = cw == 3 ? wd2 : (const char*)se_w1;
        if (KW) { SB PINS(sp); PINS(se_w2); PINS(out); }
        if (PRI == 2) __builtin_amdgcn_s_setprio(1);
        set_m0<0>(c.ldsb);
        SB
        TP(tz1)
        WAITCNT(0, 15);
        asm volatile("" ::: "memory");
        STAMP(1)
        TP(c.tq[0])
        ia[0] = LDS(const u32x2, ib0 + 96); ic[0] = LDS(const u32x2, ib0 + 96 + 832);
#pragma unroll
        for (int q = 1; q < 3; ++q) { ia[q] = LDS(const u32x2, ib[q - 1]); ic[q] = LDS(const u32x2, ib[q - 1] + 832); }
        SB
#define GATHER(q) issueA<0>(c, ia[q][0] * 128u + srcA); issueA<1>(c, ic[q][0] * 128u + srcB); issueA<2>(c, ia[q][1] * 128u + srcA); issueA<3>(c, ic[q][1] * 128u + srcB);
        GATHER(0)
        set_m0<SLOT>(c.ldsb);
        SB
        GATHER(1)
#define SM0 asm volatile("s_mov_b32 m0, %2\n\ts_mov_b32 exec_hi, 0\n\tglobal_load_lds_dwordx4 %0, %1\n\ts_mov_b32 exec_hi, -1" :: "v"(sm[0]), "s"(w_bi), "s"(OFF_WBI + cw * 512) : "memory");
#define SM1 dma4(sm[1], sp, OFF_SW1 + cw * 256); dma4(sm[2], se_w2, OFF_SW2 + cw * 256);
        SM0 SM1
        set_m0<2 * SLOT>(c.ldsb);
        TP(c.tq[1])
    }
    if (PRO & 4) PINS(d.m1);
    int crs = deep ^ 1;
    PINS(crs); PINS(deep);
    if (crs == 0) {
        Deep& c = d;
        int nq_ = nq, cw_ = cw, rho_ = rho, st_ = st, xcd_ = xcd, r0_ = r0;
        if (DJ) asm volatile(".Ldeep:" : "+s"(nq_), "+s"(cw_), "+s"(rho_), "+s"(st_), "+s"(xcd_), "+s"(r0_), "+s"(c.unit), "+s"(c.m1) :: "memory");
        SB
        WAITCNT(63, 0);
        TP(tz2)
        SB
        const int nq = nq_, cw = cw_, rho = rho_, st = st_, xcd = xcd_, r0 = r0_, w = cw + 4;
        __builtin_assume(nq >= 0 && nq < 4); __builtin_assume(cw >= 0 && cw < 4); __builtin_assume(rho >= 0 && rho < 4);
        int lane;
        asm volatile("v_mbcnt_lo_u32_b32 %0, -1, 0\n\tv_mbcnt_hi_u32_b32 %0, -1, %0" : "=v"(lane));
        const int l15 = lane & 15, g = lane >> 4, tid = w * 64 + lane;
        PINS(ka);
        const gp out = (gp)ka[10];
        c.c = cw; c.nq = nq; c.rho = rho; c.st = st; c.lane16 = (unsigned)lane * 16u; c.ws = (char*)ws32;
#define MV 0
#define B4 issueB<0>(c); issueB<1>(c); issueB<2>(c); issueB<3>(c);
#define B1L(v) if (v == 0u) { prepB<1>(c); snop(0); SB B4 }
#define EARLY if (MV != 3) { PINS(c.m1); produce<5>(c); } PINS(soon); \
        if (MV == 2) { PINS(c.m1); B1L(c.m1) } else if (MV == 3) { B1L(n3) } else { B1L(soon) } \
        if (MV != 2) prepB<2>(c); \
        TP(c.tq[0]) \
        BARRIER;            \
        TP(c.tq[1]) \
        if (MV == 2) { B1L(n3) prepB<2>(c); snop(0); SB } \
        B4
        unsigned n3 = (unsigned)(nq ^ 3);
        if (MV) { PINS(n3); mkboff(d, lane, cw, nq); SB }
        if (MV == 1 || MV == 2) { EARLY PINS(c.m1); SB }
        if (MV == 3) { PINS(c.m1); produce<5>(c); PINS(c.m1); B1L(c.m1) PINS(c.m1); SB }
        c.m3 = (unsigned)(cw ^ 3); c.m4 = c.m3 | min(c.m1, (unsigned)nq);
        c.tad = tag_addr(lane, c.unit, nq, st, xcd);
        PIN(c.tad);
        SB
        if (!MV) mkboff(d, lane, cw, nq);
        {
            const unsigned a0 = (unsigned)(cw * 4096 + (cw & 1) * 2048 + l15 * 128 + ((4 * (cw >> 1) + g) ^ (l15 >> 1)) * 16);
            const int q = l15 >> 2, p = l15 & 3;
            const unsigned b0 = (unsigned)(OFF_B + cw * 4096 + (8 * g + q) * 128 + ((p >> 1) ^ (q & 2) ^ ((g & 1) << 2)) * 16 + (p & 1) * 8);
#pragma unroll
            for (int k = 0; k < 4; ++k) { c.ra[k] = a0 ^ (unsigned)(k * 4096); c.rb[k] = b0 ^ (unsigned)(k * 32); PIN(c.ra[k]); PIN(c.rb[k]); }
#pragma unroll
            for (int k = 0; k < 16; ++k) { c.ac[k >> 2][k & 3] = f32x4{0.f, 0.f, 0.f, 0.f}; PIN(c.ac[k >> 2][k & 3]); }
        }
        unsigned exw[4], fl = (c.lane16 >> 2) + (unsigned)(OFF_FL + cw * 256), fr = c.lane16 + OFF_FL, s8 = c.lane16 >> 1;
        Fin fk; fin(fk);
#pragma unroll
        for (int k = 1; k < 4; ++k) { exw[k] = (c.lane16 >> 2) + (unsigned)(OFF_B + (cw ^ k) * 4096); PIN(exw[k]); }
        double big = BIG;
        f32x4 va = {0.f, 0.f, 0.f, 0.f};
        float gc2 = 1.0f / TRC;
        f32x2 gcp = {gc0, gc1}, gcq = {gc2, gc2};
        c.w2[0][0] = OFF_WD2 + g * 16; c.w2[1][0] = OFF_WD2 + 64 + g * 16; PIN(c.w2[0]); PIN(c.w2[1]);
        const unsigned dl = (unsigned)((l15 >> 2) ^ (2 * (g & 1))), on = (l15 & 1) ? 0x3f800000u : 0x3f80u;
        u32x4 unit = {dl == 0u ? on : 0u, dl == 0u ? on : 0u, dl == 1u ? on : 0u, dl == 1u ? on : 0u};
        unsigned uix = (l15 & 2) ? 0xddddeeeeu : 0xdddd4444u;
        u32x2 pr[4][4];
#pragma unroll
        for (int k = 0; k < 4; ++k) { pr[0][k] = u32x2{0u, 0u}; PIN(pr[0][k]); }
        PIN(uix);
        if (PFN) LDS(f32x4, fr + (unsigned)(cw * 1024)) = va;
        PIN(fl); PIN(fr); PIN(s8); PIN(big); PIN(gcp); PIN(gcq); gc0 = gcp[0]; gc1 = gcp[1]; gc2 = gcq[0]; PIN(va); PIN(unit);
        c.wsf[0] = c.ws + WS_FRAG + 16384 + nq * 65536 + cw * 4096; c.wsf[1] = c.wsf[0] + 32768;
        c.wsh[0] = c.ws + WS_FRAG + 32768 + nq * 65536 + cw * 2048; c.wsh[1] = c.wsh[0] + 8192; c.ldsh = (unsigned)(cw * 2048);
        PINS(c.wsh[0]); PINS(c.wsh[1]); PINS(c.wsf[0]); PINS(c.wsf[1]); PINS(c.ldsh);
        c.pmask = cw != 3 ? 0ull : (c.m1 == 0u ? 0x1ffull : 0x100ull);
        unsigned long long wmask = (cw ^ 3) + nq ? 0ull : 0xe00ull;
        unsigned m5 = c.m3 | c.m1;
        PINS(wmask); PINS(m5);
        constexpr unsigned xb1 = xbits(1), xb2 = xbits(2), xb3 = xbits(3), xb4 = xbits(4);
        c.xa = c.m1 == 0u ? (cw == 3 ? xb4 : xb3) : cw != 3 ? 0u : nq ? xb1 : xb2;
        c.one = 1u; PIN(c.one);
        PINS(c.pmask); PINS(c.m3); PINS(c.m4); PINS(c.xa);
        if (VT) { c.tb = c.m1 == 0u ? TBP : (TB >> (4 * nq)) & 15; PINS(c.tb); }
        gp sums, outp;
        unsigned tga;
#define DTL sums = (gp)c.ws + (unsigned)(WS_SUM + r0 * 8); outp = out + (unsigned)(r0 * 2); tga = OFF_TAG + (c.lane16 >> 2); PINS(sums); PINS(outp); PIN(tga);
        if (DV & 1) { DTL PLB }
        if (CV & 2) { SB SCOUT SB }
        if (MV == 0 || MV == 3) { EARLY }
        prepB<3>(c);
        TP(c.tq[2])
        if (!(DV & 1)) { DTL PLB }
        WAITX(8, 2, c.m1)
        c.tag = LDSV(tga);
        if (!(DV & 2)) PIN(c.tag);
        TP(c.tq[3])
        BARRIER;
        TP(c.tq[4])
        SB
        DStep<0>::run(c);
        TS(c.ts[13])
        u32x2 px[4][4];
#define BX 0
#define EX2(e, k) if (BX & 1) { px[e][k][0] = cvt_o(c.ac[e][k][0], c.ac[e][k][1]); px[e][k][1] = cvt_o(c.ac[e][k][2], c.ac[e][k][3]); SB } else { px[e][k][0] = pk_bf16(c.ac[e][k][0], c.ac[e][k][1]); px[e][k][1] = pk_bf16(c.ac[e][k][2], c.ac[e][k][3]); USE(px[e][k][0]); USE(px[e][k][1]); SB }
#define EXO(e, k) (((e) - 1) * 1024 + ((k) & 1) * 512)
#define WT1(e, k, d) { asm volatile("ds_write_addtid_b32 %0 offset:%1" :: "v"(px[e][k][d]), "n"(((k) < 2 ? 62464 : 32768) + EXO(e, k) + (d) * 256) : "memory"); SB }
#define WT(e, k) { asm volatile("ds_write_addtid_b32 %0 offset:%2\n\tds_write_addtid_b32 %1 offset:%3" ST :: "v"(px[e][k][0]), "v"(px[e][k][1]), \
            "n"(((k) < 2 ? 62464 : 32768) + EXO(e, k)), "n"(((k) < 2 ? 62464 : 32768) + EXO(e, k) + 256) : "memory"); SB }
#define W6(k) WT(3, k) WT(2, k) WT(1, k)
#define RD(h_) { constexpr int h = (h_) & 1; if (!(h_)) f = LDSV(fr + 1024); asm volatile("" ::: "memory"); SB \
            _Pragma("unroll") for (int k = 2 * (h); k < 2 * (h) + 2; ++k) _Pragma("unroll") for (int e = 3; e > 0; --e) _Pragma("unroll") for (int d = 0; d < 2; ++d) \
                pr[e][k][d] = LDS(const unsigned, exw[e] + ((h) ? 0 : 3 * SLOT) + EXO(e, k) + d * 256); SB }
#define CK(h) { if (__builtin_expect(__builtin_amdgcn_ballot_w64(f == 0u) != 0ull, 0)) { unsigned& slow = c.slow; (void)slow; SLOW(3) \
            _Pragma("unroll 1") for (int it = 0; it < (1 << 20); ++it) { RD(0) RD(1) if (__builtin_amdgcn_ballot_w64(f == 0u) == 0ull) break; } } SB }
#define AD(k) asm volatile("v_smfmac_f32_16x16x64_bf16 %0, %1, %2, %3" ST : "+v"(c.ac[0][k]) : "v"(unit), \
            "v"(u32x8{pr[0][k][0], pr[0][k][1], pr[1][k][0], pr[1][k][1], pr[2][k][0], pr[2][k][1], pr[3][k][0], pr[3][k][1]}), "v"(uix));
#define G4(m, k, ins) asm volatile(ins : m(y[4 * k]), m(y[4 * k + 1]), m(y[4 * k + 2]), m(y[4 * k + 3]) \
            : "v"(c.ac[0][k][0]), "v"(c.ac[0][k][1]), "v"(c.ac[0][k][2]), "v"(c.ac[0][k][3]), "v"(gc0), "v"(gc1), "v"(gc2));
#define GA_ "v_mul_f32 %0, %4, %4\n\tv_mul_f32 %1, %5, %5\n\tv_mul_f32 %2, %6, %6\n\tv_mul_f32 %3, %7, %7\n\t" \
            "v_fma_f32 %0, %0, %9, %8\n\tv_fma_f32 %1, %1, %9, %8\n\tv_fma_f32 %2, %2, %9, %8\n\tv_fma_f32 %3, %3, %9, %8\n\t" ST_
#define GM_ "v_mul_f32 %0, %0, %4\n\tv_mul_f32 %1, %1, %5\n\tv_mul_f32 %2, %2, %6\n\tv_mul_f32 %3, %3, %7\n\t" ST_
#define GB_ GM_ "v_exp_f32 %0, %0\n\tv_exp_f32 %1, %1\n\tv_exp_f32 %2, %2\n\tv_exp_f32 %3, %3\n\t"
#define GC_ ST_ "v_fma_f32 %0, %0, %10, %10\n\tv_fma_f32 %1, %1, %10, %10\n\tv_fma_f32 %2, %2, %10, %10\n\tv_fma_f32 %3, %3, %10, %10\n\t" ST_ \
            "v_rcp_f32 %0, %0\n\tv_rcp_f32 %1, %1\n\tv_rcp_f32 %2, %2\n\tv_rcp_f32 %3, %3\n\t" GM_
#define GELU(k) { G4("=&v", k, GA_ GB_ GC_) }
#define GEL1(k, I0) { G4("=&v", k, GA_) I0 G4("+v", k, GB_ GC_) }
#define GEL2(k, I1, I2) { if (k < 2 || PU2 == 2 * k + 6) { G4("=&v", k, GA_ GB_) I1 G4("+v", k, GC_) } else GELU(k) }
#define WDOT(q) asm volatile("v_smfmac_f32_16x16x64_bf16 %0, %1, %2, %3 abid:1" :: "v"(va), "v"(c.w2[q]), \
            "v"(f32x8{y[8 * q], y[8 * q + 1], y[8 * q + 2], y[8 * q + 3], y[8 * q + 4], y[8 * q + 5], y[8 * q + 6], y[8 * q + 7]}), "v"(uix));
#define PROTO { watch = __builtin_amdgcn_ballot_w64(tw == c.tag) & wmask; \
            if (!PML) PROT2 \
            PINS(watch); } SB
#define PML 1
#define PROT2 if (__builtin_expect(m5 == 0u, 0)) { \
                if (PML) asm volatile("s_mov_b64 exec, -1" ::: "memory"); \
                const u32x4 e4 = LDS(const u32x4, fr + (OFF_NB - OFF_FL)), l4 = LDS(const u32x4, fr + (OFF_NB + 1024 - OFF_FL)); \
                watch |= __builtin_amdgcn_ballot_w64(e4[0] == l4[0]) | __builtin_amdgcn_ballot_w64(e4[1] == l4[1]) | __builtin_amdgcn_ballot_w64(e4[2] == l4[2]) | __builtin_amdgcn_ballot_w64(e4[3] == l4[3]); \
                if (PML) asm volatile("s_mov_b64 exec, 0xffff" : "+s"(watch) :: "memory"); \
            }
#define FLA { LDS(unsigned, fl + 1024) = fl; SB }
#define FLB { LDS(unsigned, fl + 3072) = fl; SB }
        unsigned f = 0;
        float y[16];
        unsigned long long watch = 0ull;
#define FW 4
#define FLP 1
#define WSP 1
#define PD 3
#define PR(n) { if ((n) == PU) __builtin_amdgcn_s_setprio(2); if ((n) == PD || (n) == PD2) __builtin_amdgcn_s_setprio(0); if ((n) == PU2) __builtin_amdgcn_s_setprio(2); SB }
#define PU2 13
#define PD2 -1
#define FH(n) if ((n) == FW) { WAITCNT(0, 15); SB set_m0<OFF_B + 3 * SLOT - 62464>(c.ldsb); LDS(unsigned, fl + 3072) = fl; SB }
#define FS(n) if ((n) == FLP) { WAITCNT(0, 15); SB LDS(unsigned, fl) = fl; SB publish(c, c.pmask); SB TE(c.tq[3]) }
#define PP 4
#define PQ(n) if ((n) == PP) { PROTO }
        PR(0) FH(0)
        TE(c.tq[0])
        mk<12, 3, 0>(c); mk<12, 2, 0>(c); mk<12, 1, 0>(c); FH(1) mk<12, 3, 1>(c); mk<12, 2, 1>(c); mk<12, 1, 1>(c); FH(2)
        mk<12, 3, 2>(c); mk<12, 2, 2>(c); mk<12, 1, 2>(c); FH(3) EX2(3, 0) mk<12, 3, 3>(c); EX2(2, 0) mk<12, 2, 3>(c); EX2(1, 0) mk<12, 1, 3>(c); FH(4) PR(1)
#define OWN 4
#define OW(k) if ((k) < OWN) mk<12, 0, k>(c);
#define OX(k) if ((k) >= OWN) mk<12, 0, k>(c);
        WT(3, 0) EX2(3, 1) OW(0) WT(2, 0) EX2(2, 1) WT(1, 0) EX2(1, 1) OW(1) WT(3, 1) EX2(3, 2) WT(2, 1) EX2(2, 2) OW(2) WT(1, 1) set_m0<OFF_B - 32768>(c.ldsb); EX2(1, 2) WT(3, 2) EX2(3, 3) OW(3) WT(2, 2) EX2(2, 3) WT(1, 2) EX2(1, 3)
        W6(3) set_m0<FOFF[2] + 8192>(c.ldsh); SB FLA
        TE(c.tq[1])
        static_assert(NE == 6 && WSP == 1, "");
        PR(2) dqs<14, NE, 8>(c);
        const unsigned tw = LDSV(fl + (OFF_TAG + 512 - OFF_FL - 768));
        SB
#define CKL 2
#define FRP 12
        dqs<14, 8, FRP>(c);
        if (CKL == 3) asm volatile("ds_read_b32 %0, %1 offset:1024" : "=v"(f) : "v"(fr) : "memory");
        dqs<14, FRP, 12>(c); PR(3)
        if (DEF11) m4<11, 0>(c);
        OX(0) OX(1) OX(2) OX(3)
        PQ(2) PR(9)
#define R2(n, a, o) "ds_read2st64_b32 %" #n ", %" #a " offset0:" #o " offset1:" #o "+1\n\t"
#define R3(n0, n1, n2, o) R2(n0, 16, o+8) R2(n1, 15, o+4) R2(n2, 14, o) ST_
        if (CKL == 1) {
            asm volatile("1:\n\tds_read_b32 %0, %13 offset:1024\n\t" R3(1, 2, 3, 192) R3(4, 5, 6, 194) R3(7, 8, 9, 0) R3(10, 11, 12, 2)
                         "s_waitcnt lgkmcnt(12)\n\tv_cmp_eq_u32 vcc, 0, %0\n\ts_cbranch_vccnz 1b"
                         : "=&v"(f), "=&v"(pr[3][0]), "=&v"(pr[2][0]), "=&v"(pr[1][0]), "=&v"(pr[3][1]), "=&v"(pr[2][1]), "=&v"(pr[1][1]),
                           "=&v"(pr[3][2]), "=&v"(pr[2][2]), "=&v"(pr[1][2]), "=&v"(pr[3][3]), "=&v"(pr[2][3]), "=&v"(pr[1][3])
                         : "v"(fr), "v"(exw[1]), "v"(exw[2]), "v"(exw[3]) : "memory", "vcc");
            PR(4)
        }
        else if (CKL == 3) {
            asm volatile("1:\n\ts_waitcnt lgkmcnt(0)\n\tv_cmp_eq_u32 vcc, 0, %0\n\tds_read_b32 %0, %13 offset:1024\n\ts_cbranch_vccnz 1b\n\t"
                         R3(1, 2, 3, 192) R3(4, 5, 6, 194) R3(7, 8, 9, 0) R3(10, 11, 12, 2)
                         : "+&v"(f), "=&v"(pr[3][0]), "=&v"(pr[2][0]), "=&v"(pr[1][0]), "=&v"(pr[3][1]), "=&v"(pr[2][1]), "=&v"(pr[1][1]),
                           "=&v"(pr[3][2]), "=&v"(pr[2][2]), "=&v"(pr[1][2]), "=&v"(pr[3][3]), "=&v"(pr[2][3]), "=&v"(pr[1][3])
                         : "v"(fr), "v"(exw[1]), "v"(exw[2]), "v"(exw[3]) : "memory", "vcc");
            PR(4) PQ(3)
        }
        else if (CKL == 2) { while (__builtin_amdgcn_ballot_w64(LDSV(fr + 1024) == 0u) != 0ull) { } RD(2) RD(1) PR(4) }
        else { RD(0) RD(1) PR(4) CK(0) }
        TE(c.tq[2])
#define AW(n) if (CKL & 1) { SB WAITCNT(63, n); SB }
#define ADN 0
        AW(9) AD(0) USE(f); AW(6) AD(1) AW(3) AD(2) AW(0) AD(3) snop(ADN); SB
        PR(5) PQ(4) FS(0)
        if (FLP == 1) GEL2(0, FS(1), ) else GELU(0)
        FS(2) PR(6) PQ(5)
        if (FLP == 3) GEL2(1, FS(3) PR(7), ) else GELU(1)
        FS(4) PR(8)
#define WDP 1
#define GP 0
#define PK2(op, a, b, m) op " %0, %0, " a m "\n\t" op " %1, %1, " b m "\n\t"
#define T4(op) op " %2, %2\n\t" op " %3, %3\n\t" op " %4, %4\n\t" op " %5, %5\n\t"
#define GPK(k, I) { f32x2 p0, p1; asm volatile("" : "=v"(p0), "=v"(p1)); I \
            asm volatile("v_pk_mul_f32 %0, %6, %6\n\tv_pk_mul_f32 %1, %7, %7\n\t" PK2("v_pk_fma_f32", "%8, %8", "%8, %8", " op_sel:[0,1,0] op_sel_hi:[1,1,0]") PK2("v_pk_mul_f32", "%6", "%7", "") \
                T4("v_exp_f32") ST_ PK2("v_pk_fma_f32", "%9, %9", "%9, %9", " op_sel_hi:[1,0,0]") T4("v_rcp_f32") ST_ PK2("v_pk_mul_f32", "%6", "%7", "") \
                :: "v"(p0), "v"(p1), "v"(p0[0]), "v"(p0[1]), "v"(p1[0]), "v"(p1[1]), "v"(f32x2{c.ac[0][k][0], c.ac[0][k][1]}), "v"(f32x2{c.ac[0][k][2], c.ac[0][k][3]}), "v"(gcp), "v"(gcq)); \
            y[4 * k] = p0[0]; y[4 * k + 1] = p0[1]; y[4 * k + 2] = p1[0]; y[4 * k + 3] = p1[1]; }
        if (GP & 4) GPK(2, ) else GEL2(2, PR(10), )
        PR(11)
        if (GP & 8) GPK(3, ) else GEL2(3, PR(12), )
        PR(13)
        if (GP & 8) { snop(0); SB }
        WDOT(0)
        WDOT(1)
        asm volatile("s_mov_b64 exec, 0xffff" : "+v"(va));
        snop(6);
        SB
        TE(c.tq[4])
        const float v = va[0];
        TS(c.ts[14])
        deliver(c, v, big, 2 * nq, s8, fk, sums, outp, [&] { if (PML) { PROT2 PINS(watch); } });
        ALL
        if (__builtin_expect(watch != 0ull, 0)) {
            asm volatile("s_mov_b64 exec, -1" ::: "memory");
            unsigned& slow = c.slow; (void)slow; SLOW(5)
            const u32x4 e4 = LDS(const u32x4, OFF_NB + c.lane16), l4 = LDS(const u32x4, OFF_NB + 1024 + c.lane16);
            bool opn[4];
#pragma unroll
            for (int k = 0; k < 4; ++k) opn[k] = c.m1 == 0u && e4[k] == l4[k];
            bool own = nq == 0 && lane >= 9 && lane < 12 && LDSV(OFF_TAG + 512 + lane * 4) == c.tag;
#pragma unroll 1
            for (int it = 0; it < (1 << 22) && __builtin_amdgcn_ballot_w64(opn[0] || opn[1] || opn[2] || opn[3] || own) != 0ull; ++it) {
                bool chg = false;
#pragma unroll
                for (int k = 0; k < 4; ++k)
                    if (opn[k] && __hip_atomic_load(ws32 + lane * 4 + k, __ATOMIC_RELAXED, __HIP_MEMORY_SCOPE_AGENT) != e4[k]) { opn[k] = false; chg = true; }
                if (own && __hip_atomic_load(ws32 + (c.tad >> 2), __ATOMIC_RELAXED, __HIP_MEMORY_SCOPE_AGENT) != c.tag) { own = false; chg = true; }
                if (__builtin_amdgcn_ballot_w64(chg) != 0ull) publish(c, c.pmask);
                else __builtin_amdgcn_s_sleep(2);
            }
        }
        FINISH(c)
    } else {
        Cross& c = x;
#pragma unroll
        for (int q = 3; q < NP; ++q) { ia[q] = LDS(const u32x2, ib[(q - 1) & 3] + ((q - 1) >> 2) * 32); ic[q] = LDS(const u32x2, ib[(q - 1) & 3] + ((q - 1) >> 2) * 32 + 832); }
#pragma unroll
        for (int q = 3; q < NP; ++q) {
            c.ga[4 * q - 12] = ia[q][0] * 128u + srcA; c.ga[4 * q - 11] = ic[q][0] * 128u + srcB;
            c.ga[4 * q - 10] = ia[q][1] * 128u + srcA; c.ga[4 * q - 9] = ic[q][1] * 128u + srcB;
        }
#pragma unroll
        for (int k = 0; k < 40; ++k) PIN(c.ga[k]);
        if (!(CV & 1)) CSET
        SB
        if (!(CV & 6)) SCOUT
        unsigned g2[4];
#define G2(cmp) asm volatile(cmp " %8, 0\n\ts_cbranch_scc1 1f\n\tglobal_load_lds_dwordx4 %0, %4\n\tglobal_load_lds_dwordx4 %1, %5 offset:1024\n\t" \
            "global_load_lds_dwordx4 %2, %6 offset:2048\n\tglobal_load_lds_dwordx4 %3, %7 offset:3072\n1:" :: "v"(g2[0]), "v"(g2[1]), "v"(g2[2]), "v"(g2[3]), \
            "s"(c.tb[0]), "s"(c.tb[1]), "s"(c.tb[2]), "s"(c.tb[3]), "s"(pm) : "memory", "scc");
        if (PG) {
            pm = (unsigned)((nq ^ 1) | (st & 1) | ((xcd + 8 * ((st & 7) >> 1) + 32 - NF) >> 5));
            if (PG == 2) pm = 1u;
            if (PG == 3) pm = min(pm, (unsigned)(nq ^ 3));
            if (PG == 4) pm = 0u;
            g2[0] = ia[2][0] * 128u + srcA; g2[1] = ic[2][0] * 128u + srcB; g2[2] = ia[2][1] * 128u + srcA; g2[3] = ic[2][1] * 128u + srcB;
            G2("s_cmp_lg_u32") SB
        }
        static_assert(SMP == 0, "");
        BARRIER;
        TP(tz2)
        SB
        if (PG) { G2("s_cmp_eq_u32") } else { GATHER(2) }
        SB
        static_assert(SMP != 1 && SMP != 2, "");
        TP(c.tq[2])
        {
            const int q = (lane >> 2) & 3, p = lane & 3;
            c.wad[0] = (unsigned)(OFF_WBI + (8 * g + q) * 32 + p * 8); c.wad[1] = (unsigned)(OFF_WBI + (8 * g + q) * 32 + ((p + rho) & 3) * 8);
            PIN(c.wad[0]); PIN(c.wad[1]);
        }
        gp sums = (gp)c.ws + (unsigned)(WS_SUM + (s0 + 16 * cw) * 8), outp = out + (unsigned)((s0 + 16 * cw) * 2);
        PINS(sums); PINS(outp);
        c.wsf = c.ws + WS_FRAG + nq * 65536 + cw * 4096; PINS(c.wsf);
        unsigned long long act = 0xf0000f0000f0000full, m1 = 0xaaaaaaaaaaaaaaaaull, m2 = 0xccccccccccccccccull;
        double bigs = BIG;
        PINS(act); PINS(m1); PINS(m2); PINS(bigs);
        Fin fk; fin(fk);
        TP(c.tq[3])
        WAITCNT(11, 0); BARRIER;
        TP(c.tq[4])
        if (!PRI) __builtin_amdgcn_s_setprio(1);
        CStep<0>::run(c);
        TS(c.ts[11])
        u32x4 bq[4];
        u32x4 (&bp)[4] = c.bp;
        f32x2 T[6][8];
#define SPLIT(s0, s1, i0, i1) { _Pragma("unroll") for (int s = s0; s < s1; ++s) _Pragma("unroll") for (int i_ = i0; i_ < i1; ++i_) { \
            const f32x4 x = c.ew[2 * (1 + 4 * (s >> 1) + (i_ >> 1)) + (s & 1)]; T[s][i_] = f32x2{x[2 * (i_ & 1)], x[2 * (i_ & 1) + 1]}; } }
#define CB(B, i, h, k) { B[(2 * (i) + (h)) & 3][k] = cvt_o(T[2 * (k)][i][h], T[2 * (k) + 1][i][h]); }
#define SC(i, k) { asm volatile("v_pk_mul_f32 %0, %1, %0 op_sel_hi:[0,1]" : "+v"(T[2 * (k)][i]) : "v"(ao[k])); asm volatile("v_pk_mul_f32 %0, %1, %0 op_sel:[1,0]" : "+v"(T[2 * (k) + 1][i]) : "v"(ao[k])); }
        SPLIT(0, 4, 0, 8)
        SB
#define FLZ 8
#define SW3 6
#define SWA 8
#define FL1 3072
#define FLR 25
#define FLW 27
        STAMP(2)
        FI(0)
#define HO 1
#define M45 asm volatile("v_mfma_f32_16x16x32_bf16 %0, %2, %3, %0\n\tv_mfma_f32_16x16x32_bf16 %1, %2, %4, %1" :: "v"(c.ew[24]), "v"(c.ew[25]), "v"(c.wb[1]), "v"(c.fe[0][1][0]), "v"(c.fe[0][1][1]));
        cmm<12, 0>(c); cmm<12, 2>(c); cmm<12, 3>(c); cmm<12, 1>(c); M45
        rdX<12>(c);
        zpack<2>(c);
        FI(0x42)
        swl<8, 9>(c);
        const f32x4 hp = mf0(c.s1, c.zq);
        TS(c.ts[12])
        FI(0x50) TH(0)
        f32x2 hb;
        float hq[2];
        asm volatile("" : "=v"(hb), "=v"(hq[0]), "=v"(hq[1]));
        if (HO == 0) snop(5);
        SB
        swl<8 + HO, 8 + SWA>(c);
        FI(1) TH(1)
        asm volatile("v_mul_f32 %0, %4, %4\n\tv_mul_f32 %1, %5, %5\n\tv_mul_f32 %2, %6, %6\n\tv_mul_f32 %3, %7, %7\n\t"
                     "v_fma_f32 %0, %0, %9, %8\n\tv_fma_f32 %1, %1, %9, %8\n\tv_fma_f32 %2, %2, %9, %8\n\tv_fma_f32 %3, %3, %9, %8\n\t"
                     "v_mul_f32 %0, %0, %4\n\tv_mul_f32 %1, %1, %5\n\tv_mul_f32 %2, %2, %6\n\tv_mul_f32 %3, %3, %7\n\t"
                     "v_exp_f32 %0, %0\n\tv_exp_f32 %1, %1\n\tv_exp_f32 %2, %2\n\tv_exp_f32 %3, %3\n\t"
                     "v_fma_f32 %0, %0, %10, %10\n\tv_fma_f32 %1, %1, %10, %10\n\tv_fma_f32 %2, %2, %10, %10\n\tv_fma_f32 %3, %3, %10, %10\n\t"
                     "v_rcp_f32 %0, %0\n\tv_rcp_f32 %1, %1\n\tv_rcp_f32 %2, %2\n\tv_rcp_f32 %3, %3\n\t"
                     "v_mul_f32 %0, %0, %4\n\tv_mul_f32 %1, %1, %5\n\tv_mul_f32 %2, %2, %6\n\tv_mul_f32 %3, %3, %7\n\t"
                     "v_cvt_pk_bf16_f32 %0, %0, %1\n\tv_cvt_pk_bf16_f32 %1, %2, %3"
                     :: "v"(hb[0]), "v"(hb[1]), "v"(hq[0]), "v"(hq[1]), "v"(hp[0]), "v"(hp[1]), "v"(hp[2]), "v"(hp[3]), "v"(c.k0), "v"(c.k1), "v"(c.k2));
        FI(2) TH(2)
        swl<8 + SWA, 10 + SWA>(c);
        f32x4 av[2];
        asm volatile("v_mfma_f32_16x16x16_bf16 %0, %1, %2, 0" : "=&v"(av[0]) : "v"(c.wa[0]), "v"(hb));
        asm volatile("v_mfma_f32_16x16x16_bf16 %0, %1, %2, 0" : "=&v"(av[1]) : "v"(c.wa[1]), "v"(hb));
        FI(3)
        swl<10 + SWA, 18 + SWA>(c);
        TH(3) FI(4)
        TS(c.ts[13])
        SB
        float as[8];
        asm volatile("v_exp_f32 %0, %8\n\tv_exp_f32 %1, %9\n\tv_exp_f32 %2, %10\n\tv_exp_f32 %3, %11\n\tv_exp_f32 %4, %12\n\tv_exp_f32 %5, %13\n\tv_exp_f32 %6, %14\n\tv_exp_f32 %7, %15\n\t"
                     "v_add_f32 %0, 1.0, %0\n\tv_add_f32 %1, 1.0, %1\n\tv_add_f32 %2, 1.0, %2\n\tv_add_f32 %3, 1.0, %3\n\tv_add_f32 %4, 1.0, %4\n\tv_add_f32 %5, 1.0, %5\n\tv_add_f32 %6, 1.0, %6\n\tv_add_f32 %7, 1.0, %7\n\t"
                     "v_rcp_f32 %0, %0\n\tv_rcp_f32 %1, %1\n\tv_rcp_f32 %2, %2\n\tv_rcp_f32 %3, %3\n\tv_rcp_f32 %4, %4\n\tv_rcp_f32 %5, %5\n\tv_rcp_f32 %6, %6\n\tv_rcp_f32 %7, %7"
                     : "=&v"(as[0]), "=&v"(as[1]), "=&v"(as[2]), "=&v"(as[3]), "=&v"(as[4]), "=&v"(as[5]), "=&v"(as[6]), "=&v"(as[7])
                     : "v"(av[0][0]), "v"(av[0][1]), "v"(av[0][2]), "v"(av[0][3]), "v"(av[1][0]), "v"(av[1][1]), "v"(av[1][2]), "v"(av[1][3]));
        FI(5) TH(4)
        unsigned flg = LDSV(c.wf[1] + (OFF_FL + 2048 - OFF_B - SLOT));
        SB
        swl<18 + SWA, 18 + SWA + SW3>(c);
        FI(6)
        TG(c.tq[0]) TF(0)
        SB
        WAITCNT(63, 0);
        SB
        wait_set<0>(flg, c.wf[1] + (OFF_FL + 2048 - OFF_B - SLOT), c.slow);
        SB TF(1)
#define WSEQ(n) LDS(const u32x4, c.wf[((n) >> 4) & 1] + ((n) >> 5) * SLOT + (((n) & 15) ^ ((~(n) >> 2) & 1)) * 1024)
        u32x4 wq[64];
#pragma unroll
        for (int r = 0; r < WPRE; ++r) wq[r] = WSEQ(r);
        SB TF(2)
        swl<18 + SWA + SW3, 32>(c);
        SPLIT(4, 6, 0, 8)
        CB(bp, 0, 0, 2) CB(bp, 0, 1, 2)
        f32x2 ao[4] = {{as[0], as[1]}, {as[2], as[3]}, {as[4], as[5]}, {as[6], as[7]}};
        {
            f32x2 m[4] = {{c.ew[0][0], c.ew[0][1]}, {c.ew[1][0], c.ew[1][1]}, {c.ew[0][2], c.ew[0][3]}, {c.ew[1][2], c.ew[1][3]}};
#pragma unroll
            for (int r = 0; r < 4; r += 2) {
                asm volatile("v_pk_mul_f32 %0, %1, %0 op_sel_hi:[0,1]" : "+v"(m[r]) : "v"(ao[3]));
                asm volatile("v_pk_mul_f32 %0, %1, %0 op_sel:[1,0]" : "+v"(m[r + 1]) : "v"(ao[3]));
            }
            SC(0, 0) SC(0, 1) SC(0, 2) TF(3)
#pragma unroll
            for (int r = 0; r < 4; ++r) bq[r][3] = cvt_o(m[r & 2][r & 1], m[(r & 2) + 1][r & 1]);
        }
        ao[3][0] = sel(c.am, as[7], as[6]);
        PIN(ao[3]);
        SB
        FI(8)
        TG(c.tq[1]) TF(4)
        f32x4 u[2][4];
        float S1[7][16];
#define SQ(n) (((n) >> 1) + 4 * ((n) & 1))
#define FM(n, h, k) if ((k) < NJP) { asm volatile("v_pk_fma_f32 %0, %1, %2, %3" ST : "+v"(T[2 * (k) + (h)][SQ(n)]) : "v"(f32x2{u[h][2 + ((k) >> 1)][2 * ((k) & 1)], u[h][2 + ((k) >> 1)][2 * ((k) & 1) + 1]}), "v"(ao[k]), "v"(f32x2{u[h][(k) >> 1][2 * ((k) & 1)], u[h][(k) >> 1][2 * ((k) & 1) + 1]})); }
#define F1(n, h, s) if ((s) >= 2 * NJP) { float r_; \
            if ((s) < 6) { r_ = T[(s) % 6][SQ(n)][h]; asm volatile("v_fma_f32 %0, %1, %2, %3" : "+v"(r_) : "v"(u[h][2 + ((s) >> 2)][(s) & 3]), "v"(ao[(s) >> 1][(s) & 1]), "v"(u[h][(s) >> 2][(s) & 3])); } \
            else { asm volatile("v_fma_f32 %0, %1, %2, %3" : "=v"(r_) : "v"(u[h][3][2]), "v"(ao[3][0]), "v"(u[h][1][2]), "v"(u[h][3][3]), "v"(u[h][1][3])); } S1[s][2 * SQ(n) + (h)] = r_; }
#define FS1(h, k, d) { asm volatile("v_fma_f32 %0, %1, %2, %3" :: "v"(T[2 * (k) + (h)][7][d]), "v"(u[h][2 + ((k) >> 1)][2 * ((k) & 1) + (d)]), "v"(ao[k][d]), "v"(u[h][(k) >> 1][2 * ((k) & 1) + (d)])); }
#define LI 1
#define TT8(s, q) f32x8{T[s][2 * (q)][0], T[s][2 * (q)][1], T[s][2 * (q) + 1][0], T[s][2 * (q) + 1][1], T[s][2 * (q) + 4][0], T[s][2 * (q) + 4][1], T[s][2 * (q) + 5][0], T[s][2 * (q) + 5][1]}
#define SS8(s, q) f32x8{S1[s][4 * (q)], S1[s][4 * (q) + 1], S1[s][4 * (q) + 2], S1[s][4 * (q) + 3], S1[s][4 * (q) + 8], S1[s][4 * (q) + 9], S1[s][4 * (q) + 10], S1[s][4 * (q) + 11]}
#define DOT1(a_, b_) asm volatile("v_smfmac_f32_16x16x64_bf16 %0, %1, %2, %3 abid:1" : "+v"(c.dg) : "v"(a_), "v"(b_), "v"(c.six));
#define DOT(q, s) { if ((s) == 6) { DOT1(c.X[0], (SS8(6, q))) } else if ((s) < 2 * NJP) { DOT1(c.Y[(4 * ((s) >> 1) + ((s) & 1) + 2 * (q)) % 12], (TT8((s) % 6, q))) } \
            else { DOT1(c.X[1 + 4 * (((s) % 6) >> 1) + ((s) & 1) + 2 * (q)], (SS8(s, q))) } }
#define DPOS 0x0600000504070301ull
#define DPOS2 0x20
#define DP(n, j) if (n >= 4 && n < 6) { constexpr int s_ = (((DPOS | DPOS2) >> (4 * ((8 * n + j) & 15))) & 15); if (s_) DOT(0, (s_ + 6) % 7) }
#define XQ(n, j) (((n) & 3) * 4 + ((j) > 5 ? 4 : (j) > 3 ? 2 : (j) - 2))
#define PJ(n, j) { constexpr int i = SQ(n), i1 = SQ((n + 1) & 7), np = (n + 7) & 7, z = 8 * n + j, h = j >> 2; \
                constexpr bool nx = n < 7, pv = n >= 1, al = nx && n != 1; \
                SB \
                if (z == FLW + 4 * NE - 16) { wait_set<2>(flg, c.wf[1] + (OFF_FL - OFF_B - SLOT), c.slow); SB } \
                if (z == FLW - 16) { wait_set<1>(flg, c.wf[1] + (OFF_FL + FL1 - OFF_B - SLOT), c.slow); SB } \
                if (!(j & 1)) { if (z + WPRE <= 64) WAITCNT(63, WPRE - 2); SB } \
                if (j & 2) u[h][(j & 3) ^ h ^ 1] = mf0(wq[z], bq[(2 * i + h) & 3]); else u[h][(j & 3) ^ h ^ 1] = mf0(wq[z], bp[(2 * i + h) & 3]); \
                SB \
                if (z == FLR + 4 * NE - 16) flg = LDSV(c.wf[1] + (OFF_FL - OFF_B - SLOT)); \
                if (z == FLR - 16) flg = LDSV(c.wf[1] + (OFF_FL + FL1 - OFF_B - SLOT)); \
                if (z + WPRE < 64) wq[(z + WPRE) & 63] = WSEQ(z + WPRE); \
                SB \
                if (j == 0) { CB(bq, i, 0, 0) DP(n, 0) CB(bq, i, 0, 1) } \
                if (j == 1) { CB(bq, i, 0, 2) DP(n, 1) CB(bq, i, 1, 0) CB(bq, i, 1, 1) } \
                if (n >= 4 && (j == 2 || j == 3 || j == 5)) xpair<2, XQ(n, j), XQ(n, j + 1)>(c); \
                if (j == 2) { if (pv) F1(np, 1, 4) CB(bq, i, 1, 2) DP(n, 2) if (pv) { F1(np, 1, 0) F1(np, 1, 1) F1(np, 1, 2) F1(np, 1, 3) } } \
                if (j == 3) { if (pv) F1(np, 1, 5) DP(n, 3) if (pv) F1(np, 1, 6) } \
                if (j == 4) { if (al) CB(bp, i1, 0, 0) DP(n, 4) if (al) CB(bp, i1, 0, 1) } \
                if (j == 5) { F1(n, 0, 4) DP(n, 5) F1(n, 0, 5) F1(n, 0, 6) } \
                if (j == 6) { if (nx) CB(bp, i1, 0, 2) DP(n, 6) if (al) CB(bp, i1, 1, 0) F1(n, 0, 0) F1(n, 0, 1) } \
                if (LI && n == 7) { if (j == 4 || j > 5) { FS1(0, j == 4 ? 2 : j & 1, 0) FS1(0, j == 4 ? 2 : j & 1, 1) } } \
                else if (j == 7) { \
                    F1(n, 0, 2) F1(n, 0, 3) \
                    if (al) CB(bp, i1, 1, 1) \
                    if (nx) CB(bp, i1, 1, 2) \
                    FM(n, 0, 0) FM(n, 0, 1) FM(n, 0, 2) \
                    if (nx) { SC(i1, 0) SC(i1, 1) SC(i1, 2) } else asm volatile("s_nop 3"); \
                    FM(n, 1, 0) FM(n, 1, 1) FM(n, 1, 2) \
                    if (n == 3) { f32x2 a_, b_; asm volatile("v_mov_b64 %0, 0\n\tv_mov_b64 %1, 0" : "=v"(a_), "=v"(b_)); c.dg = f32x4{a_[0], a_[1], b_[0], b_[1]}; } \
                } }
        static_assert(FLW + WPRE <= 32 && FLZ <= FLR && FLR < FLW, "");
#define CP 9
#define PN(n) TI(n) if (n == CP) { __builtin_amdgcn_s_setprio(0); SB } PJ(n, 0) PJ(n, 1) PJ(n, 2) PJ(n, 3) PJ(n, 4) PJ(n, 5) PJ(n, 6) PJ(n, 7)
        PN(0)
        PN(1)
        TG(c.tq[2])
        PN(2)
        PN(3)
        TG(c.tq[3])
        PN(4) PN(5) PN(6) PN(7) TI(8)
        unsigned s8;
        double big;
#define SU0 { SB s8 = __builtin_amdgcn_mbcnt_lo((unsigned)act, 0u); asm volatile("" :: "v"(s8)); SB }
#define SU1 { SB s8 = __builtin_amdgcn_mbcnt_hi((unsigned)(act >> 32), s8); asm volatile("" :: "v"(s8)); SB }
#define SU2 { SB s8 <<= 3; asm volatile("" :: "v"(s8)); SB }
#define SU3 { SB big = bigs; PIN(big); SB }
#undef DOT1
#define DOT1(a_, b_) asm volatile("v_smfmac_f32_16x16x64_bf16 %0, %1, %2, %3 abid:1" :: "v"(c.dg), "v"(a_), "v"(b_), "v"(c.six));
        if (LI) {
            DOT(1, 4) SU0 FS1(1, 0, 0) DOT(1, 0) FS1(1, 0, 1) FS1(1, 1, 0) DOT(1, 2) FS1(1, 1, 1) FS1(1, 2, 0) DOT(1, 1) FS1(1, 2, 1) F1(7, 1, 6)
            DOT(1, 3) SU1 DOT(1, 5) SU2 DOT(1, 6) SU3
        } else {
            SU0 SU1
            DOT(1, 0) SU2 DOT(1, 2) F1(7, 1, 6) DOT(1, 1) SU3 DOT(1, 3) DOT(1, 4) DOT(1, 5) DOT(1, 6)
        }
        asm volatile("s_mov_b64 exec, %1" : "+v"(c.dg) : "s"(act));
        snop(LI ? 5 : 6);
        SB
        const bool b0 = __builtin_amdgcn_inverse_ballot_w64(m1), b1 = __builtin_amdgcn_inverse_ballot_w64(m2);
        const float va = b0 ? c.dg[1] : c.dg[0], vb = b0 ? c.dg[3] : c.dg[2], v = b1 ? vb : va;
        TG(c.tq[4])
        STAMP(4)
        TS(c.ts[14])
        deliver(c, v, big, 2 * nq + 1, s8, fk, sums, outp, [] { });
        ALL
        const int ln = __builtin_amdgcn_mbcnt_hi(~0u, __builtin_amdgcn_mbcnt_lo(~0u, 0u));
        const int lane = ln, tid = w * 64 + ln;
        (void)lane; (void)tid;
        FINISH(c)
    }
}

extern "C" void kernel_launch(void* const* d_in, const int* in_sizes, int n_in,
                              void* d_out, int out_size, void* d_ws, size_t ws_size,
                              hipStream_t stream) {
    const int* idx = (const int*)d_in[0];
    const uint16_t* table = (const uint16_t*)d_in[1];
    const uint16_t* se_w1 = (const uint16_t*)d_in[2];
    const uint16_t* se_w2 = (const uint16_t*)d_in[3];
    const uint16_t* w_bi = (const uint16_t*)d_in[4];
    const uint16_t* w_cross = (const uint16_t*)d_in[5];
    const uint16_t* w_deep1 = (const uint16_t*)d_in[6];
    const uint16_t* w_deep2 = (const uint16_t*)d_in[7];
    const char* w1a = (const char*)((unsigned long long)w_deep1 & 0xffffffffffffull);
    const unsigned long long w1b = 0x0002000000000000ull | (NF * 32768);
    (void)hipFuncSetAttribute((const void*)k_main, hipFuncAttributeMaxDynamicSharedMemorySize, LDS_T);
    k_main<<<256, 512, LDS_T, stream>>>(idx, table, w1a, w1b, w_cross, (unsigned*)d_ws, se_w1, w_bi, se_w2, w_deep2, (__hip_bfloat16*)d_out);
}
```
